```python
import torch

def _softmax(x, axis=-1):
    return torch.softmax(x, dim=axis)

def _gelu(x):
    return torch.nn.functional.gelu(x, approximate='tanh')

def _silu(x):
    return torch.nn.functional.silu(x)

def _sigmoid(x):
    return torch.sigmoid(x)

def _layernorm(x, axis=-1, eps=1e-05):
    m = x.mean(dim=axis, keepdim=True)
    v = x.var(dim=axis, keepdim=True, unbiased=False)
    return (x - m) / torch.sqrt(v + eps)

def _scatter_add(tgt, idx, src, axis=0):
    return tgt.clone().index_add_(axis, idx, src)

def _segment_sum(src, seg, n):
    out = torch.zeros((n,) + src.shape[1:], dtype=src.dtype)
    return out.index_add_(0, seg, src)

def _pad(x, pw):
    flat = tuple((v for pair in reversed(pw) for v in pair))
    return torch.nn.functional.pad(x, flat)
kernel_name = 'adaLN_cond__gqa_block_cached__mixer_channel_mlp__prompt'

def setup_inputs():

    _w = lambda a: (lambda t: t.to(torch.bfloat16) if t.is_floating_point() else t)(torch.as_tensor(a))

    def _A_setup():
        B, Sq, D = (1, 2048, 448)
        Hq, Hkv, Sk, Dh = (16, 8, 2048, 256)
        assert Hq % Hkv == 0, (Hq, Hkv)
        return {'x': _w((lambda *s: torch.randn(*s, dtype=torch.bfloat16) * 0.1)(B, Sq, D)), 'k_cache': _w((lambda *s: torch.randn(*s, dtype=torch.bfloat16) * 0.8)(B, Hkv, Sk, Dh)), 'v_cache': _w((lambda *s: torch.randn(*s, dtype=torch.bfloat16) * 1.6)(B, Hkv, Sk, Dh)), 'wq': _w((lambda *s: torch.randn(*s, dtype=torch.bfloat16) * 0.8)(D, Hq * Dh) * D ** (-0.5)), 'wo': _w((lambda *s: torch.randn(*s, dtype=torch.bfloat16) * 1.6)(Hq * Dh, D) * (Hq * Dh) ** (-0.5))}
    _A_in = _A_setup()

    def _B_setup():
        B, S, D, F = (1, 2048, 448, 1792)
        return {'x': _w((lambda *s: torch.randn(*s, dtype=torch.bfloat16) * 0.1)(B, S, D)), 'w1': _w((lambda *s: torch.randn(*s, dtype=torch.bfloat16) * 3.2)(D, F) * D ** (-0.5)), 'b1': _w((lambda *s: torch.randn(*s, dtype=torch.bfloat16) * 3.2)(F)), 'w2': _w((lambda *s: torch.randn(*s, dtype=torch.bfloat16) * 3.2)(F, D) * F ** (-0.5)), 'b2': _w((lambda *s: torch.randn(*s, dtype=torch.bfloat16) * 3.2)(D))}
    _B_in = _B_setup()
    return {'A_x': _A_in['x'], 'A_k_cache': _A_in['k_cache'], 'A_v_cache': _A_in['v_cache'], 'A_wq': _A_in['wq'], 'A_wo': _A_in['wo'], 'B_x': _B_in['x'], 'B_w1': _B_in['w1'], 'B_b1': _B_in['b1'], 'B_w2': _B_in['w2'], 'B_b2': _B_in['b2'], 'c': _w((lambda *s: torch.randn(*s, dtype=torch.bfloat16) * 0.2)(1, 448) * 8.0), 'w_mod': _w((lambda *s: torch.randn(*s, dtype=torch.bfloat16) * 0.2)(448, 6 * 448) * 448 ** (-0.5) * 8.0)}

def reference(A_x, A_k_cache, A_v_cache, A_wq, A_wo, B_x, B_w1, B_b1, B_w2, B_b2, c, w_mod):

    def _A_ref(x, k_cache, v_cache, wq, wo):
        B, Sq, D = x.shape
        Hq, Dh = (16, 256)
        Hkv = k_cache.shape[1]
        rep = Hq // Hkv
        q = (lambda x, *d: x.permute(*d))((x @ wq).reshape(B, Sq, Hq, Dh), 0, 2, 1, 3)
        k = (lambda x, n, axis: torch.repeat_interleave(x, n, dim=axis))(k_cache, rep, axis=1)
        v = (lambda x, n, axis: torch.repeat_interleave(x, n, dim=axis))(v_cache, rep, axis=1)
        scores = torch.einsum('bhqd,bhkd->bhqk', q, k) * Dh ** (-0.5)
        p = _softmax(scores, axis=-1)
        o = torch.einsum('bhqk,bhkd->bhqd', p, v)
        return (lambda x, *d: x.permute(*d))(o, 0, 2, 1, 3).reshape(B, Sq, Hq * Dh) @ wo

    def _B_ref(x, w1, b1, w2, b2):
        y = _layernorm(x, axis=-1)
        h = _gelu(torch.einsum('bsd,df->bsf', y, w1) + b1)
        return torch.einsum('bsf,fd->bsd', h, w2) + b2
    x = A_x
    mod = (_silu(c) @ w_mod).reshape(c.shape[0], 6, 1, -1)
    sa, ca, ga, sb, cb, gb = (mod[:, 0].clone(), mod[:, 1].clone(), mod[:, 2].clone(), mod[:, 3].clone(), mod[:, 4].clone(), mod[:, 5].clone())
    h = _layernorm(x, axis=-1) * (1 + ca) + sa
    x = x + ga * _A_ref(h, A_k_cache, A_v_cache, A_wq, A_wo)
    h = _layernorm(x, axis=-1) * (1 + cb) + sb
    x = x + gb * _B_ref(h, B_w1, B_b1, B_w2, B_b2)
    return x
```

```cpp
#include <hip/hip_runtime.h>
#include <hip/hip_bf16.h>
typedef unsigned short u16;
typedef float f32x4 __attribute__((ext_vector_type(4)));
typedef float f32x16 __attribute__((ext_vector_type(16)));
typedef float f32x2 __attribute__((ext_vector_type(2)));
typedef unsigned u32x2 __attribute__((ext_vector_type(2)));
typedef unsigned u32x4 __attribute__((ext_vector_type(4)));
typedef unsigned u32x16 __attribute__((ext_vector_type(16)));
typedef unsigned u32x8 __attribute__((ext_vector_type(8)));
typedef _Float16 h16x2 __attribute__((ext_vector_type(2)));
typedef __bf16 b16x2 __attribute__((ext_vector_type(2)));
#define K0_ASM \
"buffer_load_dword v99, off, s[76:79], 0 sc1\n" \
"s_cmp_ge_u32 s31, 84\n" \
"s_cbranch_scc1 L_k0x_%=\n" \
"buffer_load_ushort v92, v102, s[36:39], 0 offen offset:0\n" \
"buffer_load_ushort v93, v102, s[36:39], 0 offen offset:64\n" \
"buffer_load_ushort v94, v102, s[36:39], 0 offen offset:128\n" \
"buffer_load_ushort v95, v102, s[36:39], 0 offen offset:192\n" \
"buffer_load_ushort v96, v102, s[36:39], 0 offen offset:256\n" \
"buffer_load_ushort v97, v102, s[36:39], 0 offen offset:320\n" \
"buffer_load_ushort v98, v102, s[36:39], 0 offen offset:384\n" \
"s_mov_b32 s68, 0\n" \
"buffer_load_dwordx4 v[64:67], v101, s[32:35], s68 offen\n" \
"s_mov_b32 s68, 172032\n" \
"buffer_load_dwordx4 v[68:71], v101, s[32:35], s68 offen\n" \
"s_mov_b32 s68, 344064\n" \
"buffer_load_dwordx4 v[72:75], v101, s[32:35], s68 offen\n" \
"s_mov_b32 s68, 516096\n" \
"buffer_load_dwordx4 v[76:79], v101, s[32:35], s68 offen\n" \
"s_mov_b32 s68, 688128\n" \
"buffer_load_dwordx4 v[80:83], v101, s[32:35], s68 offen\n" \
"s_mov_b32 s68, 860160\n" \
"buffer_load_dwordx4 v[84:87], v101, s[32:35], s68 offen\n" \
"s_mov_b32 s68, 1032192\n" \
"buffer_load_dwordx4 v[88:91], v101, s[32:35], s68 offen\n" \
"s_branch L_k0e_%=\n" \
"L_k0x_%=:\n" \
"s_movk_i32 s70, 3584\n" \
"s_movk_i32 s68, 7168\n" \
"s_movk_i32 s69, 10752\n" \
"buffer_load_dwordx4 v[0:3], v103, s[72:75], 0 offen offset:0\n" \
"buffer_load_dwordx4 v[4:7], v103, s[72:75], 0 offen offset:896\n" \
"buffer_load_dwordx4 v[8:11], v103, s[72:75], 0 offen offset:1792\n" \
"buffer_load_dwordx4 v[12:15], v103, s[72:75], 0 offen offset:2688\n" \
"buffer_load_dwordx4 v[16:19], v103, s[72:75], s70 offen offset:0\n" \
"buffer_load_dwordx4 v[20:23], v103, s[72:75], s70 offen offset:896\n" \
"buffer_load_dwordx4 v[24:27], v103, s[72:75], s70 offen offset:1792\n" \
"buffer_load_dwordx4 v[28:31], v103, s[72:75], s70 offen offset:2688\n" \
"buffer_load_dwordx4 v[32:35], v103, s[72:75], s68 offen offset:0\n" \
"buffer_load_dwordx4 v[36:39], v103, s[72:75], s68 offen offset:896\n" \
"buffer_load_dwordx4 v[40:43], v103, s[72:75], s68 offen offset:1792\n" \
"buffer_load_dwordx4 v[44:47], v103, s[72:75], s68 offen offset:2688\n" \
"buffer_load_dwordx4 v[48:51], v103, s[72:75], s69 offen offset:0\n" \
"buffer_load_dwordx4 v[52:55], v103, s[72:75], s69 offen offset:896\n" \
"buffer_load_dwordx4 v[56:59], v103, s[72:75], s69 offen offset:1792\n" \
"buffer_load_dwordx4 v[60:63], v103, s[72:75], s69 offen offset:2688\n" \
"buffer_load_dword v100, v104, s[56:59], 0 offen\n" \
"buffer_load_dword v100, v105, s[56:59], 0 offen\n" \
"L_k0e_%=:\n"
#define K2_ASM \
"s_cmp_ge_u32 s31, 84\n" \
"s_cbranch_scc1 L_h0_%=\n" \
"s_movk_i32 s70, 3584\n" \
"s_movk_i32 s68, 7168\n" \
"s_movk_i32 s69, 10752\n" \
"buffer_load_dwordx4 v[0:3], v103, s[72:75], 0 offen offset:0\n" \
"buffer_load_dwordx4 v[4:7], v103, s[72:75], 0 offen offset:896\n" \
"buffer_load_dwordx4 v[8:11], v103, s[72:75], 0 offen offset:1792\n" \
"buffer_load_dwordx4 v[12:15], v103, s[72:75], 0 offen offset:2688\n" \
"buffer_load_dwordx4 v[16:19], v103, s[72:75], s70 offen offset:0\n" \
"buffer_load_dwordx4 v[20:23], v103, s[72:75], s70 offen offset:896\n" \
"buffer_load_dwordx4 v[24:27], v103, s[72:75], s70 offen offset:1792\n" \
"buffer_load_dwordx4 v[28:31], v103, s[72:75], s70 offen offset:2688\n" \
"buffer_load_dwordx4 v[32:35], v103, s[72:75], s68 offen offset:0\n" \
"buffer_load_dwordx4 v[36:39], v103, s[72:75], s68 offen offset:896\n" \
"buffer_load_dwordx4 v[40:43], v103, s[72:75], s68 offen offset:1792\n" \
"buffer_load_dwordx4 v[44:47], v103, s[72:75], s68 offen offset:2688\n" \
"buffer_load_dwordx4 v[48:51], v103, s[72:75], s69 offen offset:0\n" \
"buffer_load_dwordx4 v[52:55], v103, s[72:75], s69 offen offset:896\n" \
"buffer_load_dwordx4 v[56:59], v103, s[72:75], s69 offen offset:1792\n" \
"buffer_load_dwordx4 v[60:63], v103, s[72:75], s69 offen offset:2688\n" \
"s_waitcnt vmcnt(16)\n" \
"L_h0_%=:\n" \
"s_waitcnt vmcnt(18)\n" \
"v_readfirstlane_b32 s30, v99\n" \
"s_cmp_ge_u32 s31, 84\n" \
"s_mul_i32 s30, s30, 0x9E3779B1\n" \
"s_cbranch_scc1 L_nogemv_%=\n" \
"v_lshlrev_b32 v92, 16, v92\n" \
"v_lshlrev_b32 v93, 16, v93\n" \
"v_lshlrev_b32 v94, 16, v94\n" \
"v_lshlrev_b32 v95, 16, v95\n" \
"v_lshlrev_b32 v96, 16, v96\n" \
"v_lshlrev_b32 v97, 16, v97\n" \
"v_lshlrev_b32 v98, 16, v98\n" \
"v_mul_f32 v110, 0xbfb8aa3b, v92\n" \
"v_mul_f32 v111, 0xbfb8aa3b, v93\n" \
"v_mul_f32 v112, 0xbfb8aa3b, v94\n" \
"v_mul_f32 v113, 0xbfb8aa3b, v95\n" \
"v_mul_f32 v114, 0xbfb8aa3b, v96\n" \
"v_mul_f32 v115, 0xbfb8aa3b, v97\n" \
"v_mul_f32 v116, 0xbfb8aa3b, v98\n" \
"v_exp_f32 v110, v110\n" \
"v_exp_f32 v111, v111\n" \
"v_exp_f32 v112, v112\n" \
"v_exp_f32 v113, v113\n" \
"v_exp_f32 v114, v114\n" \
"v_exp_f32 v115, v115\n" \
"v_exp_f32 v116, v116\n" \
"v_add_f32 v110, 1.0, v110\n" \
"v_add_f32 v111, 1.0, v111\n" \
"v_add_f32 v112, 1.0, v112\n" \
"v_add_f32 v113, 1.0, v113\n" \
"v_add_f32 v114, 1.0, v114\n" \
"v_add_f32 v115, 1.0, v115\n" \
"v_add_f32 v116, 1.0, v116\n" \
"v_rcp_f32 v110, v110\n" \
"v_rcp_f32 v111, v111\n" \
"v_rcp_f32 v112, v112\n" \
"v_rcp_f32 v113, v113\n" \
"v_rcp_f32 v114, v114\n" \
"v_rcp_f32 v115, v115\n" \
"v_rcp_f32 v116, v116\n" \
"v_mul_f32 v92, v92, v110\n" \
"v_mul_f32 v93, v93, v111\n" \
"v_mul_f32 v94, v94, v112\n" \
"v_mul_f32 v95, v95, v113\n" \
"v_mul_f32 v96, v96, v114\n" \
"v_mul_f32 v97, v97, v115\n" \
"v_mul_f32 v98, v98, v116\n" \
"v_lshlrev_b32 v120, 16, v64\n" \
"v_and_b32 v121, 0xffff0000, v64\n" \
"v_lshlrev_b32 v122, 16, v65\n" \
"v_and_b32 v123, 0xffff0000, v65\n" \
"v_lshlrev_b32 v124, 16, v66\n" \
"v_and_b32 v125, 0xffff0000, v66\n" \
"v_lshlrev_b32 v126, 16, v67\n" \
"v_and_b32 v127, 0xffff0000, v67\n" \
"v_mul_f32 v128, v120, v92\n" \
"v_mul_f32 v129, v121, v92\n" \
"v_mul_f32 v130, v122, v92\n" \
"v_mul_f32 v131, v123, v92\n" \
"v_mul_f32 v132, v124, v92\n" \
"v_mul_f32 v133, v125, v92\n" \
"v_mul_f32 v134, v126, v92\n" \
"v_mul_f32 v135, v127, v92\n" \
"v_lshlrev_b32 v120, 16, v68\n" \
"v_and_b32 v121, 0xffff0000, v68\n" \
"v_lshlrev_b32 v122, 16, v69\n" \
"v_and_b32 v123, 0xffff0000, v69\n" \
"v_lshlrev_b32 v124, 16, v70\n" \
"v_and_b32 v125, 0xffff0000, v70\n" \
"v_lshlrev_b32 v126, 16, v71\n" \
"v_and_b32 v127, 0xffff0000, v71\n" \
"v_fmac_f32 v128, v120, v93\n" \
"v_fmac_f32 v129, v121, v93\n" \
"v_fmac_f32 v130, v122, v93\n" \
"v_fmac_f32 v131, v123, v93\n" \
"v_fmac_f32 v132, v124, v93\n" \
"v_fmac_f32 v133, v125, v93\n" \
"v_fmac_f32 v134, v126, v93\n" \
"v_fmac_f32 v135, v127, v93\n" \
"v_lshlrev_b32 v120, 16, v72\n" \
"v_and_b32 v121, 0xffff0000, v72\n" \
"v_lshlrev_b32 v122, 16, v73\n" \
"v_and_b32 v123, 0xffff0000, v73\n" \
"v_lshlrev_b32 v124, 16, v74\n" \
"v_and_b32 v125, 0xffff0000, v74\n" \
"v_lshlrev_b32 v126, 16, v75\n" \
"v_and_b32 v127, 0xffff0000, v75\n" \
"v_fmac_f32 v128, v120, v94\n" \
"v_fmac_f32 v129, v121, v94\n" \
"v_fmac_f32 v130, v122, v94\n" \
"v_fmac_f32 v131, v123, v94\n" \
"v_fmac_f32 v132, v124, v94\n" \
"v_fmac_f32 v133, v125, v94\n" \
"v_fmac_f32 v134, v126, v94\n" \
"v_fmac_f32 v135, v127, v94\n" \
"v_lshlrev_b32 v120, 16, v76\n" \
"v_and_b32 v121, 0xffff0000, v76\n" \
"v_lshlrev_b32 v122, 16, v77\n" \
"v_and_b32 v123, 0xffff0000, v77\n" \
"v_lshlrev_b32 v124, 16, v78\n" \
"v_and_b32 v125, 0xffff0000, v78\n" \
"v_lshlrev_b32 v126, 16, v79\n" \
"v_and_b32 v127, 0xffff0000, v79\n" \
"v_fmac_f32 v128, v120, v95\n" \
"v_fmac_f32 v129, v121, v95\n" \
"v_fmac_f32 v130, v122, v95\n" \
"v_fmac_f32 v131, v123, v95\n" \
"v_fmac_f32 v132, v124, v95\n" \
"v_fmac_f32 v133, v125, v95\n" \
"v_fmac_f32 v134, v126, v95\n" \
"v_fmac_f32 v135, v127, v95\n" \
"v_lshlrev_b32 v120, 16, v80\n" \
"v_and_b32 v121, 0xffff0000, v80\n" \
"v_lshlrev_b32 v122, 16, v81\n" \
"v_and_b32 v123, 0xffff0000, v81\n" \
"v_lshlrev_b32 v124, 16, v82\n" \
"v_and_b32 v125, 0xffff0000, v82\n" \
"v_lshlrev_b32 v126, 16, v83\n" \
"v_and_b32 v127, 0xffff0000, v83\n" \
"v_fmac_f32 v128, v120, v96\n" \
"v_fmac_f32 v129, v121, v96\n" \
"v_fmac_f32 v130, v122, v96\n" \
"v_fmac_f32 v131, v123, v96\n" \
"v_fmac_f32 v132, v124, v96\n" \
"v_fmac_f32 v133, v125, v96\n" \
"v_fmac_f32 v134, v126, v96\n" \
"v_fmac_f32 v135, v127, v96\n" \
"v_lshlrev_b32 v120, 16, v84\n" \
"v_and_b32 v121, 0xffff0000, v84\n" \
"v_lshlrev_b32 v122, 16, v85\n" \
"v_and_b32 v123, 0xffff0000, v85\n" \
"v_lshlrev_b32 v124, 16, v86\n" \
"v_and_b32 v125, 0xffff0000, v86\n" \
"v_lshlrev_b32 v126, 16, v87\n" \
"v_and_b32 v127, 0xffff0000, v87\n" \
"v_fmac_f32 v128, v120, v97\n" \
"v_fmac_f32 v129, v121, v97\n" \
"v_fmac_f32 v130, v122, v97\n" \
"v_fmac_f32 v131, v123, v97\n" \
"v_fmac_f32 v132, v124, v97\n" \
"v_fmac_f32 v133, v125, v97\n" \
"v_fmac_f32 v134, v126, v97\n" \
"v_fmac_f32 v135, v127, v97\n" \
"v_lshlrev_b32 v120, 16, v88\n" \
"v_and_b32 v121, 0xffff0000, v88\n" \
"v_lshlrev_b32 v122, 16, v89\n" \
"v_and_b32 v123, 0xffff0000, v89\n" \
"v_lshlrev_b32 v124, 16, v90\n" \
"v_and_b32 v125, 0xffff0000, v90\n" \
"v_lshlrev_b32 v126, 16, v91\n" \
"v_and_b32 v127, 0xffff0000, v91\n" \
"v_fmac_f32 v128, v120, v98\n" \
"v_fmac_f32 v129, v121, v98\n" \
"v_fmac_f32 v130, v122, v98\n" \
"v_fmac_f32 v131, v123, v98\n" \
"v_fmac_f32 v132, v124, v98\n" \
"v_fmac_f32 v133, v125, v98\n" \
"v_fmac_f32 v134, v126, v98\n" \
"v_fmac_f32 v135, v127, v98\n" \
"v_add_f32_dpp v128, v128, v128 row_ror:8 row_mask:0xf bank_mask:0xf\n" \
"v_add_f32_dpp v129, v129, v129 row_ror:8 row_mask:0xf bank_mask:0xf\n" \
"v_add_f32_dpp v130, v130, v130 row_ror:8 row_mask:0xf bank_mask:0xf\n" \
"v_add_f32_dpp v131, v131, v131 row_ror:8 row_mask:0xf bank_mask:0xf\n" \
"v_add_f32_dpp v132, v132, v132 row_ror:8 row_mask:0xf bank_mask:0xf\n" \
"v_add_f32_dpp v133, v133, v133 row_ror:8 row_mask:0xf bank_mask:0xf\n" \
"v_add_f32_dpp v134, v134, v134 row_ror:8 row_mask:0xf bank_mask:0xf\n" \
"v_add_f32_dpp v135, v135, v135 row_ror:8 row_mask:0xf bank_mask:0xf\n" \
"s_mov_b32 exec_lo, 0x00ff00ff\n" \
"s_mov_b32 exec_hi, 0x00ff00ff\n" \
"ds_write_b128 v107, v[128:131]\n" \
"ds_write_b128 v107, v[132:135] offset:16\n" \
"s_mov_b64 exec, -1\n" \
"s_waitcnt lgkmcnt(0)\n" \
"s_barrier\n" \
"ds_read2st64_b32 v[110:111], v108 offset0:0 offset1:1\n" \
"ds_read2st64_b32 v[112:113], v108 offset0:2 offset1:3\n" \
"ds_read2st64_b32 v[114:115], v108 offset0:4 offset1:5\n" \
"ds_read2st64_b32 v[116:117], v108 offset0:6 offset1:7\n" \
"ds_read2st64_b32 v[118:119], v108 offset0:8 offset1:9\n" \
"ds_read2st64_b32 v[120:121], v108 offset0:10 offset1:11\n" \
"ds_read2st64_b32 v[122:123], v108 offset0:12 offset1:13\n" \
"ds_read2st64_b32 v[124:125], v108 offset0:14 offset1:15\n" \
"s_add_u32 s25, s30, 0x7F4A7C15\n" \
"v_mov_b32 v129, s25\n" \
"v_mov_b32 v130, s29\n" \
"s_waitcnt lgkmcnt(0)\n" \
"v_pk_add_f32 v[110:111], v[110:111], v[112:113]\n" \
"v_pk_add_f32 v[114:115], v[114:115], v[116:117]\n" \
"v_pk_add_f32 v[118:119], v[118:119], v[120:121]\n" \
"v_pk_add_f32 v[122:123], v[122:123], v[124:125]\n" \
"v_pk_add_f32 v[110:111], v[110:111], v[114:115]\n" \
"v_pk_add_f32 v[118:119], v[118:119], v[122:123]\n" \
"v_pk_add_f32 v[110:111], v[110:111], v[118:119]\n" \
"v_add_f32 v110, v110, v111\n" \
"v_fma_f32 v128, v110, s28, v130\n" \
"buffer_store_dwordx2 v[128:129], v106, s[76:79], 0 offen sc1\n" \
"buffer_load_dword v100, off, s[76:79], 0\n" \
"buffer_load_dword v100, off, s[76:79], 0\n" \
"L_nogemv_%=:\n" \
"s_add_u32 s30, s30, 0x7F4A7C15\n" \
"s_mov_b32 s25, 0x3f803f80\n" \
"s_waitcnt vmcnt(10)\n" \
"v_dot2_f32_bf16 v176, v0, s25, 0\n" \
"v_dot2_f32_bf16 v177, v4, s25, 0\n" \
"v_dot2_f32_bf16 v178, v8, s25, 0\n" \
"v_dot2_f32_bf16 v179, v12, s25, 0\n" \
"v_dot2_f32_bf16 v180, v16, s25, 0\n" \
"v_dot2_f32_bf16 v181, v20, s25, 0\n" \
"v_dot2_f32_bf16 v182, v24, s25, 0\n" \
"v_dot2_f32_bf16 v183, v28, s25, 0\n" \
"v_dot2_f32_bf16 v176, v1, s25, v176\n" \
"v_dot2_f32_bf16 v177, v5, s25, v177\n" \
"v_dot2_f32_bf16 v178, v9, s25, v178\n" \
"v_dot2_f32_bf16 v179, v13, s25, v179\n" \
"v_dot2_f32_bf16 v180, v17, s25, v180\n" \
"v_dot2_f32_bf16 v181, v21, s25, v181\n" \
"v_dot2_f32_bf16 v182, v25, s25, v182\n" \
"v_dot2_f32_bf16 v183, v29, s25, v183\n" \
"v_dot2_f32_bf16 v176, v2, s25, v176\n" \
"v_dot2_f32_bf16 v177, v6, s25, v177\n" \
"v_dot2_f32_bf16 v178, v10, s25, v178\n" \
"v_dot2_f32_bf16 v179, v14, s25, v179\n" \
"v_dot2_f32_bf16 v180, v18, s25, v180\n" \
"v_dot2_f32_bf16 v181, v22, s25, v181\n" \
"v_dot2_f32_bf16 v182, v26, s25, v182\n" \
"v_dot2_f32_bf16 v183, v30, s25, v183\n" \
"v_dot2_f32_bf16 v176, v3, s25, v176\n" \
"v_dot2_f32_bf16 v177, v7, s25, v177\n" \
"v_dot2_f32_bf16 v178, v11, s25, v178\n" \
"v_dot2_f32_bf16 v179, v15, s25, v179\n" \
"v_dot2_f32_bf16 v180, v19, s25, v180\n" \
"v_dot2_f32_bf16 v181, v23, s25, v181\n" \
"v_dot2_f32_bf16 v182, v27, s25, v182\n" \
"v_dot2_f32_bf16 v183, v31, s25, v183\n" \
"v_lshlrev_b32 v64, 16, v0\n" \
"v_and_b32 v65, 0xffff0000, v0\n" \
"v_lshlrev_b32 v66, 16, v1\n" \
"v_and_b32 v67, 0xffff0000, v1\n" \
"v_lshlrev_b32 v68, 16, v2\n" \
"v_and_b32 v69, 0xffff0000, v2\n" \
"v_lshlrev_b32 v70, 16, v3\n" \
"v_and_b32 v71, 0xffff0000, v3\n" \
"v_lshlrev_b32 v72, 16, v4\n" \
"v_and_b32 v73, 0xffff0000, v4\n" \
"v_lshlrev_b32 v74, 16, v5\n" \
"v_and_b32 v75, 0xffff0000, v5\n" \
"v_lshlrev_b32 v76, 16, v6\n" \
"v_and_b32 v77, 0xffff0000, v6\n" \
"v_lshlrev_b32 v78, 16, v7\n" \
"v_and_b32 v79, 0xffff0000, v7\n" \
"v_lshlrev_b32 v80, 16, v8\n" \
"v_and_b32 v81, 0xffff0000, v8\n" \
"v_lshlrev_b32 v82, 16, v9\n" \
"v_and_b32 v83, 0xffff0000, v9\n" \
"v_lshlrev_b32 v84, 16, v10\n" \
"v_and_b32 v85, 0xffff0000, v10\n" \
"v_lshlrev_b32 v86, 16, v11\n" \
"v_and_b32 v87, 0xffff0000, v11\n" \
"v_lshlrev_b32 v88, 16, v12\n" \
"v_and_b32 v89, 0xffff0000, v12\n" \
"v_lshlrev_b32 v90, 16, v13\n" \
"v_and_b32 v91, 0xffff0000, v13\n" \
"v_lshlrev_b32 v92, 16, v14\n" \
"v_and_b32 v93, 0xffff0000, v14\n" \
"v_lshlrev_b32 v94, 16, v15\n" \
"v_and_b32 v95, 0xffff0000, v15\n" \
"s_waitcnt vmcnt(2)\n" \
"v_dot2_f32_bf16 v184, v32, s25, 0\n" \
"v_dot2_f32_bf16 v185, v36, s25, 0\n" \
"v_dot2_f32_bf16 v186, v40, s25, 0\n" \
"v_dot2_f32_bf16 v187, v44, s25, 0\n" \
"v_dot2_f32_bf16 v188, v48, s25, 0\n" \
"v_dot2_f32_bf16 v189, v52, s25, 0\n" \
"v_dot2_f32_bf16 v190, v56, s25, 0\n" \
"v_dot2_f32_bf16 v191, v60, s25, 0\n" \
"v_dot2_f32_bf16 v184, v33, s25, v184\n" \
"v_dot2_f32_bf16 v185, v37, s25, v185\n" \
"v_dot2_f32_bf16 v186, v41, s25, v186\n" \
"v_dot2_f32_bf16 v187, v45, s25, v187\n" \
"v_dot2_f32_bf16 v188, v49, s25, v188\n" \
"v_dot2_f32_bf16 v189, v53, s25, v189\n" \
"v_dot2_f32_bf16 v190, v57, s25, v190\n" \
"v_dot2_f32_bf16 v191, v61, s25, v191\n" \
"v_dot2_f32_bf16 v184, v34, s25, v184\n" \
"v_dot2_f32_bf16 v185, v38, s25, v185\n" \
"v_dot2_f32_bf16 v186, v42, s25, v186\n" \
"v_dot2_f32_bf16 v187, v46, s25, v187\n" \
"v_dot2_f32_bf16 v188, v50, s25, v188\n" \
"v_dot2_f32_bf16 v189, v54, s25, v189\n" \
"v_dot2_f32_bf16 v190, v58, s25, v190\n" \
"v_dot2_f32_bf16 v191, v62, s25, v191\n" \
"v_dot2_f32_bf16 v184, v35, s25, v184\n" \
"v_dot2_f32_bf16 v185, v39, s25, v185\n" \
"v_dot2_f32_bf16 v186, v43, s25, v186\n" \
"v_dot2_f32_bf16 v187, v47, s25, v187\n" \
"v_dot2_f32_bf16 v188, v51, s25, v188\n" \
"v_dot2_f32_bf16 v189, v55, s25, v189\n" \
"v_dot2_f32_bf16 v190, v59, s25, v190\n" \
"v_dot2_f32_bf16 v191, v63, s25, v191\n" \
"s_nop 3\n" \
"v_permlane32_swap_b32 v176, v184\n" \
"v_permlane32_swap_b32 v177, v185\n" \
"v_permlane32_swap_b32 v178, v186\n" \
"v_permlane32_swap_b32 v179, v187\n" \
"v_permlane32_swap_b32 v180, v188\n" \
"v_permlane32_swap_b32 v181, v189\n" \
"v_permlane32_swap_b32 v182, v190\n" \
"v_permlane32_swap_b32 v183, v191\n" \
"v_add_f32 v176, v176, v184\n" \
"v_add_f32 v177, v177, v185\n" \
"v_add_f32 v178, v178, v186\n" \
"v_add_f32 v179, v179, v187\n" \
"v_add_f32 v180, v180, v188\n" \
"v_add_f32 v181, v181, v189\n" \
"v_add_f32 v182, v182, v190\n" \
"v_add_f32 v183, v183, v191\n" \
"v_permlane16_swap_b32 v176, v180\n" \
"v_permlane16_swap_b32 v177, v181\n" \
"v_permlane16_swap_b32 v178, v182\n" \
"v_permlane16_swap_b32 v179, v183\n" \
"v_add_f32 v176, v176, v180\n" \
"v_add_f32 v177, v177, v181\n" \
"v_add_f32 v178, v178, v182\n" \
"v_add_f32 v179, v179, v183\n" \
"v_add_f32_dpp v176, v176, v176 row_ror:8 row_mask:0xf bank_mask:0xf\n" \
"v_add_f32_dpp v177, v177, v177 row_ror:8 row_mask:0xf bank_mask:0xf\n" \
"v_add_f32_dpp v178, v178, v178 row_ror:8 row_mask:0xf bank_mask:0xf\n" \
"v_add_f32_dpp v179, v179, v179 row_ror:8 row_mask:0xf bank_mask:0xf\n" \
"v_add_f32_dpp v176, v176, v176 row_ror:4 row_mask:0xf bank_mask:0xf\n" \
"v_add_f32_dpp v177, v177, v177 row_ror:4 row_mask:0xf bank_mask:0xf\n" \
"v_add_f32_dpp v178, v178, v178 row_ror:4 row_mask:0xf bank_mask:0xf\n" \
"v_add_f32_dpp v179, v179, v179 row_ror:4 row_mask:0xf bank_mask:0xf\n" \
"v_add_f32_dpp v176, v176, v176 row_ror:2 row_mask:0xf bank_mask:0xf\n" \
"v_add_f32_dpp v177, v177, v177 row_ror:2 row_mask:0xf bank_mask:0xf\n" \
"v_add_f32_dpp v178, v178, v178 row_ror:2 row_mask:0xf bank_mask:0xf\n" \
"v_add_f32_dpp v179, v179, v179 row_ror:2 row_mask:0xf bank_mask:0xf\n" \
"v_add_f32_dpp v176, v176, v176 row_ror:1 row_mask:0xf bank_mask:0xf\n" \
"v_add_f32_dpp v177, v177, v177 row_ror:1 row_mask:0xf bank_mask:0xf\n" \
"v_add_f32_dpp v178, v178, v178 row_ror:1 row_mask:0xf bank_mask:0xf\n" \
"v_add_f32_dpp v179, v179, v179 row_ror:1 row_mask:0xf bank_mask:0xf\n" \
"v_mul_f32 v176, 0xbb124925, v176\n" \
"v_mul_f32 v177, 0xbb124925, v177\n" \
"v_mul_f32 v178, 0xbb124925, v178\n" \
"v_mul_f32 v179, 0xbb124925, v179\n" \
"s_nop 1\n" \
"v_readlane_b32 s80, v176, 0\n" \
"v_readlane_b32 s81, v177, 0\n" \
"v_readlane_b32 s82, v178, 0\n" \
"v_readlane_b32 s83, v179, 0\n" \
"v_readlane_b32 s84, v176, 16\n" \
"v_readlane_b32 s85, v177, 16\n" \
"v_readlane_b32 s86, v178, 16\n" \
"v_readlane_b32 s87, v179, 16\n" \
"v_readlane_b32 s88, v176, 32\n" \
"v_readlane_b32 s89, v177, 32\n" \
"v_readlane_b32 s90, v178, 32\n" \
"v_readlane_b32 s91, v179, 32\n" \
"v_readlane_b32 s92, v176, 48\n" \
"v_readlane_b32 s93, v177, 48\n" \
"v_readlane_b32 s94, v178, 48\n" \
"v_readlane_b32 s95, v179, 48\n" \
"s_waitcnt vmcnt(0)\n" \
"s_add_u32 m0, s50, 0\n" \
"s_mov_b32 s68, 0\n" \
"s_nop 0\n" \
"buffer_load_dwordx4 v224, s[56:59], s68 offen offset:0 lds\n" \
"buffer_load_dwordx4 v225, s[56:59], s68 offen offset:1024 lds\n" \
"buffer_load_dwordx4 v226, s[56:59], s68 offen offset:2048 lds\n" \
"buffer_load_dwordx4 v227, s[56:59], s68 offen offset:3072 lds\n" \
"s_add_u32 m0, s50, 16384\n" \
"s_mov_b32 s69, 524288\n" \
"s_nop 0\n" \
"buffer_load_dwordx4 v224, s[56:59], s69 offen offset:0 lds\n" \
"buffer_load_dwordx4 v225, s[56:59], s69 offen offset:1024 lds\n" \
"buffer_load_dwordx4 v226, s[56:59], s69 offen offset:2048 lds\n" \
"buffer_load_dwordx4 v227, s[56:59], s69 offen offset:3072 lds\n" \
"v_lshlrev_b32 v96, 16, v16\n" \
"v_and_b32 v97, 0xffff0000, v16\n" \
"v_lshlrev_b32 v98, 16, v17\n" \
"v_and_b32 v99, 0xffff0000, v17\n" \
"v_lshlrev_b32 v100, 16, v18\n" \
"v_and_b32 v101, 0xffff0000, v18\n" \
"v_lshlrev_b32 v102, 16, v19\n" \
"v_and_b32 v103, 0xffff0000, v19\n" \
"v_lshlrev_b32 v104, 16, v20\n" \
"v_and_b32 v105, 0xffff0000, v20\n" \
"v_lshlrev_b32 v106, 16, v21\n" \
"v_and_b32 v107, 0xffff0000, v21\n" \
"v_lshlrev_b32 v108, 16, v22\n" \
"v_and_b32 v109, 0xffff0000, v22\n" \
"v_lshlrev_b32 v110, 16, v23\n" \
"v_and_b32 v111, 0xffff0000, v23\n" \
"v_lshlrev_b32 v112, 16, v24\n" \
"v_and_b32 v113, 0xffff0000, v24\n" \
"v_lshlrev_b32 v114, 16, v25\n" \
"v_and_b32 v115, 0xffff0000, v25\n" \
"v_lshlrev_b32 v116, 16, v26\n" \
"v_and_b32 v117, 0xffff0000, v26\n" \
"v_lshlrev_b32 v118, 16, v27\n" \
"v_and_b32 v119, 0xffff0000, v27\n" \
"v_lshlrev_b32 v120, 16, v28\n" \
"v_and_b32 v121, 0xffff0000, v28\n" \
"v_lshlrev_b32 v122, 16, v29\n" \
"v_and_b32 v123, 0xffff0000, v29\n" \
"v_lshlrev_b32 v124, 16, v30\n" \
"v_and_b32 v125, 0xffff0000, v30\n" \
"v_lshlrev_b32 v126, 16, v31\n" \
"v_and_b32 v127, 0xffff0000, v31\n" \
"v_lshlrev_b32 v128, 16, v32\n" \
"v_and_b32 v129, 0xffff0000, v32\n" \
"v_lshlrev_b32 v130, 16, v33\n" \
"v_and_b32 v131, 0xffff0000, v33\n" \
"v_lshlrev_b32 v132, 16, v34\n" \
"v_and_b32 v133, 0xffff0000, v34\n" \
"v_lshlrev_b32 v134, 16, v35\n" \
"v_and_b32 v135, 0xffff0000, v35\n" \
"v_lshlrev_b32 v136, 16, v36\n" \
"v_and_b32 v137, 0xffff0000, v36\n" \
"v_lshlrev_b32 v138, 16, v37\n" \
"v_and_b32 v139, 0xffff0000, v37\n" \
"v_lshlrev_b32 v140, 16, v38\n" \
"v_and_b32 v141, 0xffff0000, v38\n" \
"v_lshlrev_b32 v142, 16, v39\n" \
"v_and_b32 v143, 0xffff0000, v39\n" \
"v_lshlrev_b32 v144, 16, v40\n" \
"v_and_b32 v145, 0xffff0000, v40\n" \
"v_lshlrev_b32 v146, 16, v41\n" \
"v_and_b32 v147, 0xffff0000, v41\n" \
"v_lshlrev_b32 v148, 16, v42\n" \
"v_and_b32 v149, 0xffff0000, v42\n" \
"v_lshlrev_b32 v150, 16, v43\n" \
"v_and_b32 v151, 0xffff0000, v43\n" \
"v_lshlrev_b32 v152, 16, v44\n" \
"v_and_b32 v153, 0xffff0000, v44\n" \
"v_lshlrev_b32 v154, 16, v45\n" \
"v_and_b32 v155, 0xffff0000, v45\n" \
"v_lshlrev_b32 v156, 16, v46\n" \
"v_and_b32 v157, 0xffff0000, v46\n" \
"v_lshlrev_b32 v158, 16, v47\n" \
"v_and_b32 v159, 0xffff0000, v47\n" \
"v_lshlrev_b32 v160, 16, v48\n" \
"v_and_b32 v161, 0xffff0000, v48\n" \
"v_lshlrev_b32 v162, 16, v49\n" \
"v_and_b32 v163, 0xffff0000, v49\n" \
"v_lshlrev_b32 v164, 16, v50\n" \
"v_and_b32 v165, 0xffff0000, v50\n" \
"v_lshlrev_b32 v166, 16, v51\n" \
"v_and_b32 v167, 0xffff0000, v51\n" \
"v_lshlrev_b32 v168, 16, v52\n" \
"v_and_b32 v169, 0xffff0000, v52\n" \
"v_lshlrev_b32 v170, 16, v53\n" \
"v_and_b32 v171, 0xffff0000, v53\n" \
"v_lshlrev_b32 v172, 16, v54\n" \
"v_and_b32 v173, 0xffff0000, v54\n" \
"v_lshlrev_b32 v174, 16, v55\n" \
"v_and_b32 v175, 0xffff0000, v55\n" \
"v_lshlrev_b32 v176, 16, v56\n" \
"v_and_b32 v177, 0xffff0000, v56\n" \
"v_lshlrev_b32 v178, 16, v57\n" \
"v_and_b32 v179, 0xffff0000, v57\n" \
"v_lshlrev_b32 v180, 16, v58\n" \
"v_and_b32 v181, 0xffff0000, v58\n" \
"v_lshlrev_b32 v182, 16, v59\n" \
"v_and_b32 v183, 0xffff0000, v59\n" \
"v_lshlrev_b32 v184, 16, v60\n" \
"v_and_b32 v185, 0xffff0000, v60\n" \
"v_lshlrev_b32 v186, 16, v61\n" \
"v_and_b32 v187, 0xffff0000, v61\n" \
"v_lshlrev_b32 v188, 16, v62\n" \
"v_and_b32 v189, 0xffff0000, v62\n" \
"v_lshlrev_b32 v190, 16, v63\n" \
"v_and_b32 v191, 0xffff0000, v63\n" \
"v_pk_add_f32 v[64:65], v[64:65], s[80:81] op_sel:[0,0] op_sel_hi:[1,0]\n" \
"v_pk_add_f32 v[72:73], v[72:73], s[80:81] op_sel:[0,1] op_sel_hi:[1,1]\n" \
"v_pk_add_f32 v[80:81], v[80:81], s[82:83] op_sel:[0,0] op_sel_hi:[1,0]\n" \
"v_pk_add_f32 v[88:89], v[88:89], s[82:83] op_sel:[0,1] op_sel_hi:[1,1]\n" \
"v_pk_mul_f32 v[0:1], v[64:65], v[64:65]\n" \
"v_pk_mul_f32 v[2:3], v[72:73], v[72:73]\n" \
"v_pk_mul_f32 v[4:5], v[80:81], v[80:81]\n" \
"v_pk_mul_f32 v[6:7], v[88:89], v[88:89]\n" \
"v_pk_add_f32 v[66:67], v[66:67], s[80:81] op_sel:[0,0] op_sel_hi:[1,0]\n" \
"v_pk_add_f32 v[74:75], v[74:75], s[80:81] op_sel:[0,1] op_sel_hi:[1,1]\n" \
"v_pk_add_f32 v[82:83], v[82:83], s[82:83] op_sel:[0,0] op_sel_hi:[1,0]\n" \
"v_pk_add_f32 v[90:91], v[90:91], s[82:83] op_sel:[0,1] op_sel_hi:[1,1]\n" \
"v_pk_fma_f32 v[0:1], v[66:67], v[66:67], v[0:1]\n" \
"v_pk_fma_f32 v[2:3], v[74:75], v[74:75], v[2:3]\n" \
"v_pk_fma_f32 v[4:5], v[82:83], v[82:83], v[4:5]\n" \
"v_pk_fma_f32 v[6:7], v[90:91], v[90:91], v[6:7]\n" \
"v_pk_add_f32 v[68:69], v[68:69], s[80:81] op_sel:[0,0] op_sel_hi:[1,0]\n" \
"v_pk_add_f32 v[76:77], v[76:77], s[80:81] op_sel:[0,1] op_sel_hi:[1,1]\n" \
"v_pk_add_f32 v[84:85], v[84:85], s[82:83] op_sel:[0,0] op_sel_hi:[1,0]\n" \
"v_pk_add_f32 v[92:93], v[92:93], s[82:83] op_sel:[0,1] op_sel_hi:[1,1]\n" \
"v_pk_fma_f32 v[0:1], v[68:69], v[68:69], v[0:1]\n" \
"v_pk_fma_f32 v[2:3], v[76:77], v[76:77], v[2:3]\n" \
"v_pk_fma_f32 v[4:5], v[84:85], v[84:85], v[4:5]\n" \
"v_pk_fma_f32 v[6:7], v[92:93], v[92:93], v[6:7]\n" \
"v_pk_add_f32 v[70:71], v[70:71], s[80:81] op_sel:[0,0] op_sel_hi:[1,0]\n" \
"v_pk_add_f32 v[78:79], v[78:79], s[80:81] op_sel:[0,1] op_sel_hi:[1,1]\n" \
"v_pk_add_f32 v[86:87], v[86:87], s[82:83] op_sel:[0,0] op_sel_hi:[1,0]\n" \
"v_pk_add_f32 v[94:95], v[94:95], s[82:83] op_sel:[0,1] op_sel_hi:[1,1]\n" \
"v_pk_fma_f32 v[0:1], v[70:71], v[70:71], v[0:1]\n" \
"v_pk_fma_f32 v[2:3], v[78:79], v[78:79], v[2:3]\n" \
"v_pk_fma_f32 v[4:5], v[86:87], v[86:87], v[4:5]\n" \
"v_pk_fma_f32 v[6:7], v[94:95], v[94:95], v[6:7]\n" \
"v_pk_add_f32 v[96:97], v[96:97], s[84:85] op_sel:[0,0] op_sel_hi:[1,0]\n" \
"v_pk_add_f32 v[104:105], v[104:105], s[84:85] op_sel:[0,1] op_sel_hi:[1,1]\n" \
"v_pk_add_f32 v[112:113], v[112:113], s[86:87] op_sel:[0,0] op_sel_hi:[1,0]\n" \
"v_pk_add_f32 v[120:121], v[120:121], s[86:87] op_sel:[0,1] op_sel_hi:[1,1]\n" \
"v_pk_mul_f32 v[8:9], v[96:97], v[96:97]\n" \
"v_pk_mul_f32 v[10:11], v[104:105], v[104:105]\n" \
"v_pk_mul_f32 v[12:13], v[112:113], v[112:113]\n" \
"v_pk_mul_f32 v[14:15], v[120:121], v[120:121]\n" \
"v_pk_add_f32 v[98:99], v[98:99], s[84:85] op_sel:[0,0] op_sel_hi:[1,0]\n" \
"v_pk_add_f32 v[106:107], v[106:107], s[84:85] op_sel:[0,1] op_sel_hi:[1,1]\n" \
"v_pk_add_f32 v[114:115], v[114:115], s[86:87] op_sel:[0,0] op_sel_hi:[1,0]\n" \
"v_pk_add_f32 v[122:123], v[122:123], s[86:87] op_sel:[0,1] op_sel_hi:[1,1]\n" \
"v_pk_fma_f32 v[8:9], v[98:99], v[98:99], v[8:9]\n" \
"v_pk_fma_f32 v[10:11], v[106:107], v[106:107], v[10:11]\n" \
"v_pk_fma_f32 v[12:13], v[114:115], v[114:115], v[12:13]\n" \
"v_pk_fma_f32 v[14:15], v[122:123], v[122:123], v[14:15]\n" \
"v_pk_add_f32 v[100:101], v[100:101], s[84:85] op_sel:[0,0] op_sel_hi:[1,0]\n" \
"v_pk_add_f32 v[108:109], v[108:109], s[84:85] op_sel:[0,1] op_sel_hi:[1,1]\n" \
"v_pk_add_f32 v[116:117], v[116:117], s[86:87] op_sel:[0,0] op_sel_hi:[1,0]\n" \
"v_pk_add_f32 v[124:125], v[124:125], s[86:87] op_sel:[0,1] op_sel_hi:[1,1]\n" \
"v_pk_fma_f32 v[8:9], v[100:101], v[100:101], v[8:9]\n" \
"v_pk_fma_f32 v[10:11], v[108:109], v[108:109], v[10:11]\n" \
"v_pk_fma_f32 v[12:13], v[116:117], v[116:117], v[12:13]\n" \
"v_pk_fma_f32 v[14:15], v[124:125], v[124:125], v[14:15]\n" \
"v_pk_add_f32 v[102:103], v[102:103], s[84:85] op_sel:[0,0] op_sel_hi:[1,0]\n" \
"v_pk_add_f32 v[110:111], v[110:111], s[84:85] op_sel:[0,1] op_sel_hi:[1,1]\n" \
"v_pk_add_f32 v[118:119], v[118:119], s[86:87] op_sel:[0,0] op_sel_hi:[1,0]\n" \
"v_pk_add_f32 v[126:127], v[126:127], s[86:87] op_sel:[0,1] op_sel_hi:[1,1]\n" \
"v_pk_fma_f32 v[8:9], v[102:103], v[102:103], v[8:9]\n" \
"v_pk_fma_f32 v[10:11], v[110:111], v[110:111], v[10:11]\n" \
"v_pk_fma_f32 v[12:13], v[118:119], v[118:119], v[12:13]\n" \
"v_pk_fma_f32 v[14:15], v[126:127], v[126:127], v[14:15]\n" \
"v_pk_add_f32 v[128:129], v[128:129], s[88:89] op_sel:[0,0] op_sel_hi:[1,0]\n" \
"v_pk_add_f32 v[136:137], v[136:137], s[88:89] op_sel:[0,1] op_sel_hi:[1,1]\n" \
"v_pk_add_f32 v[144:145], v[144:145], s[90:91] op_sel:[0,0] op_sel_hi:[1,0]\n" \
"v_pk_add_f32 v[152:153], v[152:153], s[90:91] op_sel:[0,1] op_sel_hi:[1,1]\n" \
"v_pk_mul_f32 v[16:17], v[128:129], v[128:129]\n" \
"v_pk_mul_f32 v[18:19], v[136:137], v[136:137]\n" \
"v_pk_mul_f32 v[20:21], v[144:145], v[144:145]\n" \
"v_pk_mul_f32 v[22:23], v[152:153], v[152:153]\n" \
"v_pk_add_f32 v[130:131], v[130:131], s[88:89] op_sel:[0,0] op_sel_hi:[1,0]\n" \
"v_pk_add_f32 v[138:139], v[138:139], s[88:89] op_sel:[0,1] op_sel_hi:[1,1]\n" \
"v_pk_add_f32 v[146:147], v[146:147], s[90:91] op_sel:[0,0] op_sel_hi:[1,0]\n" \
"v_pk_add_f32 v[154:155], v[154:155], s[90:91] op_sel:[0,1] op_sel_hi:[1,1]\n" \
"v_pk_fma_f32 v[16:17], v[130:131], v[130:131], v[16:17]\n" \
"v_pk_fma_f32 v[18:19], v[138:139], v[138:139], v[18:19]\n" \
"v_pk_fma_f32 v[20:21], v[146:147], v[146:147], v[20:21]\n" \
"v_pk_fma_f32 v[22:23], v[154:155], v[154:155], v[22:23]\n" \
"v_pk_add_f32 v[132:133], v[132:133], s[88:89] op_sel:[0,0] op_sel_hi:[1,0]\n" \
"v_pk_add_f32 v[140:141], v[140:141], s[88:89] op_sel:[0,1] op_sel_hi:[1,1]\n" \
"v_pk_add_f32 v[148:149], v[148:149], s[90:91] op_sel:[0,0] op_sel_hi:[1,0]\n" \
"v_pk_add_f32 v[156:157], v[156:157], s[90:91] op_sel:[0,1] op_sel_hi:[1,1]\n" \
"v_pk_fma_f32 v[16:17], v[132:133], v[132:133], v[16:17]\n" \
"v_pk_fma_f32 v[18:19], v[140:141], v[140:141], v[18:19]\n" \
"v_pk_fma_f32 v[20:21], v[148:149], v[148:149], v[20:21]\n" \
"v_pk_fma_f32 v[22:23], v[156:157], v[156:157], v[22:23]\n" \
"v_pk_add_f32 v[134:135], v[134:135], s[88:89] op_sel:[0,0] op_sel_hi:[1,0]\n" \
"v_pk_add_f32 v[142:143], v[142:143], s[88:89] op_sel:[0,1] op_sel_hi:[1,1]\n" \
"v_pk_add_f32 v[150:151], v[150:151], s[90:91] op_sel:[0,0] op_sel_hi:[1,0]\n" \
"v_pk_add_f32 v[158:159], v[158:159], s[90:91] op_sel:[0,1] op_sel_hi:[1,1]\n" \
"v_pk_fma_f32 v[16:17], v[134:135], v[134:135], v[16:17]\n" \
"v_pk_fma_f32 v[18:19], v[142:143], v[142:143], v[18:19]\n" \
"v_pk_fma_f32 v[20:21], v[150:151], v[150:151], v[20:21]\n" \
"v_pk_fma_f32 v[22:23], v[158:159], v[158:159], v[22:23]\n" \
"v_pk_add_f32 v[160:161], v[160:161], s[92:93] op_sel:[0,0] op_sel_hi:[1,0]\n" \
"v_pk_add_f32 v[168:169], v[168:169], s[92:93] op_sel:[0,1] op_sel_hi:[1,1]\n" \
"v_pk_add_f32 v[176:177], v[176:177], s[94:95] op_sel:[0,0] op_sel_hi:[1,0]\n" \
"v_pk_add_f32 v[184:185], v[184:185], s[94:95] op_sel:[0,1] op_sel_hi:[1,1]\n" \
"v_pk_mul_f32 v[24:25], v[160:161], v[160:161]\n" \
"v_pk_mul_f32 v[26:27], v[168:169], v[168:169]\n" \
"v_pk_mul_f32 v[28:29], v[176:177], v[176:177]\n" \
"v_pk_mul_f32 v[30:31], v[184:185], v[184:185]\n" \
"v_pk_add_f32 v[162:163], v[162:163], s[92:93] op_sel:[0,0] op_sel_hi:[1,0]\n" \
"v_pk_add_f32 v[170:171], v[170:171], s[92:93] op_sel:[0,1] op_sel_hi:[1,1]\n" \
"v_pk_add_f32 v[178:179], v[178:179], s[94:95] op_sel:[0,0] op_sel_hi:[1,0]\n" \
"v_pk_add_f32 v[186:187], v[186:187], s[94:95] op_sel:[0,1] op_sel_hi:[1,1]\n" \
"v_pk_fma_f32 v[24:25], v[162:163], v[162:163], v[24:25]\n" \
"v_pk_fma_f32 v[26:27], v[170:171], v[170:171], v[26:27]\n" \
"v_pk_fma_f32 v[28:29], v[178:179], v[178:179], v[28:29]\n" \
"v_pk_fma_f32 v[30:31], v[186:187], v[186:187], v[30:31]\n" \
"v_pk_add_f32 v[164:165], v[164:165], s[92:93] op_sel:[0,0] op_sel_hi:[1,0]\n" \
"v_pk_add_f32 v[172:173], v[172:173], s[92:93] op_sel:[0,1] op_sel_hi:[1,1]\n" \
"v_pk_add_f32 v[180:181], v[180:181], s[94:95] op_sel:[0,0] op_sel_hi:[1,0]\n" \
"v_pk_add_f32 v[188:189], v[188:189], s[94:95] op_sel:[0,1] op_sel_hi:[1,1]\n" \
"v_pk_fma_f32 v[24:25], v[164:165], v[164:165], v[24:25]\n" \
"v_pk_fma_f32 v[26:27], v[172:173], v[172:173], v[26:27]\n" \
"v_pk_fma_f32 v[28:29], v[180:181], v[180:181], v[28:29]\n" \
"v_pk_fma_f32 v[30:31], v[188:189], v[188:189], v[30:31]\n" \
"v_pk_add_f32 v[166:167], v[166:167], s[92:93] op_sel:[0,0] op_sel_hi:[1,0]\n" \
"v_pk_add_f32 v[174:175], v[174:175], s[92:93] op_sel:[0,1] op_sel_hi:[1,1]\n" \
"v_pk_add_f32 v[182:183], v[182:183], s[94:95] op_sel:[0,0] op_sel_hi:[1,0]\n" \
"v_pk_add_f32 v[190:191], v[190:191], s[94:95] op_sel:[0,1] op_sel_hi:[1,1]\n" \
"v_pk_fma_f32 v[24:25], v[166:167], v[166:167], v[24:25]\n" \
"v_pk_fma_f32 v[26:27], v[174:175], v[174:175], v[26:27]\n" \
"v_pk_fma_f32 v[28:29], v[182:183], v[182:183], v[28:29]\n" \
"v_pk_fma_f32 v[30:31], v[190:191], v[190:191], v[30:31]\n" \
"v_add_f32 v32, v0, v1\n" \
"v_add_f32 v33, v2, v3\n" \
"v_add_f32 v34, v4, v5\n" \
"v_add_f32 v35, v6, v7\n" \
"v_add_f32 v36, v8, v9\n" \
"v_add_f32 v37, v10, v11\n" \
"v_add_f32 v38, v12, v13\n" \
"v_add_f32 v39, v14, v15\n" \
"v_add_f32 v40, v16, v17\n" \
"v_add_f32 v41, v18, v19\n" \
"v_add_f32 v42, v20, v21\n" \
"v_add_f32 v43, v22, v23\n" \
"v_add_f32 v44, v24, v25\n" \
"v_add_f32 v45, v26, v27\n" \
"v_add_f32 v46, v28, v29\n" \
"v_add_f32 v47, v30, v31\n" \
"v_mul_f32 v32, v233, v32\n" \
"v_mul_f32 v33, v233, v33\n" \
"v_mul_f32 v34, v233, v34\n" \
"v_mul_f32 v35, v233, v35\n" \
"v_mul_f32 v36, v233, v36\n" \
"v_mul_f32 v37, v233, v37\n" \
"v_mul_f32 v38, v233, v38\n" \
"v_mul_f32 v39, v233, v39\n" \
"v_mul_f32 v40, v233, v40\n" \
"v_mul_f32 v41, v233, v41\n" \
"v_mul_f32 v42, v233, v42\n" \
"v_mul_f32 v43, v233, v43\n" \
"v_mul_f32 v44, v233, v44\n" \
"v_mul_f32 v45, v233, v45\n" \
"v_mul_f32 v46, v233, v46\n" \
"v_mul_f32 v47, v233, v47\n" \
"v_mov_b32 v240, 0x3727c5ac\n" \
"s_nop 3\n" \
"v_permlane32_swap_b32 v32, v40\n" \
"v_permlane32_swap_b32 v33, v41\n" \
"v_permlane32_swap_b32 v34, v42\n" \
"v_permlane32_swap_b32 v35, v43\n" \
"v_permlane32_swap_b32 v36, v44\n" \
"v_permlane32_swap_b32 v37, v45\n" \
"v_permlane32_swap_b32 v38, v46\n" \
"v_permlane32_swap_b32 v39, v47\n" \
"v_add_f32 v32, v32, v40\n" \
"v_add_f32 v33, v33, v41\n" \
"v_add_f32 v34, v34, v42\n" \
"v_add_f32 v35, v35, v43\n" \
"v_add_f32 v36, v36, v44\n" \
"v_add_f32 v37, v37, v45\n" \
"v_add_f32 v38, v38, v46\n" \
"v_add_f32 v39, v39, v47\n" \
"v_permlane16_swap_b32 v32, v36\n" \
"v_permlane16_swap_b32 v33, v37\n" \
"v_permlane16_swap_b32 v34, v38\n" \
"v_permlane16_swap_b32 v35, v39\n" \
"v_add_f32 v32, v32, v36\n" \
"v_add_f32 v33, v33, v37\n" \
"v_add_f32 v34, v34, v38\n" \
"v_add_f32 v35, v35, v39\n" \
"v_add_f32_dpp v32, v32, v32 row_ror:8 row_mask:0xf bank_mask:0xf\n" \
"v_add_f32_dpp v33, v33, v33 row_ror:8 row_mask:0xf bank_mask:0xf\n" \
"v_add_f32_dpp v34, v34, v34 row_ror:8 row_mask:0xf bank_mask:0xf\n" \
"v_add_f32_dpp v35, v35, v35 row_ror:8 row_mask:0xf bank_mask:0xf\n" \
"v_add_f32_dpp v32, v32, v32 row_ror:4 row_mask:0xf bank_mask:0xf\n" \
"v_add_f32_dpp v33, v33, v33 row_ror:4 row_mask:0xf bank_mask:0xf\n" \
"v_add_f32_dpp v34, v34, v34 row_ror:4 row_mask:0xf bank_mask:0xf\n" \
"v_add_f32_dpp v35, v35, v35 row_ror:4 row_mask:0xf bank_mask:0xf\n" \
"v_add_f32_dpp v32, v32, v32 row_ror:2 row_mask:0xf bank_mask:0xf\n" \
"v_add_f32_dpp v33, v33, v33 row_ror:2 row_mask:0xf bank_mask:0xf\n" \
"v_add_f32_dpp v34, v34, v34 row_ror:2 row_mask:0xf bank_mask:0xf\n" \
"v_add_f32_dpp v35, v35, v35 row_ror:2 row_mask:0xf bank_mask:0xf\n" \
"v_add_f32_dpp v32, v32, v32 row_ror:1 row_mask:0xf bank_mask:0xf\n" \
"v_add_f32_dpp v33, v33, v33 row_ror:1 row_mask:0xf bank_mask:0xf\n" \
"v_add_f32_dpp v34, v34, v34 row_ror:1 row_mask:0xf bank_mask:0xf\n" \
"v_add_f32_dpp v35, v35, v35 row_ror:1 row_mask:0xf bank_mask:0xf\n" \
"v_fmamk_f32 v32, v32, 0x3b124925, v240\n" \
"v_fmamk_f32 v33, v33, 0x3b124925, v240\n" \
"v_fmamk_f32 v34, v34, 0x3b124925, v240\n" \
"v_fmamk_f32 v35, v35, 0x3b124925, v240\n" \
"v_rsq_f32 v32, v32\n" \
"v_rsq_f32 v33, v33\n" \
"v_rsq_f32 v34, v34\n" \
"v_rsq_f32 v35, v35\n" \
"s_nop 1\n" \
"v_readlane_b32 s80, v32, 0\n" \
"v_readlane_b32 s81, v33, 0\n" \
"v_readlane_b32 s82, v34, 0\n" \
"v_readlane_b32 s83, v35, 0\n" \
"v_readlane_b32 s84, v32, 16\n" \
"v_readlane_b32 s85, v33, 16\n" \
"v_readlane_b32 s86, v34, 16\n" \
"v_readlane_b32 s87, v35, 16\n" \
"v_readlane_b32 s88, v32, 32\n" \
"v_readlane_b32 s89, v33, 32\n" \
"v_readlane_b32 s90, v34, 32\n" \
"v_readlane_b32 s91, v35, 32\n" \
"v_readlane_b32 s92, v32, 48\n" \
"v_readlane_b32 s93, v33, 48\n" \
"v_readlane_b32 s94, v34, 48\n" \
"v_readlane_b32 s95, v35, 48\n" \
"s_add_u32 m0, s50, 32768\n" \
"s_mov_b32 s68, 1048576\n" \
"s_nop 0\n" \
"buffer_load_dwordx4 v224, s[56:59], s68 offen offset:0 lds\n" \
"buffer_load_dwordx4 v225, s[56:59], s68 offen offset:1024 lds\n" \
"buffer_load_dwordx4 v226, s[56:59], s68 offen offset:2048 lds\n" \
"buffer_load_dwordx4 v227, s[56:59], s68 offen offset:3072 lds\n" \
"s_add_u32 m0, s50, 65536\n" \
"s_mov_b32 s69, 1572864\n" \
"s_nop 0\n" \
"buffer_load_dwordx4 v224, s[56:59], s69 offen offset:0 lds\n" \
"buffer_load_dwordx4 v225, s[56:59], s69 offen offset:1024 lds\n" \
"buffer_load_dwordx4 v226, s[56:59], s69 offen offset:2048 lds\n" \
"buffer_load_dwordx4 v227, s[56:59], s69 offen offset:3072 lds\n" \
"v_pk_mul_f32 v[64:65], v[64:65], s[80:81] op_sel:[0,0] op_sel_hi:[1,0]\n" \
"v_pk_mul_f32 v[66:67], v[66:67], s[80:81] op_sel:[0,0] op_sel_hi:[1,0]\n" \
"v_pk_mul_f32 v[68:69], v[68:69], s[80:81] op_sel:[0,0] op_sel_hi:[1,0]\n" \
"v_pk_mul_f32 v[70:71], v[70:71], s[80:81] op_sel:[0,0] op_sel_hi:[1,0]\n" \
"v_pk_mul_f32 v[72:73], v[72:73], s[80:81] op_sel:[0,1] op_sel_hi:[1,1]\n" \
"v_pk_mul_f32 v[74:75], v[74:75], s[80:81] op_sel:[0,1] op_sel_hi:[1,1]\n" \
"v_pk_mul_f32 v[76:77], v[76:77], s[80:81] op_sel:[0,1] op_sel_hi:[1,1]\n" \
"v_pk_mul_f32 v[78:79], v[78:79], s[80:81] op_sel:[0,1] op_sel_hi:[1,1]\n" \
"v_pk_mul_f32 v[80:81], v[80:81], s[82:83] op_sel:[0,0] op_sel_hi:[1,0]\n" \
"v_pk_mul_f32 v[82:83], v[82:83], s[82:83] op_sel:[0,0] op_sel_hi:[1,0]\n" \
"v_pk_mul_f32 v[84:85], v[84:85], s[82:83] op_sel:[0,0] op_sel_hi:[1,0]\n" \
"v_pk_mul_f32 v[86:87], v[86:87], s[82:83] op_sel:[0,0] op_sel_hi:[1,0]\n" \
"v_pk_mul_f32 v[88:89], v[88:89], s[82:83] op_sel:[0,1] op_sel_hi:[1,1]\n" \
"v_pk_mul_f32 v[90:91], v[90:91], s[82:83] op_sel:[0,1] op_sel_hi:[1,1]\n" \
"v_pk_mul_f32 v[92:93], v[92:93], s[82:83] op_sel:[0,1] op_sel_hi:[1,1]\n" \
"v_pk_mul_f32 v[94:95], v[94:95], s[82:83] op_sel:[0,1] op_sel_hi:[1,1]\n" \
"v_pk_mul_f32 v[96:97], v[96:97], s[84:85] op_sel:[0,0] op_sel_hi:[1,0]\n" \
"v_pk_mul_f32 v[98:99], v[98:99], s[84:85] op_sel:[0,0] op_sel_hi:[1,0]\n" \
"v_pk_mul_f32 v[100:101], v[100:101], s[84:85] op_sel:[0,0] op_sel_hi:[1,0]\n" \
"v_pk_mul_f32 v[102:103], v[102:103], s[84:85] op_sel:[0,0] op_sel_hi:[1,0]\n" \
"v_pk_mul_f32 v[104:105], v[104:105], s[84:85] op_sel:[0,1] op_sel_hi:[1,1]\n" \
"v_pk_mul_f32 v[106:107], v[106:107], s[84:85] op_sel:[0,1] op_sel_hi:[1,1]\n" \
"v_pk_mul_f32 v[108:109], v[108:109], s[84:85] op_sel:[0,1] op_sel_hi:[1,1]\n" \
"v_pk_mul_f32 v[110:111], v[110:111], s[84:85] op_sel:[0,1] op_sel_hi:[1,1]\n" \
"v_pk_mul_f32 v[112:113], v[112:113], s[86:87] op_sel:[0,0] op_sel_hi:[1,0]\n" \
"v_pk_mul_f32 v[114:115], v[114:115], s[86:87] op_sel:[0,0] op_sel_hi:[1,0]\n" \
"v_pk_mul_f32 v[116:117], v[116:117], s[86:87] op_sel:[0,0] op_sel_hi:[1,0]\n" \
"v_pk_mul_f32 v[118:119], v[118:119], s[86:87] op_sel:[0,0] op_sel_hi:[1,0]\n" \
"v_pk_mul_f32 v[120:121], v[120:121], s[86:87] op_sel:[0,1] op_sel_hi:[1,1]\n" \
"v_pk_mul_f32 v[122:123], v[122:123], s[86:87] op_sel:[0,1] op_sel_hi:[1,1]\n" \
"v_pk_mul_f32 v[124:125], v[124:125], s[86:87] op_sel:[0,1] op_sel_hi:[1,1]\n" \
"v_pk_mul_f32 v[126:127], v[126:127], s[86:87] op_sel:[0,1] op_sel_hi:[1,1]\n" \
"v_pk_mul_f32 v[128:129], v[128:129], s[88:89] op_sel:[0,0] op_sel_hi:[1,0]\n" \
"v_pk_mul_f32 v[130:131], v[130:131], s[88:89] op_sel:[0,0] op_sel_hi:[1,0]\n" \
"v_pk_mul_f32 v[132:133], v[132:133], s[88:89] op_sel:[0,0] op_sel_hi:[1,0]\n" \
"v_pk_mul_f32 v[134:135], v[134:135], s[88:89] op_sel:[0,0] op_sel_hi:[1,0]\n" \
"v_pk_mul_f32 v[136:137], v[136:137], s[88:89] op_sel:[0,1] op_sel_hi:[1,1]\n" \
"v_pk_mul_f32 v[138:139], v[138:139], s[88:89] op_sel:[0,1] op_sel_hi:[1,1]\n" \
"v_pk_mul_f32 v[140:141], v[140:141], s[88:89] op_sel:[0,1] op_sel_hi:[1,1]\n" \
"v_pk_mul_f32 v[142:143], v[142:143], s[88:89] op_sel:[0,1] op_sel_hi:[1,1]\n" \
"v_pk_mul_f32 v[144:145], v[144:145], s[90:91] op_sel:[0,0] op_sel_hi:[1,0]\n" \
"v_pk_mul_f32 v[146:147], v[146:147], s[90:91] op_sel:[0,0] op_sel_hi:[1,0]\n" \
"v_pk_mul_f32 v[148:149], v[148:149], s[90:91] op_sel:[0,0] op_sel_hi:[1,0]\n" \
"v_pk_mul_f32 v[150:151], v[150:151], s[90:91] op_sel:[0,0] op_sel_hi:[1,0]\n" \
"v_pk_mul_f32 v[152:153], v[152:153], s[90:91] op_sel:[0,1] op_sel_hi:[1,1]\n" \
"v_pk_mul_f32 v[154:155], v[154:155], s[90:91] op_sel:[0,1] op_sel_hi:[1,1]\n" \
"v_pk_mul_f32 v[156:157], v[156:157], s[90:91] op_sel:[0,1] op_sel_hi:[1,1]\n" \
"v_pk_mul_f32 v[158:159], v[158:159], s[90:91] op_sel:[0,1] op_sel_hi:[1,1]\n" \
"v_pk_mul_f32 v[160:161], v[160:161], s[92:93] op_sel:[0,0] op_sel_hi:[1,0]\n" \
"v_pk_mul_f32 v[162:163], v[162:163], s[92:93] op_sel:[0,0] op_sel_hi:[1,0]\n" \
"v_pk_mul_f32 v[164:165], v[164:165], s[92:93] op_sel:[0,0] op_sel_hi:[1,0]\n" \
"v_pk_mul_f32 v[166:167], v[166:167], s[92:93] op_sel:[0,0] op_sel_hi:[1,0]\n" \
"v_pk_mul_f32 v[168:169], v[168:169], s[92:93] op_sel:[0,1] op_sel_hi:[1,1]\n" \
"v_pk_mul_f32 v[170:171], v[170:171], s[92:93] op_sel:[0,1] op_sel_hi:[1,1]\n" \
"v_pk_mul_f32 v[172:173], v[172:173], s[92:93] op_sel:[0,1] op_sel_hi:[1,1]\n" \
"v_pk_mul_f32 v[174:175], v[174:175], s[92:93] op_sel:[0,1] op_sel_hi:[1,1]\n" \
"v_pk_mul_f32 v[176:177], v[176:177], s[94:95] op_sel:[0,0] op_sel_hi:[1,0]\n" \
"v_pk_mul_f32 v[178:179], v[178:179], s[94:95] op_sel:[0,0] op_sel_hi:[1,0]\n" \
"v_pk_mul_f32 v[180:181], v[180:181], s[94:95] op_sel:[0,0] op_sel_hi:[1,0]\n" \
"v_pk_mul_f32 v[182:183], v[182:183], s[94:95] op_sel:[0,0] op_sel_hi:[1,0]\n" \
"v_pk_mul_f32 v[184:185], v[184:185], s[94:95] op_sel:[0,1] op_sel_hi:[1,1]\n" \
"v_pk_mul_f32 v[186:187], v[186:187], s[94:95] op_sel:[0,1] op_sel_hi:[1,1]\n" \
"v_pk_mul_f32 v[188:189], v[188:189], s[94:95] op_sel:[0,1] op_sel_hi:[1,1]\n" \
"v_pk_mul_f32 v[190:191], v[190:191], s[94:95] op_sel:[0,1] op_sel_hi:[1,1]\n" \
"s_mov_b32 s25, 0x100000\n" \
"s_movk_i32 s70, 7168\n" \
"L_poll_%=:\n" \
"buffer_load_dwordx4 v[32:35], v234, s[76:79], 0 offen offset:0 sc1\n" \
"buffer_load_dwordx4 v[36:39], v234, s[76:79], 0 offen offset:3584 sc1\n" \
"buffer_load_dwordx4 v[40:43], v234, s[76:79], s70 offen offset:0 sc1\n" \
"buffer_load_dwordx4 v[44:47], v234, s[76:79], s70 offen offset:3584 sc1\n" \
"s_sub_u32 s25, s25, 1\n" \
"s_cbranch_scc1 L_got_%=\n" \
"s_waitcnt vmcnt(0)\n" \
"v_min_u32 v48, v33, v35\n" \
"v_max_u32 v49, v33, v35\n" \
"v_min3_u32 v48, v48, v37, v39\n" \
"v_max3_u32 v49, v49, v37, v39\n" \
"v_min3_u32 v48, v48, v41, v43\n" \
"v_max3_u32 v49, v49, v41, v43\n" \
"v_min3_u32 v48, v48, v45, v47\n" \
"v_max3_u32 v49, v49, v45, v47\n" \
"v_cmp_ne_u32 vcc, s30, v48\n" \
"v_cmp_ne_u32 s[68:69], s30, v49\n" \
"s_or_b64 vcc, vcc, s[68:69]\n" \
"s_cbranch_scc1 L_poll_%=\n" \
"L_got_%=:\n" \
"s_waitcnt vmcnt(0)\n" \
"v_add_f32 v32, v32, v36\n" \
"v_add_f32 v33, v34, v38\n" \
"v_add_f32 v40, v40, v44\n" \
"v_add_f32 v41, v42, v46\n" \
"ds_write_b64 v235, v[32:33]\n" \
"ds_write_b64 v235, v[40:41] offset:1792\n" \
"s_waitcnt lgkmcnt(0)\n" \
"s_barrier\n" \
"ds_read_b128 v[0:3], v236 offset:0\n" \
"ds_read_b128 v[4:7], v236 offset:16\n" \
"ds_read_b128 v[8:11], v236 offset:1792\n" \
"ds_read_b128 v[12:15], v236 offset:1808\n" \
"s_add_u32 m0, s50, 49152\n" \
"s_mov_b32 s68, 2097152\n" \
"s_nop 0\n" \
"buffer_load_dwordx4 v224, s[56:59], s68 offen offset:0 lds\n" \
"buffer_load_dwordx4 v225, s[56:59], s68 offen offset:1024 lds\n" \
"buffer_load_dwordx4 v226, s[56:59], s68 offen offset:2048 lds\n" \
"buffer_load_dwordx4 v227, s[56:59], s68 offen offset:3072 lds\n" \
"s_add_u32 m0, s50, 81920\n" \
"s_mov_b32 s69, 2621440\n" \
"s_nop 0\n" \
"buffer_load_dwordx4 v224, s[56:59], s69 offen offset:0 lds\n" \
"buffer_load_dwordx4 v225, s[56:59], s69 offen offset:1024 lds\n" \
"buffer_load_dwordx4 v226, s[56:59], s69 offen offset:2048 lds\n" \
"buffer_load_dwordx4 v227, s[56:59], s69 offen offset:3072 lds\n" \
"s_waitcnt lgkmcnt(0)\n" \
"v_xor_b32 v48, 0, v232\n" \
"v_pk_fma_f32 v[64:65], v[64:65], v[0:1], v[8:9]\n" \
"v_pk_fma_f32 v[66:67], v[66:67], v[2:3], v[10:11]\n" \
"v_pk_fma_f32 v[68:69], v[68:69], v[4:5], v[12:13]\n" \
"v_pk_fma_f32 v[70:71], v[70:71], v[6:7], v[14:15]\n" \
"v_cvt_pk_bf16_f32 v64, v64, v65\n" \
"v_cvt_pk_bf16_f32 v65, v66, v67\n" \
"v_cvt_pk_bf16_f32 v66, v68, v69\n" \
"v_cvt_pk_bf16_f32 v67, v70, v71\n" \
"ds_write_b128 v48, v[64:67]\n" \
"v_xor_b32 v49, 16, v232\n" \
"v_pk_fma_f32 v[72:73], v[72:73], v[0:1], v[8:9]\n" \
"v_pk_fma_f32 v[74:75], v[74:75], v[2:3], v[10:11]\n" \
"v_pk_fma_f32 v[76:77], v[76:77], v[4:5], v[12:13]\n" \
"v_pk_fma_f32 v[78:79], v[78:79], v[6:7], v[14:15]\n" \
"v_cvt_pk_bf16_f32 v72, v72, v73\n" \
"v_cvt_pk_bf16_f32 v73, v74, v75\n" \
"v_cvt_pk_bf16_f32 v74, v76, v77\n" \
"v_cvt_pk_bf16_f32 v75, v78, v79\n" \
"ds_write_b128 v49, v[72:75]\n" \
"v_xor_b32 v48, 32, v232\n" \
"v_pk_fma_f32 v[80:81], v[80:81], v[0:1], v[8:9]\n" \
"v_pk_fma_f32 v[82:83], v[82:83], v[2:3], v[10:11]\n" \
"v_pk_fma_f32 v[84:85], v[84:85], v[4:5], v[12:13]\n" \
"v_pk_fma_f32 v[86:87], v[86:87], v[6:7], v[14:15]\n" \
"v_cvt_pk_bf16_f32 v80, v80, v81\n" \
"v_cvt_pk_bf16_f32 v81, v82, v83\n" \
"v_cvt_pk_bf16_f32 v82, v84, v85\n" \
"v_cvt_pk_bf16_f32 v83, v86, v87\n" \
"ds_write_b128 v48, v[80:83]\n" \
"v_xor_b32 v49, 48, v232\n" \
"v_pk_fma_f32 v[88:89], v[88:89], v[0:1], v[8:9]\n" \
"v_pk_fma_f32 v[90:91], v[90:91], v[2:3], v[10:11]\n" \
"v_pk_fma_f32 v[92:93], v[92:93], v[4:5], v[12:13]\n" \
"v_pk_fma_f32 v[94:95], v[94:95], v[6:7], v[14:15]\n" \
"v_cvt_pk_bf16_f32 v88, v88, v89\n" \
"v_cvt_pk_bf16_f32 v89, v90, v91\n" \
"v_cvt_pk_bf16_f32 v90, v92, v93\n" \
"v_cvt_pk_bf16_f32 v91, v94, v95\n" \
"ds_write_b128 v49, v[88:91]\n" \
"v_xor_b32 v48, 64, v232\n" \
"v_pk_fma_f32 v[96:97], v[96:97], v[0:1], v[8:9]\n" \
"v_pk_fma_f32 v[98:99], v[98:99], v[2:3], v[10:11]\n" \
"v_pk_fma_f32 v[100:101], v[100:101], v[4:5], v[12:13]\n" \
"v_pk_fma_f32 v[102:103], v[102:103], v[6:7], v[14:15]\n" \
"v_cvt_pk_bf16_f32 v96, v96, v97\n" \
"v_cvt_pk_bf16_f32 v97, v98, v99\n" \
"v_cvt_pk_bf16_f32 v98, v100, v101\n" \
"v_cvt_pk_bf16_f32 v99, v102, v103\n" \
"ds_write_b128 v48, v[96:99]\n" \
"v_xor_b32 v49, 80, v232\n" \
"v_pk_fma_f32 v[104:105], v[104:105], v[0:1], v[8:9]\n" \
"v_pk_fma_f32 v[106:107], v[106:107], v[2:3], v[10:11]\n" \
"v_pk_fma_f32 v[108:109], v[108:109], v[4:5], v[12:13]\n" \
"v_pk_fma_f32 v[110:111], v[110:111], v[6:7], v[14:15]\n" \
"v_cvt_pk_bf16_f32 v104, v104, v105\n" \
"v_cvt_pk_bf16_f32 v105, v106, v107\n" \
"v_cvt_pk_bf16_f32 v106, v108, v109\n" \
"v_cvt_pk_bf16_f32 v107, v110, v111\n" \
"ds_write_b128 v49, v[104:107]\n" \
"v_xor_b32 v48, 96, v232\n" \
"v_pk_fma_f32 v[112:113], v[112:113], v[0:1], v[8:9]\n" \
"v_pk_fma_f32 v[114:115], v[114:115], v[2:3], v[10:11]\n" \
"v_pk_fma_f32 v[116:117], v[116:117], v[4:5], v[12:13]\n" \
"v_pk_fma_f32 v[118:119], v[118:119], v[6:7], v[14:15]\n" \
"v_cvt_pk_bf16_f32 v112, v112, v113\n" \
"v_cvt_pk_bf16_f32 v113, v114, v115\n" \
"v_cvt_pk_bf16_f32 v114, v116, v117\n" \
"v_cvt_pk_bf16_f32 v115, v118, v119\n" \
"ds_write_b128 v48, v[112:115]\n" \
"v_xor_b32 v49, 112, v232\n" \
"v_pk_fma_f32 v[120:121], v[120:121], v[0:1], v[8:9]\n" \
"v_pk_fma_f32 v[122:123], v[122:123], v[2:3], v[10:11]\n" \
"v_pk_fma_f32 v[124:125], v[124:125], v[4:5], v[12:13]\n" \
"v_pk_fma_f32 v[126:127], v[126:127], v[6:7], v[14:15]\n" \
"v_cvt_pk_bf16_f32 v120, v120, v121\n" \
"v_cvt_pk_bf16_f32 v121, v122, v123\n" \
"v_cvt_pk_bf16_f32 v122, v124, v125\n" \
"v_cvt_pk_bf16_f32 v123, v126, v127\n" \
"ds_write_b128 v49, v[120:123]\n" \
"v_xor_b32 v48, 128, v232\n" \
"v_pk_fma_f32 v[128:129], v[128:129], v[0:1], v[8:9]\n" \
"v_pk_fma_f32 v[130:131], v[130:131], v[2:3], v[10:11]\n" \
"v_pk_fma_f32 v[132:133], v[132:133], v[4:5], v[12:13]\n" \
"v_pk_fma_f32 v[134:135], v[134:135], v[6:7], v[14:15]\n" \
"v_cvt_pk_bf16_f32 v128, v128, v129\n" \
"v_cvt_pk_bf16_f32 v129, v130, v131\n" \
"v_cvt_pk_bf16_f32 v130, v132, v133\n" \
"v_cvt_pk_bf16_f32 v131, v134, v135\n" \
"ds_write_b128 v48, v[128:131]\n" \
"v_xor_b32 v49, 144, v232\n" \
"v_pk_fma_f32 v[136:137], v[136:137], v[0:1], v[8:9]\n" \
"v_pk_fma_f32 v[138:139], v[138:139], v[2:3], v[10:11]\n" \
"v_pk_fma_f32 v[140:141], v[140:141], v[4:5], v[12:13]\n" \
"v_pk_fma_f32 v[142:143], v[142:143], v[6:7], v[14:15]\n" \
"v_cvt_pk_bf16_f32 v136, v136, v137\n" \
"v_cvt_pk_bf16_f32 v137, v138, v139\n" \
"v_cvt_pk_bf16_f32 v138, v140, v141\n" \
"v_cvt_pk_bf16_f32 v139, v142, v143\n" \
"ds_write_b128 v49, v[136:139]\n" \
"v_xor_b32 v48, 160, v232\n" \
"v_pk_fma_f32 v[144:145], v[144:145], v[0:1], v[8:9]\n" \
"v_pk_fma_f32 v[146:147], v[146:147], v[2:3], v[10:11]\n" \
"v_pk_fma_f32 v[148:149], v[148:149], v[4:5], v[12:13]\n" \
"v_pk_fma_f32 v[150:151], v[150:151], v[6:7], v[14:15]\n" \
"v_cvt_pk_bf16_f32 v144, v144, v145\n" \
"v_cvt_pk_bf16_f32 v145, v146, v147\n" \
"v_cvt_pk_bf16_f32 v146, v148, v149\n" \
"v_cvt_pk_bf16_f32 v147, v150, v151\n" \
"ds_write_b128 v48, v[144:147]\n" \
"v_xor_b32 v49, 176, v232\n" \
"v_pk_fma_f32 v[152:153], v[152:153], v[0:1], v[8:9]\n" \
"v_pk_fma_f32 v[154:155], v[154:155], v[2:3], v[10:11]\n" \
"v_pk_fma_f32 v[156:157], v[156:157], v[4:5], v[12:13]\n" \
"v_pk_fma_f32 v[158:159], v[158:159], v[6:7], v[14:15]\n" \
"v_cvt_pk_bf16_f32 v152, v152, v153\n" \
"v_cvt_pk_bf16_f32 v153, v154, v155\n" \
"v_cvt_pk_bf16_f32 v154, v156, v157\n" \
"v_cvt_pk_bf16_f32 v155, v158, v159\n" \
"ds_write_b128 v49, v[152:155]\n" \
"v_xor_b32 v48, 192, v232\n" \
"v_pk_fma_f32 v[160:161], v[160:161], v[0:1], v[8:9]\n" \
"v_pk_fma_f32 v[162:163], v[162:163], v[2:3], v[10:11]\n" \
"v_pk_fma_f32 v[164:165], v[164:165], v[4:5], v[12:13]\n" \
"v_pk_fma_f32 v[166:167], v[166:167], v[6:7], v[14:15]\n" \
"v_cvt_pk_bf16_f32 v160, v160, v161\n" \
"v_cvt_pk_bf16_f32 v161, v162, v163\n" \
"v_cvt_pk_bf16_f32 v162, v164, v165\n" \
"v_cvt_pk_bf16_f32 v163, v166, v167\n" \
"ds_write_b128 v48, v[160:163]\n" \
"v_xor_b32 v49, 208, v232\n" \
"v_pk_fma_f32 v[168:169], v[168:169], v[0:1], v[8:9]\n" \
"v_pk_fma_f32 v[170:171], v[170:171], v[2:3], v[10:11]\n" \
"v_pk_fma_f32 v[172:173], v[172:173], v[4:5], v[12:13]\n" \
"v_pk_fma_f32 v[174:175], v[174:175], v[6:7], v[14:15]\n" \
"v_cvt_pk_bf16_f32 v168, v168, v169\n" \
"v_cvt_pk_bf16_f32 v169, v170, v171\n" \
"v_cvt_pk_bf16_f32 v170, v172, v173\n" \
"v_cvt_pk_bf16_f32 v171, v174, v175\n" \
"ds_write_b128 v49, v[168:171]\n" \
"v_xor_b32 v48, 224, v232\n" \
"v_pk_fma_f32 v[176:177], v[176:177], v[0:1], v[8:9]\n" \
"v_pk_fma_f32 v[178:179], v[178:179], v[2:3], v[10:11]\n" \
"v_pk_fma_f32 v[180:181], v[180:181], v[4:5], v[12:13]\n" \
"v_pk_fma_f32 v[182:183], v[182:183], v[6:7], v[14:15]\n" \
"v_cvt_pk_bf16_f32 v176, v176, v177\n" \
"v_cvt_pk_bf16_f32 v177, v178, v179\n" \
"v_cvt_pk_bf16_f32 v178, v180, v181\n" \
"v_cvt_pk_bf16_f32 v179, v182, v183\n" \
"ds_write_b128 v48, v[176:179]\n" \
"v_xor_b32 v49, 240, v232\n" \
"v_pk_fma_f32 v[184:185], v[184:185], v[0:1], v[8:9]\n" \
"v_pk_fma_f32 v[186:187], v[186:187], v[2:3], v[10:11]\n" \
"v_pk_fma_f32 v[188:189], v[188:189], v[4:5], v[12:13]\n" \
"v_pk_fma_f32 v[190:191], v[190:191], v[6:7], v[14:15]\n" \
"v_cvt_pk_bf16_f32 v184, v184, v185\n" \
"v_cvt_pk_bf16_f32 v185, v186, v187\n" \
"v_cvt_pk_bf16_f32 v186, v188, v189\n" \
"v_cvt_pk_bf16_f32 v187, v190, v191\n" \
"ds_write_b128 v49, v[184:187]\n" \
"s_waitcnt lgkmcnt(0)\n" \
"v_accvgpr_read_b32 v185, a4\n" \
"v_accvgpr_read_b32 v187, a5\n" \
"v_accvgpr_read_b32 v188, a0\n" \
"v_accvgpr_read_b32 v189, a1\n" \
"v_accvgpr_read_b32 v190, a2\n" \
"v_accvgpr_read_b32 v191, a3\n" \
"v_accvgpr_read_b32 v184, a12\n" \
"s_nop 1\n" \
"buffer_load_dword v186, v184, s[40:43], 0 offen\n" \
"buffer_load_dword v186, v184, s[44:47], 0 offen\n" \
"s_waitcnt vmcnt(18) lgkmcnt(0)\n" \
"s_barrier\n" \
"ds_read_b128 v[64:67], v228 offset:0\n" \
"ds_read_b128 v[68:71], v228 offset:1024\n" \
"ds_read_b128 v[72:75], v229 offset:4096\n" \
"ds_read_b128 v[76:79], v229 offset:5120\n" \
"ds_read_b128 v[80:83], v230 offset:8192\n" \
"ds_read_b128 v[84:87], v230 offset:9216\n" \
"ds_read_b128 v[88:91], v231 offset:12288\n" \
"ds_read_b128 v[92:95], v231 offset:13312\n" \
"ds_read_b64_tr_b16 a[192:193], v212 offset:0\n" \
"ds_read_b64_tr_b16 a[194:195], v212 offset:512\n" \
"ds_read_b64_tr_b16 a[196:197], v212 offset:4096\n" \
"ds_read_b64_tr_b16 a[198:199], v212 offset:4608\n" \
"ds_read_b64_tr_b16 a[200:201], v212 offset:16384\n" \
"ds_read_b64_tr_b16 a[202:203], v212 offset:16896\n" \
"ds_read_b64_tr_b16 a[204:205], v212 offset:20480\n" \
"ds_read_b64_tr_b16 a[206:207], v212 offset:20992\n" \
"ds_read_b64_tr_b16 a[208:209], v213 offset:0\n" \
"ds_read_b64_tr_b16 a[210:211], v213 offset:512\n" \
"ds_read_b64_tr_b16 a[212:213], v213 offset:4096\n" \
"ds_read_b64_tr_b16 a[214:215], v213 offset:4608\n" \
"ds_read_b64_tr_b16 a[216:217], v213 offset:16384\n" \
"ds_read_b64_tr_b16 a[218:219], v213 offset:16896\n" \
"ds_read_b64_tr_b16 a[220:221], v213 offset:20480\n" \
"ds_read_b64_tr_b16 a[222:223], v213 offset:20992\n" \
"s_waitcnt lgkmcnt(14)\n" \
"v_mfma_f32_16x16x32_bf16 v[0:3], a[192:195], v[64:67], 0\n" \
"s_waitcnt lgkmcnt(12)\n" \
"ds_read_b64_tr_b16 a[224:225], v214 offset:0\n" \
"ds_read_b64_tr_b16 a[226:227], v214 offset:512\n" \
"v_mfma_f32_16x16x32_bf16 v[0:3], a[196:199], v[72:75], v[0:3]\n" \
"s_waitcnt lgkmcnt(12)\n" \
"ds_read_b128 v[96:99], v228 offset:16384\n" \
"ds_read_b128 v[100:103], v228 offset:17408\n" \
"v_mfma_f32_16x16x32_bf16 v[0:3], a[200:203], v[80:83], v[0:3]\n" \
"s_waitcnt lgkmcnt(12)\n" \
"ds_read_b64_tr_b16 a[228:229], v214 offset:4096\n" \
"ds_read_b64_tr_b16 a[230:231], v214 offset:4608\n" \
"v_mfma_f32_16x16x32_bf16 v[0:3], a[204:207], v[88:91], v[0:3]\n" \
"ds_read_b128 v[104:107], v229 offset:20480\n" \
"ds_read_b128 v[108:111], v229 offset:21504\n" \
"v_mfma_f32_16x16x32_bf16 v[4:7], a[192:195], v[68:71], 0\n" \
"ds_read_b64_tr_b16 a[232:233], v214 offset:16384\n" \
"ds_read_b64_tr_b16 a[234:235], v214 offset:16896\n" \
"v_mfma_f32_16x16x32_bf16 v[4:7], a[196:199], v[76:79], v[4:7]\n" \
"ds_read_b128 v[112:115], v230 offset:24576\n" \
"ds_read_b128 v[116:119], v230 offset:25600\n" \
"v_mfma_f32_16x16x32_bf16 v[4:7], a[200:203], v[84:87], v[4:7]\n" \
"ds_read_b64_tr_b16 a[236:237], v214 offset:20480\n" \
"ds_read_b64_tr_b16 a[238:239], v214 offset:20992\n" \
"v_mfma_f32_16x16x32_bf16 v[4:7], a[204:207], v[92:95], v[4:7]\n" \
"s_waitcnt lgkmcnt(12)\n" \
"ds_read_b128 v[120:123], v231 offset:28672\n" \
"ds_read_b128 v[124:127], v231 offset:29696\n" \
"v_mfma_f32_16x16x32_bf16 v[8:11], a[208:211], v[64:67], 0\n" \
"ds_read_b64_tr_b16 a[240:241], v215 offset:0\n" \
"ds_read_b64_tr_b16 a[242:243], v215 offset:512\n" \
"v_mfma_f32_16x16x32_bf16 v[8:11], a[212:215], v[72:75], v[8:11]\n" \
"ds_read_b128 v[128:131], v228 offset:32768\n" \
"ds_read_b128 v[132:135], v228 offset:33792\n" \
"v_mfma_f32_16x16x32_bf16 v[8:11], a[216:219], v[80:83], v[8:11]\n" \
"ds_read_b64_tr_b16 a[244:245], v215 offset:4096\n" \
"ds_read_b64_tr_b16 a[246:247], v215 offset:4608\n" \
"v_mfma_f32_16x16x32_bf16 v[8:11], a[220:223], v[88:91], v[8:11]\n" \
"ds_read_b128 v[136:139], v229 offset:36864\n" \
"ds_read_b128 v[140:143], v229 offset:37888\n" \
"v_mfma_f32_16x16x32_bf16 v[12:15], a[208:211], v[68:71], 0\n" \
"ds_read_b64_tr_b16 a[248:249], v215 offset:16384\n" \
"ds_read_b64_tr_b16 a[250:251], v215 offset:16896\n" \
"v_mfma_f32_16x16x32_bf16 v[12:15], a[212:215], v[76:79], v[12:15]\n" \
"s_waitcnt lgkmcnt(10)\n" \
"ds_read_b128 v[144:147], v230 offset:40960\n" \
"ds_read_b128 v[148:151], v230 offset:41984\n" \
"v_mfma_f32_16x16x32_bf16 v[12:15], a[216:219], v[84:87], v[12:15]\n" \
"s_waitcnt vmcnt(10)\n" \
"ds_read_b64_tr_b16 a[252:253], v215 offset:20480\n" \
"ds_read_b64_tr_b16 a[254:255], v215 offset:20992\n" \
"v_mfma_f32_16x16x32_bf16 v[12:15], a[220:223], v[92:95], v[12:15]\n" \
"s_barrier\n" \
"ds_read_b128 v[152:155], v231 offset:45056\n" \
"ds_read_b128 v[156:159], v231 offset:46080\n" \
"v_mfma_f32_16x16x32_bf16 v[16:19], a[224:227], v[64:67], 0\n" \
"ds_read_b64_tr_b16 a[192:193], v212 offset:32768\n" \
"ds_read_b64_tr_b16 a[194:195], v212 offset:33280\n" \
"v_mfma_f32_16x16x32_bf16 v[16:19], a[228:231], v[72:75], v[16:19]\n" \
"s_mov_b32 s68, 3145728\n" \
"ds_read_b128 v[160:163], v228 offset:49152\n" \
"ds_read_b128 v[164:167], v228 offset:50176\n" \
"v_mfma_f32_16x16x32_bf16 v[16:19], a[232:235], v[80:83], v[16:19]\n" \
"s_add_u32 m0, s50, 98304\n" \
"ds_read_b64_tr_b16 a[196:197], v212 offset:36864\n" \
"ds_read_b64_tr_b16 a[198:199], v212 offset:37376\n" \
"v_mfma_f32_16x16x32_bf16 v[16:19], a[236:239], v[88:91], v[16:19]\n" \
"buffer_load_dwordx4 v224, s[56:59], s68 offen offset:0 lds\n" \
"v_mfma_f32_16x16x32_bf16 v[20:23], a[224:227], v[68:71], 0\n" \
"ds_read_b64_tr_b16 a[200:201], v216 offset:0\n" \
"ds_read_b64_tr_b16 a[202:203], v216 offset:512\n" \
"v_mfma_f32_16x16x32_bf16 v[20:23], a[228:231], v[76:79], v[20:23]\n" \
"ds_read_b128 v[168:171], v229 offset:53248\n" \
"ds_read_b128 v[172:175], v229 offset:54272\n" \
"v_mfma_f32_16x16x32_bf16 v[20:23], a[232:235], v[84:87], v[20:23]\n" \
"ds_read_b64_tr_b16 a[204:205], v216 offset:4096\n" \
"ds_read_b64_tr_b16 a[206:207], v216 offset:4608\n" \
"v_mfma_f32_16x16x32_bf16 v[20:23], a[236:239], v[92:95], v[20:23]\n" \
"s_waitcnt lgkmcnt(10)\n" \
"buffer_load_dwordx4 v225, s[56:59], s68 offen offset:1024 lds\n" \
"v_mfma_f32_16x16x32_bf16 v[24:27], a[240:243], v[64:67], 0\n" \
"ds_read_b64_tr_b16 a[208:209], v213 offset:32768\n" \
"ds_read_b64_tr_b16 a[210:211], v213 offset:33280\n" \
"v_mfma_f32_16x16x32_bf16 v[24:27], a[244:247], v[72:75], v[24:27]\n" \
"v_mfma_f32_16x16x32_bf16 v[24:27], a[248:251], v[80:83], v[24:27]\n" \
"ds_read_b64_tr_b16 a[212:213], v213 offset:36864\n" \
"ds_read_b64_tr_b16 a[214:215], v213 offset:37376\n" \
"v_mfma_f32_16x16x32_bf16 v[24:27], a[252:255], v[88:91], v[24:27]\n" \
"buffer_load_dwordx4 v226, s[56:59], s68 offen offset:2048 lds\n" \
"v_mfma_f32_16x16x32_bf16 v[28:31], a[240:243], v[68:71], 0\n" \
"ds_read_b64_tr_b16 a[216:217], v217 offset:0\n" \
"ds_read_b64_tr_b16 a[218:219], v217 offset:512\n" \
"v_mfma_f32_16x16x32_bf16 v[28:31], a[244:247], v[76:79], v[28:31]\n" \
"v_mfma_f32_16x16x32_bf16 v[28:31], a[248:251], v[84:87], v[28:31]\n" \
"ds_read_b64_tr_b16 a[220:221], v217 offset:4096\n" \
"ds_read_b64_tr_b16 a[222:223], v217 offset:4608\n" \
"v_mfma_f32_16x16x32_bf16 v[28:31], a[252:255], v[92:95], v[28:31]\n" \
"buffer_load_dwordx4 v227, s[56:59], s68 offen offset:3072 lds\n" \
"v_mfma_f32_16x16x32_bf16 v[0:3], a[192:195], v[96:99], v[0:3]\n" \
"s_waitcnt lgkmcnt(6)\n" \
"ds_read_b64_tr_b16 a[224:225], v214 offset:32768\n" \
"ds_read_b64_tr_b16 a[226:227], v214 offset:33280\n" \
"v_mfma_f32_16x16x32_bf16 v[0:3], a[196:199], v[104:107], v[0:3]\n" \
"s_mov_b32 s69, 128\n" \
"v_mfma_f32_16x16x32_bf16 v[0:3], a[200:203], v[112:115], v[0:3]\n" \
"s_add_u32 m0, s50, 114688\n" \
"ds_read_b64_tr_b16 a[228:229], v214 offset:36864\n" \
"ds_read_b64_tr_b16 a[230:231], v214 offset:37376\n" \
"v_mfma_f32_16x16x32_bf16 v[0:3], a[204:207], v[120:123], v[0:3]\n" \
"buffer_load_dwordx4 v224, s[56:59], s69 offen offset:0 lds\n" \
"v_mfma_f32_16x16x32_bf16 v[4:7], a[192:195], v[100:103], v[4:7]\n" \
"ds_read_b64_tr_b16 a[232:233], v218 offset:0\n" \
"ds_read_b64_tr_b16 a[234:235], v218 offset:512\n" \
"v_mfma_f32_16x16x32_bf16 v[4:7], a[196:199], v[108:111], v[4:7]\n" \
"v_mfma_f32_16x16x32_bf16 v[4:7], a[200:203], v[116:119], v[4:7]\n" \
"ds_read_b64_tr_b16 a[236:237], v218 offset:4096\n" \
"ds_read_b64_tr_b16 a[238:239], v218 offset:4608\n" \
"v_mfma_f32_16x16x32_bf16 v[4:7], a[204:207], v[124:127], v[4:7]\n" \
"buffer_load_dwordx4 v225, s[56:59], s69 offen offset:1024 lds\n" \
"v_mfma_f32_16x16x32_bf16 v[8:11], a[208:211], v[96:99], v[8:11]\n" \
"s_waitcnt lgkmcnt(6)\n" \
"ds_read_b64_tr_b16 a[240:241], v215 offset:32768\n" \
"ds_read_b64_tr_b16 a[242:243], v215 offset:33280\n" \
"v_mfma_f32_16x16x32_bf16 v[8:11], a[212:215], v[104:107], v[8:11]\n" \
"v_mfma_f32_16x16x32_bf16 v[8:11], a[216:219], v[112:115], v[8:11]\n" \
"ds_read_b64_tr_b16 a[244:245], v215 offset:36864\n" \
"ds_read_b64_tr_b16 a[246:247], v215 offset:37376\n" \
"v_mfma_f32_16x16x32_bf16 v[8:11], a[220:223], v[120:123], v[8:11]\n" \
"buffer_load_dwordx4 v226, s[56:59], s69 offen offset:2048 lds\n" \
"v_mfma_f32_16x16x32_bf16 v[12:15], a[208:211], v[100:103], v[12:15]\n" \
"ds_read_b64_tr_b16 a[248:249], v219 offset:0\n" \
"ds_read_b64_tr_b16 a[250:251], v219 offset:512\n" \
"v_mfma_f32_16x16x32_bf16 v[12:15], a[212:215], v[108:111], v[12:15]\n" \
"v_mfma_f32_16x16x32_bf16 v[12:15], a[216:219], v[116:119], v[12:15]\n" \
"s_waitcnt vmcnt(9)\n" \
"ds_read_b64_tr_b16 a[252:253], v219 offset:4096\n" \
"ds_read_b64_tr_b16 a[254:255], v219 offset:4608\n" \
"v_mfma_f32_16x16x32_bf16 v[12:15], a[220:223], v[124:127], v[12:15]\n" \
"s_barrier\n" \
"buffer_load_dwordx4 v227, s[56:59], s69 offen offset:3072 lds\n" \
"v_mfma_f32_16x16x32_bf16 v[16:19], a[224:227], v[96:99], v[16:19]\n" \
"s_waitcnt lgkmcnt(6)\n" \
"ds_read_b64_tr_b16 a[192:193], v212 offset:49152\n" \
"ds_read_b64_tr_b16 a[194:195], v212 offset:49664\n" \
"v_mfma_f32_16x16x32_bf16 v[16:19], a[228:231], v[104:107], v[16:19]\n" \
"s_mov_b32 s68, 524416\n" \
"v_mfma_f32_16x16x32_bf16 v[16:19], a[232:235], v[112:115], v[16:19]\n" \
"s_add_u32 m0, s50, 131072\n" \
"ds_read_b64_tr_b16 a[196:197], v212 offset:53248\n" \
"ds_read_b64_tr_b16 a[198:199], v212 offset:53760\n" \
"v_mfma_f32_16x16x32_bf16 v[16:19], a[236:239], v[120:123], v[16:19]\n" \
"buffer_load_dwordx4 v224, s[56:59], s68 offen offset:0 lds\n" \
"v_mfma_f32_16x16x32_bf16 v[20:23], a[224:227], v[100:103], v[20:23]\n" \
"ds_read_b64_tr_b16 a[200:201], v216 offset:16384\n" \
"ds_read_b64_tr_b16 a[202:203], v216 offset:16896\n" \
"v_mfma_f32_16x16x32_bf16 v[20:23], a[228:231], v[108:111], v[20:23]\n" \
"buffer_load_dwordx4 v225, s[56:59], s68 offen offset:1024 lds\n" \
"v_mfma_f32_16x16x32_bf16 v[20:23], a[232:235], v[116:119], v[20:23]\n" \
"ds_read_b64_tr_b16 a[204:205], v216 offset:20480\n" \
"ds_read_b64_tr_b16 a[206:207], v216 offset:20992\n" \
"v_mfma_f32_16x16x32_bf16 v[20:23], a[236:239], v[124:127], v[20:23]\n" \
"buffer_load_dwordx4 v226, s[56:59], s68 offen offset:2048 lds\n" \
"v_mfma_f32_16x16x32_bf16 v[24:27], a[240:243], v[96:99], v[24:27]\n" \
"s_waitcnt lgkmcnt(6)\n" \
"ds_read_b64_tr_b16 a[208:209], v213 offset:49152\n" \
"ds_read_b64_tr_b16 a[210:211], v213 offset:49664\n" \
"v_mfma_f32_16x16x32_bf16 v[24:27], a[244:247], v[104:107], v[24:27]\n" \
"s_mov_b32 s69, 1048704\n" \
"buffer_load_dwordx4 v227, s[56:59], s68 offen offset:3072 lds\n" \
"v_mfma_f32_16x16x32_bf16 v[24:27], a[248:251], v[112:115], v[24:27]\n" \
"s_add_u32 m0, s50, 147456\n" \
"ds_read_b64_tr_b16 a[212:213], v213 offset:53248\n" \
"ds_read_b64_tr_b16 a[214:215], v213 offset:53760\n" \
"v_mfma_f32_16x16x32_bf16 v[24:27], a[252:255], v[120:123], v[24:27]\n" \
"buffer_load_dwordx4 v224, s[56:59], s69 offen offset:0 lds\n" \
"v_mfma_f32_16x16x32_bf16 v[28:31], a[240:243], v[100:103], v[28:31]\n" \
"ds_read_b64_tr_b16 a[216:217], v217 offset:16384\n" \
"ds_read_b64_tr_b16 a[218:219], v217 offset:16896\n" \
"v_mfma_f32_16x16x32_bf16 v[28:31], a[244:247], v[108:111], v[28:31]\n" \
"buffer_load_dwordx4 v225, s[56:59], s69 offen offset:1024 lds\n" \
"v_mfma_f32_16x16x32_bf16 v[28:31], a[248:251], v[116:119], v[28:31]\n" \
"ds_read_b64_tr_b16 a[220:221], v217 offset:20480\n" \
"ds_read_b64_tr_b16 a[222:223], v217 offset:20992\n" \
"v_mfma_f32_16x16x32_bf16 v[28:31], a[252:255], v[124:127], v[28:31]\n" \
"buffer_load_dwordx4 v226, s[56:59], s69 offen offset:2048 lds\n" \
"v_mfma_f32_16x16x32_bf16 v[0:3], a[192:195], v[128:131], v[0:3]\n" \
"s_waitcnt lgkmcnt(6)\n" \
"ds_read_b64_tr_b16 a[224:225], v214 offset:49152\n" \
"ds_read_b64_tr_b16 a[226:227], v214 offset:49664\n" \
"v_mfma_f32_16x16x32_bf16 v[0:3], a[196:199], v[136:139], v[0:3]\n" \
"s_mov_b32 s68, 1572992\n" \
"buffer_load_dwordx4 v227, s[56:59], s69 offen offset:3072 lds\n" \
"v_mfma_f32_16x16x32_bf16 v[0:3], a[200:203], v[144:147], v[0:3]\n" \
"s_add_u32 m0, s50, 0\n" \
"ds_read_b64_tr_b16 a[228:229], v214 offset:53248\n" \
"ds_read_b64_tr_b16 a[230:231], v214 offset:53760\n" \
"v_mfma_f32_16x16x32_bf16 v[0:3], a[204:207], v[152:155], v[0:3]\n" \
"buffer_load_dwordx4 v224, s[56:59], s68 offen offset:0 lds\n" \
"v_mfma_f32_16x16x32_bf16 v[4:7], a[192:195], v[132:135], v[4:7]\n" \
"ds_read_b64_tr_b16 a[232:233], v218 offset:16384\n" \
"ds_read_b64_tr_b16 a[234:235], v218 offset:16896\n" \
"v_mfma_f32_16x16x32_bf16 v[4:7], a[196:199], v[140:143], v[4:7]\n" \
"buffer_load_dwordx4 v225, s[56:59], s68 offen offset:1024 lds\n" \
"v_mfma_f32_16x16x32_bf16 v[4:7], a[200:203], v[148:151], v[4:7]\n" \
"ds_read_b64_tr_b16 a[236:237], v218 offset:20480\n" \
"ds_read_b64_tr_b16 a[238:239], v218 offset:20992\n" \
"v_mfma_f32_16x16x32_bf16 v[4:7], a[204:207], v[156:159], v[4:7]\n" \
"buffer_load_dwordx4 v226, s[56:59], s68 offen offset:2048 lds\n" \
"v_mfma_f32_16x16x32_bf16 v[8:11], a[208:211], v[128:131], v[8:11]\n" \
"s_waitcnt lgkmcnt(6)\n" \
"ds_read_b64_tr_b16 a[240:241], v215 offset:49152\n" \
"ds_read_b64_tr_b16 a[242:243], v215 offset:49664\n" \
"v_mfma_f32_16x16x32_bf16 v[8:11], a[212:215], v[136:139], v[8:11]\n" \
"s_mov_b32 s69, 2097280\n" \
"buffer_load_dwordx4 v227, s[56:59], s68 offen offset:3072 lds\n" \
"v_mfma_f32_16x16x32_bf16 v[8:11], a[216:219], v[144:147], v[8:11]\n" \
"s_add_u32 m0, s50, 16384\n" \
"ds_read_b64_tr_b16 a[244:245], v215 offset:53248\n" \
"ds_read_b64_tr_b16 a[246:247], v215 offset:53760\n" \
"v_mfma_f32_16x16x32_bf16 v[8:11], a[220:223], v[152:155], v[8:11]\n" \
"buffer_load_dwordx4 v224, s[56:59], s69 offen offset:0 lds\n" \
"v_mfma_f32_16x16x32_bf16 v[12:15], a[208:211], v[132:135], v[12:15]\n" \
"ds_read_b64_tr_b16 a[248:249], v219 offset:16384\n" \
"ds_read_b64_tr_b16 a[250:251], v219 offset:16896\n" \
"v_mfma_f32_16x16x32_bf16 v[12:15], a[212:215], v[140:143], v[12:15]\n" \
"buffer_load_dwordx4 v225, s[56:59], s69 offen offset:1024 lds\n" \
"v_mfma_f32_16x16x32_bf16 v[12:15], a[216:219], v[148:151], v[12:15]\n" \
"s_waitcnt vmcnt(14)\n" \
"ds_read_b64_tr_b16 a[252:253], v219 offset:20480\n" \
"ds_read_b64_tr_b16 a[254:255], v219 offset:20992\n" \
"v_mfma_f32_16x16x32_bf16 v[12:15], a[220:223], v[156:159], v[12:15]\n" \
"s_barrier\n" \
"buffer_load_dwordx4 v226, s[56:59], s69 offen offset:2048 lds\n" \
"v_mfma_f32_16x16x32_bf16 v[16:19], a[224:227], v[128:131], v[16:19]\n" \
"s_waitcnt lgkmcnt(6)\n" \
"ds_read_b64_tr_b16 a[192:193], v216 offset:32768\n" \
"ds_read_b64_tr_b16 a[194:195], v216 offset:33280\n" \
"v_mfma_f32_16x16x32_bf16 v[16:19], a[228:231], v[136:139], v[16:19]\n" \
"s_mov_b32 s68, 2621568\n" \
"buffer_load_dwordx4 v227, s[56:59], s69 offen offset:3072 lds\n" \
"v_mfma_f32_16x16x32_bf16 v[16:19], a[232:235], v[144:147], v[16:19]\n" \
"s_add_u32 m0, s50, 32768\n" \
"ds_read_b64_tr_b16 a[196:197], v216 offset:36864\n" \
"ds_read_b64_tr_b16 a[198:199], v216 offset:37376\n" \
"v_mfma_f32_16x16x32_bf16 v[16:19], a[236:239], v[152:155], v[16:19]\n" \
"buffer_load_dwordx4 v224, s[56:59], s68 offen offset:0 lds\n" \
"v_mfma_f32_16x16x32_bf16 v[20:23], a[224:227], v[132:135], v[20:23]\n" \
"ds_read_b64_tr_b16 a[200:201], v217 offset:32768\n" \
"ds_read_b64_tr_b16 a[202:203], v217 offset:33280\n" \
"v_mfma_f32_16x16x32_bf16 v[20:23], a[228:231], v[140:143], v[20:23]\n" \
"v_mfma_f32_16x16x32_bf16 v[20:23], a[232:235], v[148:151], v[20:23]\n" \
"ds_read_b64_tr_b16 a[204:205], v217 offset:36864\n" \
"ds_read_b64_tr_b16 a[206:207], v217 offset:37376\n" \
"v_mfma_f32_16x16x32_bf16 v[20:23], a[236:239], v[156:159], v[20:23]\n" \
"buffer_load_dwordx4 v225, s[56:59], s68 offen offset:1024 lds\n" \
"v_mfma_f32_16x16x32_bf16 v[24:27], a[240:243], v[128:131], v[24:27]\n" \
"s_waitcnt lgkmcnt(6)\n" \
"ds_read_b64_tr_b16 a[208:209], v218 offset:32768\n" \
"ds_read_b64_tr_b16 a[210:211], v218 offset:33280\n" \
"v_mfma_f32_16x16x32_bf16 v[24:27], a[244:247], v[136:139], v[24:27]\n" \
"v_mfma_f32_16x16x32_bf16 v[24:27], a[248:251], v[144:147], v[24:27]\n" \
"ds_read_b64_tr_b16 a[212:213], v218 offset:36864\n" \
"ds_read_b64_tr_b16 a[214:215], v218 offset:37376\n" \
"v_mfma_f32_16x16x32_bf16 v[24:27], a[252:255], v[152:155], v[24:27]\n" \
"buffer_load_dwordx4 v226, s[56:59], s68 offen offset:2048 lds\n" \
"v_mfma_f32_16x16x32_bf16 v[28:31], a[240:243], v[132:135], v[28:31]\n" \
"ds_read_b64_tr_b16 a[216:217], v219 offset:32768\n" \
"ds_read_b64_tr_b16 a[218:219], v219 offset:33280\n" \
"v_mfma_f32_16x16x32_bf16 v[28:31], a[244:247], v[140:143], v[28:31]\n" \
"v_mfma_f32_16x16x32_bf16 v[28:31], a[248:251], v[148:151], v[28:31]\n" \
"ds_read_b64_tr_b16 a[220:221], v219 offset:36864\n" \
"ds_read_b64_tr_b16 a[222:223], v219 offset:37376\n" \
"v_mfma_f32_16x16x32_bf16 v[28:31], a[252:255], v[156:159], v[28:31]\n" \
"buffer_load_dwordx4 v227, s[56:59], s68 offen offset:3072 lds\n" \
"v_mfma_f32_16x16x32_bf16 v[0:3], a[192:195], v[160:163], v[0:3]\n" \
"s_waitcnt lgkmcnt(6)\n" \
"ds_read_b64_tr_b16 a[224:225], v216 offset:49152\n" \
"ds_read_b64_tr_b16 a[226:227], v216 offset:49664\n" \
"v_mfma_f32_16x16x32_bf16 v[0:3], a[196:199], v[168:171], v[0:3]\n" \
"s_mov_b32 s69, 3145856\n" \
"v_mfma_f32_16x16x32_bf16 v[4:7], a[192:195], v[164:167], v[4:7]\n" \
"s_add_u32 m0, s50, 65536\n" \
"ds_read_b64_tr_b16 a[228:229], v216 offset:53248\n" \
"ds_read_b64_tr_b16 a[230:231], v216 offset:53760\n" \
"v_mfma_f32_16x16x32_bf16 v[4:7], a[196:199], v[172:175], v[4:7]\n" \
"buffer_load_dwordx4 v224, s[56:59], s69 offen offset:0 lds\n" \
"v_mfma_f32_16x16x32_bf16 v[8:11], a[200:203], v[160:163], v[8:11]\n" \
"ds_read_b64_tr_b16 a[232:233], v217 offset:49152\n" \
"ds_read_b64_tr_b16 a[234:235], v217 offset:49664\n" \
"v_mfma_f32_16x16x32_bf16 v[8:11], a[204:207], v[168:171], v[8:11]\n" \
"v_mfma_f32_16x16x32_bf16 v[12:15], a[200:203], v[164:167], v[12:15]\n" \
"ds_read_b64_tr_b16 a[236:237], v217 offset:53248\n" \
"ds_read_b64_tr_b16 a[238:239], v217 offset:53760\n" \
"v_mfma_f32_16x16x32_bf16 v[12:15], a[204:207], v[172:175], v[12:15]\n" \
"buffer_load_dwordx4 v225, s[56:59], s69 offen offset:1024 lds\n" \
"v_mfma_f32_16x16x32_bf16 v[16:19], a[208:211], v[160:163], v[16:19]\n" \
"s_waitcnt lgkmcnt(6)\n" \
"ds_read_b64_tr_b16 a[240:241], v218 offset:49152\n" \
"ds_read_b64_tr_b16 a[242:243], v218 offset:49664\n" \
"v_mfma_f32_16x16x32_bf16 v[16:19], a[212:215], v[168:171], v[16:19]\n" \
"v_mfma_f32_16x16x32_bf16 v[20:23], a[208:211], v[164:167], v[20:23]\n" \
"ds_read_b64_tr_b16 a[244:245], v218 offset:53248\n" \
"ds_read_b64_tr_b16 a[246:247], v218 offset:53760\n" \
"v_mfma_f32_16x16x32_bf16 v[20:23], a[212:215], v[172:175], v[20:23]\n" \
"buffer_load_dwordx4 v226, s[56:59], s69 offen offset:2048 lds\n" \
"v_mfma_f32_16x16x32_bf16 v[24:27], a[216:219], v[160:163], v[24:27]\n" \
"ds_read_b64_tr_b16 a[248:249], v219 offset:49152\n" \
"ds_read_b64_tr_b16 a[250:251], v219 offset:49664\n" \
"v_mfma_f32_16x16x32_bf16 v[24:27], a[220:223], v[168:171], v[24:27]\n" \
"v_cvt_pk_bf16_f32 v176, v0, v1\n" \
"v_cvt_pk_bf16_f32 v177, v2, v3\n" \
"v_mfma_f32_16x16x32_bf16 v[28:31], a[216:219], v[164:167], v[28:31]\n" \
"s_waitcnt vmcnt(15)\n" \
"ds_read_b64_tr_b16 a[252:253], v219 offset:53248\n" \
"ds_read_b64_tr_b16 a[254:255], v219 offset:53760\n" \
"v_mfma_f32_16x16x32_bf16 v[28:31], a[220:223], v[172:175], v[28:31]\n" \
"s_barrier\n" \
"buffer_load_dwordx4 v227, s[56:59], s69 offen offset:3072 lds\n" \
"v_mfma_f32_16x16x32_bf16 v[32:35], a[224:227], v[64:67], 0\n" \
"s_waitcnt lgkmcnt(6)\n" \
"ds_read_b64_tr_b16 a[192:193], v220 offset:0\n" \
"ds_read_b64_tr_b16 a[194:195], v220 offset:512\n" \
"v_mfma_f32_16x16x32_bf16 v[32:35], a[228:231], v[72:75], v[32:35]\n" \
"s_mov_b32 s68, 256\n" \
"v_cvt_pk_bf16_f32 v178, v8, v9\n" \
"v_cvt_pk_bf16_f32 v179, v10, v11\n" \
"v_mfma_f32_16x16x32_bf16 v[36:39], a[224:227], v[68:71], 0\n" \
"s_add_u32 m0, s50, 49152\n" \
"ds_read_b64_tr_b16 a[196:197], v220 offset:4096\n" \
"ds_read_b64_tr_b16 a[198:199], v220 offset:4608\n" \
"v_mfma_f32_16x16x32_bf16 v[36:39], a[228:231], v[76:79], v[36:39]\n" \
"buffer_load_dwordx4 v224, s[56:59], s68 offen offset:0 lds\n" \
"v_mfma_f32_16x16x32_bf16 v[40:43], a[232:235], v[64:67], 0\n" \
"ds_read_b64_tr_b16 a[200:201], v220 offset:16384\n" \
"ds_read_b64_tr_b16 a[202:203], v220 offset:16896\n" \
"v_mfma_f32_16x16x32_bf16 v[40:43], a[236:239], v[72:75], v[40:43]\n" \
"v_cvt_pk_bf16_f32 v180, v4, v5\n" \
"v_cvt_pk_bf16_f32 v181, v6, v7\n" \
"v_mfma_f32_16x16x32_bf16 v[44:47], a[232:235], v[68:71], 0\n" \
"ds_read_b64_tr_b16 a[204:205], v220 offset:20480\n" \
"ds_read_b64_tr_b16 a[206:207], v220 offset:20992\n" \
"v_mfma_f32_16x16x32_bf16 v[44:47], a[236:239], v[76:79], v[44:47]\n" \
"buffer_load_dwordx4 v225, s[56:59], s68 offen offset:1024 lds\n" \
"v_mfma_f32_16x16x32_bf16 v[48:51], a[240:243], v[64:67], 0\n" \
"s_waitcnt lgkmcnt(6)\n" \
"ds_read_b64_tr_b16 a[208:209], v221 offset:0\n" \
"ds_read_b64_tr_b16 a[210:211], v221 offset:512\n" \
"v_mfma_f32_16x16x32_bf16 v[48:51], a[244:247], v[72:75], v[48:51]\n" \
"v_cvt_pk_bf16_f32 v182, v12, v13\n" \
"v_cvt_pk_bf16_f32 v183, v14, v15\n" \
"v_mfma_f32_16x16x32_bf16 v[52:55], a[240:243], v[68:71], 0\n" \
"ds_read_b64_tr_b16 a[212:213], v221 offset:4096\n" \
"ds_read_b64_tr_b16 a[214:215], v221 offset:4608\n" \
"v_mfma_f32_16x16x32_bf16 v[52:55], a[244:247], v[76:79], v[52:55]\n" \
"buffer_load_dwordx4 v226, s[56:59], s68 offen offset:2048 lds\n" \
"v_mfma_f32_16x16x32_bf16 v[56:59], a[248:251], v[64:67], 0\n" \
"ds_read_b64_tr_b16 a[216:217], v221 offset:16384\n" \
"ds_read_b64_tr_b16 a[218:219], v221 offset:16896\n" \
"v_mfma_f32_16x16x32_bf16 v[56:59], a[252:255], v[72:75], v[56:59]\n" \
"v_permlane32_swap_b32 v176, v178\n" \
"v_permlane32_swap_b32 v177, v179\n" \
"v_mfma_f32_16x16x32_bf16 v[60:63], a[248:251], v[68:71], 0\n" \
"ds_read_b64_tr_b16 a[220:221], v221 offset:20480\n" \
"ds_read_b64_tr_b16 a[222:223], v221 offset:20992\n" \
"v_mfma_f32_16x16x32_bf16 v[60:63], a[252:255], v[76:79], v[60:63]\n" \
"buffer_load_dwordx4 v227, s[56:59], s68 offen offset:3072 lds\n" \
"v_mfma_f32_16x16x32_bf16 v[32:35], a[192:195], v[80:83], v[32:35]\n" \
"s_waitcnt lgkmcnt(6)\n" \
"ds_read_b64_tr_b16 a[224:225], v222 offset:0\n" \
"ds_read_b64_tr_b16 a[226:227], v222 offset:512\n" \
"v_mfma_f32_16x16x32_bf16 v[32:35], a[196:199], v[88:91], v[32:35]\n" \
"s_mov_b32 s69, 524544\n" \
"v_permlane32_swap_b32 v180, v182\n" \
"v_permlane32_swap_b32 v181, v183\n" \
"v_mfma_f32_16x16x32_bf16 v[32:35], a[200:203], v[96:99], v[32:35]\n" \
"s_add_u32 m0, s50, 81920\n" \
"ds_read_b64_tr_b16 a[228:229], v222 offset:4096\n" \
"ds_read_b64_tr_b16 a[230:231], v222 offset:4608\n" \
"v_mfma_f32_16x16x32_bf16 v[32:35], a[204:207], v[104:107], v[32:35]\n" \
"buffer_load_dwordx4 v224, s[56:59], s69 offen offset:0 lds\n" \
"v_mfma_f32_16x16x32_bf16 v[36:39], a[192:195], v[84:87], v[36:39]\n" \
"ds_read_b64_tr_b16 a[232:233], v222 offset:16384\n" \
"ds_read_b64_tr_b16 a[234:235], v222 offset:16896\n" \
"v_mfma_f32_16x16x32_bf16 v[36:39], a[196:199], v[92:95], v[36:39]\n" \
"v_permlane16_swap_b32 v176, v178\n" \
"v_permlane16_swap_b32 v177, v179\n" \
"v_mfma_f32_16x16x32_bf16 v[36:39], a[200:203], v[100:103], v[36:39]\n" \
"ds_read_b64_tr_b16 a[236:237], v222 offset:20480\n" \
"ds_read_b64_tr_b16 a[238:239], v222 offset:20992\n" \
"v_mfma_f32_16x16x32_bf16 v[36:39], a[204:207], v[108:111], v[36:39]\n" \
"buffer_load_dwordx4 v225, s[56:59], s69 offen offset:1024 lds\n" \
"v_mfma_f32_16x16x32_bf16 v[40:43], a[208:211], v[80:83], v[40:43]\n" \
"s_waitcnt lgkmcnt(6)\n" \
"ds_read_b64_tr_b16 a[240:241], v223 offset:0\n" \
"ds_read_b64_tr_b16 a[242:243], v223 offset:512\n" \
"v_mfma_f32_16x16x32_bf16 v[40:43], a[212:215], v[88:91], v[40:43]\n" \
"v_permlane16_swap_b32 v180, v182\n" \
"v_permlane16_swap_b32 v181, v183\n" \
"v_mfma_f32_16x16x32_bf16 v[40:43], a[216:219], v[96:99], v[40:43]\n" \
"ds_read_b64_tr_b16 a[244:245], v223 offset:4096\n" \
"ds_read_b64_tr_b16 a[246:247], v223 offset:4608\n" \
"v_mfma_f32_16x16x32_bf16 v[40:43], a[220:223], v[104:107], v[40:43]\n" \
"buffer_load_dwordx4 v226, s[56:59], s69 offen offset:2048 lds\n" \
"v_mfma_f32_16x16x32_bf16 v[44:47], a[208:211], v[84:87], v[44:47]\n" \
"ds_read_b64_tr_b16 a[248:249], v223 offset:16384\n" \
"ds_read_b64_tr_b16 a[250:251], v223 offset:16896\n" \
"v_mfma_f32_16x16x32_bf16 v[44:47], a[212:215], v[92:95], v[44:47]\n" \
"v_accvgpr_write_b32 a128, v176\n" \
"v_accvgpr_write_b32 a129, v177\n" \
"v_mfma_f32_16x16x32_bf16 v[44:47], a[216:219], v[100:103], v[44:47]\n" \
"s_waitcnt vmcnt(15)\n" \
"ds_read_b64_tr_b16 a[252:253], v223 offset:20480\n" \
"ds_read_b64_tr_b16 a[254:255], v223 offset:20992\n" \
"v_mfma_f32_16x16x32_bf16 v[44:47], a[220:223], v[108:111], v[44:47]\n" \
"s_barrier\n" \
"buffer_load_dwordx4 v227, s[56:59], s69 offen offset:3072 lds\n" \
"v_mfma_f32_16x16x32_bf16 v[48:51], a[224:227], v[80:83], v[48:51]\n" \
"s_waitcnt lgkmcnt(6)\n" \
"ds_read_b64_tr_b16 a[192:193], v212 offset:0\n" \
"ds_read_b64_tr_b16 a[194:195], v212 offset:512\n" \
"v_mfma_f32_16x16x32_bf16 v[48:51], a[228:231], v[88:91], v[48:51]\n" \
"s_mov_b32 s68, 1048832\n" \
"v_accvgpr_write_b32 a132, v180\n" \
"v_accvgpr_write_b32 a133, v181\n" \
"v_mfma_f32_16x16x32_bf16 v[48:51], a[232:235], v[96:99], v[48:51]\n" \
"s_add_u32 m0, s50, 98304\n" \
"ds_read_b64_tr_b16 a[196:197], v212 offset:4096\n" \
"ds_read_b64_tr_b16 a[198:199], v212 offset:4608\n" \
"v_mfma_f32_16x16x32_bf16 v[48:51], a[236:239], v[104:107], v[48:51]\n" \
"buffer_load_dwordx4 v224, s[56:59], s68 offen offset:0 lds\n" \
"v_mfma_f32_16x16x32_bf16 v[52:55], a[224:227], v[84:87], v[52:55]\n" \
"ds_read_b64_tr_b16 a[200:201], v212 offset:16384\n" \
"ds_read_b64_tr_b16 a[202:203], v212 offset:16896\n" \
"v_mfma_f32_16x16x32_bf16 v[52:55], a[228:231], v[92:95], v[52:55]\n" \
"v_accvgpr_write_b32 a130, v178\n" \
"v_accvgpr_write_b32 a131, v179\n" \
"v_mfma_f32_16x16x32_bf16 v[52:55], a[232:235], v[100:103], v[52:55]\n" \
"ds_read_b64_tr_b16 a[204:205], v212 offset:20480\n" \
"ds_read_b64_tr_b16 a[206:207], v212 offset:20992\n" \
"v_mfma_f32_16x16x32_bf16 v[52:55], a[236:239], v[108:111], v[52:55]\n" \
"buffer_load_dwordx4 v225, s[56:59], s68 offen offset:1024 lds\n" \
"v_mfma_f32_16x16x32_bf16 v[56:59], a[240:243], v[80:83], v[56:59]\n" \
"s_waitcnt lgkmcnt(6)\n" \
"ds_read_b64_tr_b16 a[208:209], v213 offset:0\n" \
"ds_read_b64_tr_b16 a[210:211], v213 offset:512\n" \
"v_mfma_f32_16x16x32_bf16 v[56:59], a[244:247], v[88:91], v[56:59]\n" \
"v_accvgpr_write_b32 a134, v182\n" \
"v_accvgpr_write_b32 a135, v183\n" \
"v_mfma_f32_16x16x32_bf16 v[56:59], a[248:251], v[96:99], v[56:59]\n" \
"ds_read_b64_tr_b16 a[212:213], v213 offset:4096\n" \
"ds_read_b64_tr_b16 a[214:215], v213 offset:4608\n" \
"v_mfma_f32_16x16x32_bf16 v[56:59], a[252:255], v[104:107], v[56:59]\n" \
"buffer_load_dwordx4 v226, s[56:59], s68 offen offset:2048 lds\n" \
"v_mfma_f32_16x16x32_bf16 v[60:63], a[240:243], v[84:87], v[60:63]\n" \
"ds_read_b64_tr_b16 a[216:217], v213 offset:16384\n" \
"ds_read_b64_tr_b16 a[218:219], v213 offset:16896\n" \
"v_mfma_f32_16x16x32_bf16 v[60:63], a[244:247], v[92:95], v[60:63]\n" \
"v_cvt_pk_bf16_f32 v232, v16, v17\n" \
"v_cvt_pk_bf16_f32 v233, v18, v19\n" \
"v_mfma_f32_16x16x32_bf16 v[60:63], a[248:251], v[100:103], v[60:63]\n" \
"ds_read_b64_tr_b16 a[220:221], v213 offset:20480\n" \
"ds_read_b64_tr_b16 a[222:223], v213 offset:20992\n" \
"v_mfma_f32_16x16x32_bf16 v[60:63], a[252:255], v[108:111], v[60:63]\n" \
"buffer_load_dwordx4 v227, s[56:59], s68 offen offset:3072 lds\n" \
"v_mfma_f32_16x16x32_bf16 v[32:35], a[192:195], v[112:115], v[32:35]\n" \
"s_waitcnt lgkmcnt(6)\n" \
"ds_read_b64_tr_b16 a[224:225], v214 offset:0\n" \
"ds_read_b64_tr_b16 a[226:227], v214 offset:512\n" \
"v_mfma_f32_16x16x32_bf16 v[32:35], a[196:199], v[120:123], v[32:35]\n" \
"s_mov_b32 s69, 1573120\n" \
"v_cvt_pk_bf16_f32 v234, v24, v25\n" \
"v_cvt_pk_bf16_f32 v235, v26, v27\n" \
"v_mfma_f32_16x16x32_bf16 v[32:35], a[200:203], v[128:131], v[32:35]\n" \
"s_add_u32 m0, s50, 114688\n" \
"ds_read_b64_tr_b16 a[228:229], v214 offset:4096\n" \
"ds_read_b64_tr_b16 a[230:231], v214 offset:4608\n" \
"v_mfma_f32_16x16x32_bf16 v[32:35], a[204:207], v[136:139], v[32:35]\n" \
"buffer_load_dwordx4 v224, s[56:59], s69 offen offset:0 lds\n" \
"v_mfma_f32_16x16x32_bf16 v[36:39], a[192:195], v[116:119], v[36:39]\n" \
"ds_read_b64_tr_b16 a[232:233], v214 offset:16384\n" \
"ds_read_b64_tr_b16 a[234:235], v214 offset:16896\n" \
"v_mfma_f32_16x16x32_bf16 v[36:39], a[196:199], v[124:127], v[36:39]\n" \
"v_cvt_pk_bf16_f32 v236, v20, v21\n" \
"v_cvt_pk_bf16_f32 v237, v22, v23\n" \
"v_mfma_f32_16x16x32_bf16 v[36:39], a[200:203], v[132:135], v[36:39]\n" \
"ds_read_b64_tr_b16 a[236:237], v214 offset:20480\n" \
"ds_read_b64_tr_b16 a[238:239], v214 offset:20992\n" \
"v_mfma_f32_16x16x32_bf16 v[36:39], a[204:207], v[140:143], v[36:39]\n" \
"buffer_load_dwordx4 v225, s[56:59], s69 offen offset:1024 lds\n" \
"v_mfma_f32_16x16x32_bf16 v[40:43], a[208:211], v[112:115], v[40:43]\n" \
"s_waitcnt lgkmcnt(6)\n" \
"ds_read_b64_tr_b16 a[240:241], v215 offset:0\n" \
"ds_read_b64_tr_b16 a[242:243], v215 offset:512\n" \
"v_mfma_f32_16x16x32_bf16 v[40:43], a[212:215], v[120:123], v[40:43]\n" \
"v_cvt_pk_bf16_f32 v238, v28, v29\n" \
"v_cvt_pk_bf16_f32 v239, v30, v31\n" \
"v_mfma_f32_16x16x32_bf16 v[40:43], a[216:219], v[128:131], v[40:43]\n" \
"ds_read_b64_tr_b16 a[244:245], v215 offset:4096\n" \
"ds_read_b64_tr_b16 a[246:247], v215 offset:4608\n" \
"v_mfma_f32_16x16x32_bf16 v[40:43], a[220:223], v[136:139], v[40:43]\n" \
"buffer_load_dwordx4 v226, s[56:59], s69 offen offset:2048 lds\n" \
"v_mfma_f32_16x16x32_bf16 v[44:47], a[208:211], v[116:119], v[44:47]\n" \
"ds_read_b64_tr_b16 a[248:249], v215 offset:16384\n" \
"ds_read_b64_tr_b16 a[250:251], v215 offset:16896\n" \
"v_mfma_f32_16x16x32_bf16 v[44:47], a[212:215], v[124:127], v[44:47]\n" \
"v_permlane32_swap_b32 v232, v234\n" \
"v_permlane32_swap_b32 v233, v235\n" \
"v_mfma_f32_16x16x32_bf16 v[44:47], a[216:219], v[132:135], v[44:47]\n" \
"s_waitcnt vmcnt(15)\n" \
"ds_read_b64_tr_b16 a[252:253], v215 offset:20480\n" \
"ds_read_b64_tr_b16 a[254:255], v215 offset:20992\n" \
"v_mfma_f32_16x16x32_bf16 v[44:47], a[220:223], v[140:143], v[44:47]\n" \
"s_barrier\n" \
"buffer_load_dwordx4 v227, s[56:59], s69 offen offset:3072 lds\n" \
"v_mfma_f32_16x16x32_bf16 v[48:51], a[224:227], v[112:115], v[48:51]\n" \
"s_waitcnt lgkmcnt(6)\n" \
"ds_read_b64_tr_b16 a[192:193], v212 offset:32768\n" \
"ds_read_b64_tr_b16 a[194:195], v212 offset:33280\n" \
"v_mfma_f32_16x16x32_bf16 v[48:51], a[228:231], v[120:123], v[48:51]\n" \
"s_mov_b32 s68, 2097408\n" \
"v_permlane32_swap_b32 v236, v238\n" \
"v_permlane32_swap_b32 v237, v239\n" \
"v_mfma_f32_16x16x32_bf16 v[48:51], a[232:235], v[128:131], v[48:51]\n" \
"s_add_u32 m0, s50, 131072\n" \
"ds_read_b64_tr_b16 a[196:197], v212 offset:36864\n" \
"ds_read_b64_tr_b16 a[198:199], v212 offset:37376\n" \
"v_mfma_f32_16x16x32_bf16 v[48:51], a[236:239], v[136:139], v[48:51]\n" \
"buffer_load_dwordx4 v224, s[56:59], s68 offen offset:0 lds\n" \
"v_mfma_f32_16x16x32_bf16 v[52:55], a[224:227], v[116:119], v[52:55]\n" \
"ds_read_b64_tr_b16 a[200:201], v216 offset:0\n" \
"ds_read_b64_tr_b16 a[202:203], v216 offset:512\n" \
"v_mfma_f32_16x16x32_bf16 v[52:55], a[228:231], v[124:127], v[52:55]\n" \
"v_permlane16_swap_b32 v232, v234\n" \
"v_permlane16_swap_b32 v233, v235\n" \
"v_mfma_f32_16x16x32_bf16 v[52:55], a[232:235], v[132:135], v[52:55]\n" \
"ds_read_b64_tr_b16 a[204:205], v216 offset:4096\n" \
"ds_read_b64_tr_b16 a[206:207], v216 offset:4608\n" \
"v_mfma_f32_16x16x32_bf16 v[52:55], a[236:239], v[140:143], v[52:55]\n" \
"buffer_load_dwordx4 v225, s[56:59], s68 offen offset:1024 lds\n" \
"v_mfma_f32_16x16x32_bf16 v[56:59], a[240:243], v[112:115], v[56:59]\n" \
"s_waitcnt lgkmcnt(6)\n" \
"ds_read_b64_tr_b16 a[208:209], v213 offset:32768\n" \
"ds_read_b64_tr_b16 a[210:211], v213 offset:33280\n" \
"v_mfma_f32_16x16x32_bf16 v[56:59], a[244:247], v[120:123], v[56:59]\n" \
"v_permlane16_swap_b32 v236, v238\n" \
"v_permlane16_swap_b32 v237, v239\n" \
"v_mfma_f32_16x16x32_bf16 v[56:59], a[248:251], v[128:131], v[56:59]\n" \
"ds_read_b64_tr_b16 a[212:213], v213 offset:36864\n" \
"ds_read_b64_tr_b16 a[214:215], v213 offset:37376\n" \
"v_mfma_f32_16x16x32_bf16 v[56:59], a[252:255], v[136:139], v[56:59]\n" \
"buffer_load_dwordx4 v226, s[56:59], s68 offen offset:2048 lds\n" \
"v_mfma_f32_16x16x32_bf16 v[60:63], a[240:243], v[116:119], v[60:63]\n" \
"ds_read_b64_tr_b16 a[216:217], v217 offset:0\n" \
"ds_read_b64_tr_b16 a[218:219], v217 offset:512\n" \
"v_mfma_f32_16x16x32_bf16 v[60:63], a[244:247], v[124:127], v[60:63]\n" \
"v_accvgpr_write_b32 a136, v232\n" \
"v_accvgpr_write_b32 a137, v233\n" \
"v_mfma_f32_16x16x32_bf16 v[60:63], a[248:251], v[132:135], v[60:63]\n" \
"ds_read_b64_tr_b16 a[220:221], v217 offset:4096\n" \
"ds_read_b64_tr_b16 a[222:223], v217 offset:4608\n" \
"v_mfma_f32_16x16x32_bf16 v[60:63], a[252:255], v[140:143], v[60:63]\n" \
"buffer_load_dwordx4 v227, s[56:59], s68 offen offset:3072 lds\n" \
"v_mfma_f32_16x16x32_bf16 v[32:35], a[192:195], v[144:147], v[32:35]\n" \
"s_waitcnt lgkmcnt(6)\n" \
"ds_read_b64_tr_b16 a[224:225], v214 offset:32768\n" \
"ds_read_b64_tr_b16 a[226:227], v214 offset:33280\n" \
"v_mfma_f32_16x16x32_bf16 v[32:35], a[196:199], v[152:155], v[32:35]\n" \
"s_mov_b32 s69, 2621696\n" \
"v_accvgpr_write_b32 a140, v236\n" \
"v_accvgpr_write_b32 a141, v237\n" \
"v_mfma_f32_16x16x32_bf16 v[32:35], a[200:203], v[160:163], v[32:35]\n" \
"s_add_u32 m0, s50, 147456\n" \
"ds_read_b64_tr_b16 a[228:229], v214 offset:36864\n" \
"ds_read_b64_tr_b16 a[230:231], v214 offset:37376\n" \
"v_mfma_f32_16x16x32_bf16 v[32:35], a[204:207], v[168:171], v[32:35]\n" \
"buffer_load_dwordx4 v224, s[56:59], s69 offen offset:0 lds\n" \
"v_mfma_f32_16x16x32_bf16 v[36:39], a[192:195], v[148:151], v[36:39]\n" \
"ds_read_b64_tr_b16 a[232:233], v218 offset:0\n" \
"ds_read_b64_tr_b16 a[234:235], v218 offset:512\n" \
"v_mfma_f32_16x16x32_bf16 v[36:39], a[196:199], v[156:159], v[36:39]\n" \
"v_accvgpr_write_b32 a138, v234\n" \
"v_accvgpr_write_b32 a139, v235\n" \
"v_mfma_f32_16x16x32_bf16 v[36:39], a[200:203], v[164:167], v[36:39]\n" \
"ds_read_b64_tr_b16 a[236:237], v218 offset:4096\n" \
"ds_read_b64_tr_b16 a[238:239], v218 offset:4608\n" \
"v_mfma_f32_16x16x32_bf16 v[36:39], a[204:207], v[172:175], v[36:39]\n" \
"buffer_load_dwordx4 v225, s[56:59], s69 offen offset:1024 lds\n" \
"v_mfma_f32_16x16x32_bf16 v[40:43], a[208:211], v[144:147], v[40:43]\n" \
"s_waitcnt lgkmcnt(6)\n" \
"ds_read_b64_tr_b16 a[240:241], v215 offset:32768\n" \
"ds_read_b64_tr_b16 a[242:243], v215 offset:33280\n" \
"v_mfma_f32_16x16x32_bf16 v[40:43], a[212:215], v[152:155], v[40:43]\n" \
"v_accvgpr_write_b32 a142, v238\n" \
"v_accvgpr_write_b32 a143, v239\n" \
"v_mfma_f32_16x16x32_bf16 v[40:43], a[216:219], v[160:163], v[40:43]\n" \
"ds_read_b64_tr_b16 a[244:245], v215 offset:36864\n" \
"ds_read_b64_tr_b16 a[246:247], v215 offset:37376\n" \
"v_mfma_f32_16x16x32_bf16 v[40:43], a[220:223], v[168:171], v[40:43]\n" \
"buffer_load_dwordx4 v226, s[56:59], s69 offen offset:2048 lds\n" \
"v_mfma_f32_16x16x32_bf16 v[44:47], a[208:211], v[148:151], v[44:47]\n" \
"ds_read_b64_tr_b16 a[248:249], v219 offset:0\n" \
"ds_read_b64_tr_b16 a[250:251], v219 offset:512\n" \
"v_mfma_f32_16x16x32_bf16 v[44:47], a[212:215], v[156:159], v[44:47]\n" \
"v_mfma_f32_16x16x32_bf16 v[44:47], a[216:219], v[164:167], v[44:47]\n" \
"s_waitcnt vmcnt(15)\n" \
"ds_read_b64_tr_b16 a[252:253], v219 offset:4096\n" \
"ds_read_b64_tr_b16 a[254:255], v219 offset:4608\n" \
"v_mfma_f32_16x16x32_bf16 v[44:47], a[220:223], v[172:175], v[44:47]\n" \
"s_barrier\n" \
"buffer_load_dwordx4 v227, s[56:59], s69 offen offset:3072 lds\n" \
"v_mfma_f32_16x16x32_bf16 v[48:51], a[224:227], v[144:147], v[48:51]\n" \
"s_waitcnt lgkmcnt(6)\n" \
"ds_read_b64_tr_b16 a[192:193], v212 offset:49152\n" \
"ds_read_b64_tr_b16 a[194:195], v212 offset:49664\n" \
"v_mfma_f32_16x16x32_bf16 v[48:51], a[228:231], v[152:155], v[48:51]\n" \
"s_mov_b32 s68, 3145984\n" \
"v_mfma_f32_16x16x32_bf16 v[48:51], a[232:235], v[160:163], v[48:51]\n" \
"s_add_u32 m0, s50, 0\n" \
"ds_read_b64_tr_b16 a[196:197], v212 offset:53248\n" \
"ds_read_b64_tr_b16 a[198:199], v212 offset:53760\n" \
"v_mfma_f32_16x16x32_bf16 v[48:51], a[236:239], v[168:171], v[48:51]\n" \
"buffer_load_dwordx4 v224, s[56:59], s68 offen offset:0 lds\n" \
"v_mfma_f32_16x16x32_bf16 v[52:55], a[224:227], v[148:151], v[52:55]\n" \
"ds_read_b64_tr_b16 a[200:201], v216 offset:16384\n" \
"ds_read_b64_tr_b16 a[202:203], v216 offset:16896\n" \
"v_mfma_f32_16x16x32_bf16 v[52:55], a[228:231], v[156:159], v[52:55]\n" \
"v_cvt_pk_bf16_f32 v176, v32, v33\n" \
"v_cvt_pk_bf16_f32 v177, v34, v35\n" \
"v_mfma_f32_16x16x32_bf16 v[52:55], a[232:235], v[164:167], v[52:55]\n" \
"ds_read_b64_tr_b16 a[204:205], v216 offset:20480\n" \
"ds_read_b64_tr_b16 a[206:207], v216 offset:20992\n" \
"v_mfma_f32_16x16x32_bf16 v[52:55], a[236:239], v[172:175], v[52:55]\n" \
"buffer_load_dwordx4 v225, s[56:59], s68 offen offset:1024 lds\n" \
"v_mfma_f32_16x16x32_bf16 v[56:59], a[240:243], v[144:147], v[56:59]\n" \
"s_waitcnt lgkmcnt(6)\n" \
"ds_read_b64_tr_b16 a[208:209], v213 offset:49152\n" \
"ds_read_b64_tr_b16 a[210:211], v213 offset:49664\n" \
"v_mfma_f32_16x16x32_bf16 v[56:59], a[244:247], v[152:155], v[56:59]\n" \
"v_cvt_pk_bf16_f32 v178, v40, v41\n" \
"v_cvt_pk_bf16_f32 v179, v42, v43\n" \
"v_mfma_f32_16x16x32_bf16 v[56:59], a[248:251], v[160:163], v[56:59]\n" \
"ds_read_b64_tr_b16 a[212:213], v213 offset:53248\n" \
"ds_read_b64_tr_b16 a[214:215], v213 offset:53760\n" \
"v_mfma_f32_16x16x32_bf16 v[56:59], a[252:255], v[168:171], v[56:59]\n" \
"buffer_load_dwordx4 v226, s[56:59], s68 offen offset:2048 lds\n" \
"v_mfma_f32_16x16x32_bf16 v[60:63], a[240:243], v[148:151], v[60:63]\n" \
"ds_read_b64_tr_b16 a[216:217], v217 offset:16384\n" \
"ds_read_b64_tr_b16 a[218:219], v217 offset:16896\n" \
"v_mfma_f32_16x16x32_bf16 v[60:63], a[244:247], v[156:159], v[60:63]\n" \
"v_cvt_pk_bf16_f32 v180, v36, v37\n" \
"v_cvt_pk_bf16_f32 v181, v38, v39\n" \
"v_mfma_f32_16x16x32_bf16 v[60:63], a[248:251], v[164:167], v[60:63]\n" \
"ds_read_b64_tr_b16 a[220:221], v217 offset:20480\n" \
"ds_read_b64_tr_b16 a[222:223], v217 offset:20992\n" \
"v_mfma_f32_16x16x32_bf16 v[60:63], a[252:255], v[172:175], v[60:63]\n" \
"buffer_load_dwordx4 v227, s[56:59], s68 offen offset:3072 lds\n" \
"v_mfma_f32_16x16x32_bf16 v[0:3], a[192:195], v[64:67], 0\n" \
"s_waitcnt lgkmcnt(6)\n" \
"ds_read_b64_tr_b16 a[224:225], v214 offset:49152\n" \
"ds_read_b64_tr_b16 a[226:227], v214 offset:49664\n" \
"v_mfma_f32_16x16x32_bf16 v[0:3], a[196:199], v[72:75], v[0:3]\n" \
"s_mov_b32 s69, 384\n" \
"v_cvt_pk_bf16_f32 v182, v44, v45\n" \
"v_cvt_pk_bf16_f32 v183, v46, v47\n" \
"v_mfma_f32_16x16x32_bf16 v[0:3], a[200:203], v[80:83], v[0:3]\n" \
"s_add_u32 m0, s50, 16384\n" \
"ds_read_b64_tr_b16 a[228:229], v214 offset:53248\n" \
"ds_read_b64_tr_b16 a[230:231], v214 offset:53760\n" \
"v_mfma_f32_16x16x32_bf16 v[0:3], a[204:207], v[88:91], v[0:3]\n" \
"buffer_load_dwordx4 v224, s[56:59], s69 offen offset:0 lds\n" \
"v_mfma_f32_16x16x32_bf16 v[4:7], a[192:195], v[68:71], 0\n" \
"ds_read_b64_tr_b16 a[232:233], v218 offset:16384\n" \
"ds_read_b64_tr_b16 a[234:235], v218 offset:16896\n" \
"v_mfma_f32_16x16x32_bf16 v[4:7], a[196:199], v[76:79], v[4:7]\n" \
"v_permlane32_swap_b32 v176, v178\n" \
"v_permlane32_swap_b32 v177, v179\n" \
"v_mfma_f32_16x16x32_bf16 v[4:7], a[200:203], v[84:87], v[4:7]\n" \
"ds_read_b64_tr_b16 a[236:237], v218 offset:20480\n" \
"ds_read_b64_tr_b16 a[238:239], v218 offset:20992\n" \
"v_mfma_f32_16x16x32_bf16 v[4:7], a[204:207], v[92:95], v[4:7]\n" \
"buffer_load_dwordx4 v225, s[56:59], s69 offen offset:1024 lds\n" \
"v_mfma_f32_16x16x32_bf16 v[8:11], a[208:211], v[64:67], 0\n" \
"s_waitcnt lgkmcnt(6)\n" \
"ds_read_b64_tr_b16 a[240:241], v215 offset:49152\n" \
"ds_read_b64_tr_b16 a[242:243], v215 offset:49664\n" \
"v_mfma_f32_16x16x32_bf16 v[8:11], a[212:215], v[72:75], v[8:11]\n" \
"v_permlane32_swap_b32 v180, v182\n" \
"v_permlane32_swap_b32 v181, v183\n" \
"v_mfma_f32_16x16x32_bf16 v[8:11], a[216:219], v[80:83], v[8:11]\n" \
"ds_read_b64_tr_b16 a[244:245], v215 offset:53248\n" \
"ds_read_b64_tr_b16 a[246:247], v215 offset:53760\n" \
"v_mfma_f32_16x16x32_bf16 v[8:11], a[220:223], v[88:91], v[8:11]\n" \
"buffer_load_dwordx4 v226, s[56:59], s69 offen offset:2048 lds\n" \
"v_mfma_f32_16x16x32_bf16 v[12:15], a[208:211], v[68:71], 0\n" \
"ds_read_b64_tr_b16 a[248:249], v219 offset:16384\n" \
"ds_read_b64_tr_b16 a[250:251], v219 offset:16896\n" \
"v_mfma_f32_16x16x32_bf16 v[12:15], a[212:215], v[76:79], v[12:15]\n" \
"v_permlane16_swap_b32 v176, v178\n" \
"v_permlane16_swap_b32 v177, v179\n" \
"v_mfma_f32_16x16x32_bf16 v[12:15], a[216:219], v[84:87], v[12:15]\n" \
"s_waitcnt vmcnt(15)\n" \
"ds_read_b64_tr_b16 a[252:253], v219 offset:20480\n" \
"ds_read_b64_tr_b16 a[254:255], v219 offset:20992\n" \
"v_mfma_f32_16x16x32_bf16 v[12:15], a[220:223], v[92:95], v[12:15]\n" \
"s_barrier\n" \
"buffer_load_dwordx4 v227, s[56:59], s69 offen offset:3072 lds\n" \
"v_mfma_f32_16x16x32_bf16 v[16:19], a[224:227], v[64:67], 0\n" \
"s_waitcnt lgkmcnt(6)\n" \
"ds_read_b64_tr_b16 a[192:193], v216 offset:32768\n" \
"ds_read_b64_tr_b16 a[194:195], v216 offset:33280\n" \
"v_mfma_f32_16x16x32_bf16 v[16:19], a[228:231], v[72:75], v[16:19]\n" \
"s_mov_b32 s68, 524672\n" \
"v_permlane16_swap_b32 v180, v182\n" \
"v_permlane16_swap_b32 v181, v183\n" \
"v_mfma_f32_16x16x32_bf16 v[16:19], a[232:235], v[80:83], v[16:19]\n" \
"s_add_u32 m0, s50, 32768\n" \
"ds_read_b64_tr_b16 a[196:197], v216 offset:36864\n" \
"ds_read_b64_tr_b16 a[198:199], v216 offset:37376\n" \
"v_mfma_f32_16x16x32_bf16 v[16:19], a[236:239], v[88:91], v[16:19]\n" \
"buffer_load_dwordx4 v224, s[56:59], s68 offen offset:0 lds\n" \
"v_mfma_f32_16x16x32_bf16 v[20:23], a[224:227], v[68:71], 0\n" \
"ds_read_b64_tr_b16 a[200:201], v216 offset:49152\n" \
"ds_read_b64_tr_b16 a[202:203], v216 offset:49664\n" \
"v_mfma_f32_16x16x32_bf16 v[20:23], a[228:231], v[76:79], v[20:23]\n" \
"v_accvgpr_write_b32 a144, v176\n" \
"v_accvgpr_write_b32 a145, v177\n" \
"v_mfma_f32_16x16x32_bf16 v[20:23], a[232:235], v[84:87], v[20:23]\n" \
"ds_read_b64_tr_b16 a[204:205], v216 offset:53248\n" \
"ds_read_b64_tr_b16 a[206:207], v216 offset:53760\n" \
"v_mfma_f32_16x16x32_bf16 v[20:23], a[236:239], v[92:95], v[20:23]\n" \
"buffer_load_dwordx4 v225, s[56:59], s68 offen offset:1024 lds\n" \
"v_mfma_f32_16x16x32_bf16 v[24:27], a[240:243], v[64:67], 0\n" \
"s_waitcnt lgkmcnt(6)\n" \
"ds_read_b64_tr_b16 a[208:209], v217 offset:32768\n" \
"ds_read_b64_tr_b16 a[210:211], v217 offset:33280\n" \
"v_mfma_f32_16x16x32_bf16 v[24:27], a[244:247], v[72:75], v[24:27]\n" \
"v_accvgpr_write_b32 a148, v180\n" \
"v_accvgpr_write_b32 a149, v181\n" \
"v_mfma_f32_16x16x32_bf16 v[24:27], a[248:251], v[80:83], v[24:27]\n" \
"ds_read_b64_tr_b16 a[212:213], v217 offset:36864\n" \
"ds_read_b64_tr_b16 a[214:215], v217 offset:37376\n" \
"v_mfma_f32_16x16x32_bf16 v[24:27], a[252:255], v[88:91], v[24:27]\n" \
"buffer_load_dwordx4 v226, s[56:59], s68 offen offset:2048 lds\n" \
"v_mfma_f32_16x16x32_bf16 v[28:31], a[240:243], v[68:71], 0\n" \
"ds_read_b64_tr_b16 a[216:217], v217 offset:49152\n" \
"ds_read_b64_tr_b16 a[218:219], v217 offset:49664\n" \
"v_mfma_f32_16x16x32_bf16 v[28:31], a[244:247], v[76:79], v[28:31]\n" \
"v_accvgpr_write_b32 a146, v178\n" \
"v_accvgpr_write_b32 a147, v179\n" \
"v_mfma_f32_16x16x32_bf16 v[28:31], a[248:251], v[84:87], v[28:31]\n" \
"ds_read_b64_tr_b16 a[220:221], v217 offset:53248\n" \
"ds_read_b64_tr_b16 a[222:223], v217 offset:53760\n" \
"v_mfma_f32_16x16x32_bf16 v[28:31], a[252:255], v[92:95], v[28:31]\n" \
"buffer_load_dwordx4 v227, s[56:59], s68 offen offset:3072 lds\n" \
"v_mfma_f32_16x16x32_bf16 v[0:3], a[192:195], v[96:99], v[0:3]\n" \
"s_waitcnt lgkmcnt(6)\n" \
"ds_read_b64_tr_b16 a[224:225], v218 offset:32768\n" \
"ds_read_b64_tr_b16 a[226:227], v218 offset:33280\n" \
"v_mfma_f32_16x16x32_bf16 v[0:3], a[196:199], v[104:107], v[0:3]\n" \
"s_mov_b32 s69, 1048960\n" \
"v_accvgpr_write_b32 a150, v182\n" \
"v_accvgpr_write_b32 a151, v183\n" \
"v_mfma_f32_16x16x32_bf16 v[0:3], a[200:203], v[112:115], v[0:3]\n" \
"s_add_u32 m0, s50, 65536\n" \
"ds_read_b64_tr_b16 a[228:229], v218 offset:36864\n" \
"ds_read_b64_tr_b16 a[230:231], v218 offset:37376\n" \
"v_mfma_f32_16x16x32_bf16 v[0:3], a[204:207], v[120:123], v[0:3]\n" \
"buffer_load_dwordx4 v224, s[56:59], s69 offen offset:0 lds\n" \
"v_mfma_f32_16x16x32_bf16 v[4:7], a[192:195], v[100:103], v[4:7]\n" \
"ds_read_b64_tr_b16 a[232:233], v218 offset:49152\n" \
"ds_read_b64_tr_b16 a[234:235], v218 offset:49664\n" \
"v_mfma_f32_16x16x32_bf16 v[4:7], a[196:199], v[108:111], v[4:7]\n" \
"v_cvt_pk_bf16_f32 v232, v48, v49\n" \
"v_cvt_pk_bf16_f32 v233, v50, v51\n" \
"v_mfma_f32_16x16x32_bf16 v[4:7], a[200:203], v[116:119], v[4:7]\n" \
"ds_read_b64_tr_b16 a[236:237], v218 offset:53248\n" \
"ds_read_b64_tr_b16 a[238:239], v218 offset:53760\n" \
"v_mfma_f32_16x16x32_bf16 v[4:7], a[204:207], v[124:127], v[4:7]\n" \
"buffer_load_dwordx4 v225, s[56:59], s69 offen offset:1024 lds\n" \
"v_mfma_f32_16x16x32_bf16 v[8:11], a[208:211], v[96:99], v[8:11]\n" \
"s_waitcnt lgkmcnt(6)\n" \
"ds_read_b64_tr_b16 a[240:241], v219 offset:32768\n" \
"ds_read_b64_tr_b16 a[242:243], v219 offset:33280\n" \
"v_mfma_f32_16x16x32_bf16 v[8:11], a[212:215], v[104:107], v[8:11]\n" \
"v_cvt_pk_bf16_f32 v234, v56, v57\n" \
"v_cvt_pk_bf16_f32 v235, v58, v59\n" \
"v_mfma_f32_16x16x32_bf16 v[8:11], a[216:219], v[112:115], v[8:11]\n" \
"ds_read_b64_tr_b16 a[244:245], v219 offset:36864\n" \
"ds_read_b64_tr_b16 a[246:247], v219 offset:37376\n" \
"v_mfma_f32_16x16x32_bf16 v[8:11], a[220:223], v[120:123], v[8:11]\n" \
"buffer_load_dwordx4 v226, s[56:59], s69 offen offset:2048 lds\n" \
"v_mfma_f32_16x16x32_bf16 v[12:15], a[208:211], v[100:103], v[12:15]\n" \
"ds_read_b64_tr_b16 a[248:249], v219 offset:49152\n" \
"ds_read_b64_tr_b16 a[250:251], v219 offset:49664\n" \
"v_mfma_f32_16x16x32_bf16 v[12:15], a[212:215], v[108:111], v[12:15]\n" \
"v_cvt_pk_bf16_f32 v236, v52, v53\n" \
"v_cvt_pk_bf16_f32 v237, v54, v55\n" \
"v_mfma_f32_16x16x32_bf16 v[12:15], a[216:219], v[116:119], v[12:15]\n" \
"s_waitcnt vmcnt(15)\n" \
"ds_read_b64_tr_b16 a[252:253], v219 offset:53248\n" \
"ds_read_b64_tr_b16 a[254:255], v219 offset:53760\n" \
"v_mfma_f32_16x16x32_bf16 v[12:15], a[220:223], v[124:127], v[12:15]\n" \
"s_barrier\n" \
"buffer_load_dwordx4 v227, s[56:59], s69 offen offset:3072 lds\n" \
"v_mfma_f32_16x16x32_bf16 v[16:19], a[224:227], v[96:99], v[16:19]\n" \
"s_waitcnt lgkmcnt(6)\n" \
"ds_read_b64_tr_b16 a[192:193], v220 offset:0\n" \
"ds_read_b64_tr_b16 a[194:195], v220 offset:512\n" \
"v_mfma_f32_16x16x32_bf16 v[16:19], a[228:231], v[104:107], v[16:19]\n" \
"s_mov_b32 s68, 1573248\n" \
"v_cvt_pk_bf16_f32 v238, v60, v61\n" \
"v_cvt_pk_bf16_f32 v239, v62, v63\n" \
"v_mfma_f32_16x16x32_bf16 v[16:19], a[232:235], v[112:115], v[16:19]\n" \
"s_add_u32 m0, s50, 49152\n" \
"ds_read_b64_tr_b16 a[196:197], v220 offset:4096\n" \
"ds_read_b64_tr_b16 a[198:199], v220 offset:4608\n" \
"v_mfma_f32_16x16x32_bf16 v[16:19], a[236:239], v[120:123], v[16:19]\n" \
"buffer_load_dwordx4 v224, s[56:59], s68 offen offset:0 lds\n" \
"v_mfma_f32_16x16x32_bf16 v[20:23], a[224:227], v[100:103], v[20:23]\n" \
"ds_read_b64_tr_b16 a[200:201], v220 offset:16384\n" \
"ds_read_b64_tr_b16 a[202:203], v220 offset:16896\n" \
"v_mfma_f32_16x16x32_bf16 v[20:23], a[228:231], v[108:111], v[20:23]\n" \
"v_permlane32_swap_b32 v232, v234\n" \
"v_permlane32_swap_b32 v233, v235\n" \
"v_mfma_f32_16x16x32_bf16 v[20:23], a[232:235], v[116:119], v[20:23]\n" \
"ds_read_b64_tr_b16 a[204:205], v220 offset:20480\n" \
"ds_read_b64_tr_b16 a[206:207], v220 offset:20992\n" \
"v_mfma_f32_16x16x32_bf16 v[20:23], a[236:239], v[124:127], v[20:23]\n" \
"buffer_load_dwordx4 v225, s[56:59], s68 offen offset:1024 lds\n" \
"v_mfma_f32_16x16x32_bf16 v[24:27], a[240:243], v[96:99], v[24:27]\n" \
"s_waitcnt lgkmcnt(6)\n" \
"ds_read_b64_tr_b16 a[208:209], v221 offset:0\n" \
"ds_read_b64_tr_b16 a[210:211], v221 offset:512\n" \
"v_mfma_f32_16x16x32_bf16 v[24:27], a[244:247], v[104:107], v[24:27]\n" \
"v_permlane32_swap_b32 v236, v238\n" \
"v_permlane32_swap_b32 v237, v239\n" \
"v_mfma_f32_16x16x32_bf16 v[24:27], a[248:251], v[112:115], v[24:27]\n" \
"ds_read_b64_tr_b16 a[212:213], v221 offset:4096\n" \
"ds_read_b64_tr_b16 a[214:215], v221 offset:4608\n" \
"v_mfma_f32_16x16x32_bf16 v[24:27], a[252:255], v[120:123], v[24:27]\n" \
"buffer_load_dwordx4 v226, s[56:59], s68 offen offset:2048 lds\n" \
"v_mfma_f32_16x16x32_bf16 v[28:31], a[240:243], v[100:103], v[28:31]\n" \
"ds_read_b64_tr_b16 a[216:217], v221 offset:16384\n" \
"ds_read_b64_tr_b16 a[218:219], v221 offset:16896\n" \
"v_mfma_f32_16x16x32_bf16 v[28:31], a[244:247], v[108:111], v[28:31]\n" \
"v_permlane16_swap_b32 v232, v234\n" \
"v_permlane16_swap_b32 v233, v235\n" \
"v_mfma_f32_16x16x32_bf16 v[28:31], a[248:251], v[116:119], v[28:31]\n" \
"ds_read_b64_tr_b16 a[220:221], v221 offset:20480\n" \
"ds_read_b64_tr_b16 a[222:223], v221 offset:20992\n" \
"v_mfma_f32_16x16x32_bf16 v[28:31], a[252:255], v[124:127], v[28:31]\n" \
"buffer_load_dwordx4 v227, s[56:59], s68 offen offset:3072 lds\n" \
"v_mfma_f32_16x16x32_bf16 v[0:3], a[192:195], v[128:131], v[0:3]\n" \
"s_waitcnt lgkmcnt(6)\n" \
"ds_read_b64_tr_b16 a[224:225], v222 offset:0\n" \
"ds_read_b64_tr_b16 a[226:227], v222 offset:512\n" \
"v_mfma_f32_16x16x32_bf16 v[0:3], a[196:199], v[136:139], v[0:3]\n" \
"s_mov_b32 s69, 2097536\n" \
"v_permlane16_swap_b32 v236, v238\n" \
"v_permlane16_swap_b32 v237, v239\n" \
"v_mfma_f32_16x16x32_bf16 v[0:3], a[200:203], v[144:147], v[0:3]\n" \
"s_add_u32 m0, s50, 81920\n" \
"ds_read_b64_tr_b16 a[228:229], v222 offset:4096\n" \
"ds_read_b64_tr_b16 a[230:231], v222 offset:4608\n" \
"v_mfma_f32_16x16x32_bf16 v[0:3], a[204:207], v[152:155], v[0:3]\n" \
"buffer_load_dwordx4 v224, s[56:59], s69 offen offset:0 lds\n" \
"v_mfma_f32_16x16x32_bf16 v[4:7], a[192:195], v[132:135], v[4:7]\n" \
"ds_read_b64_tr_b16 a[232:233], v222 offset:16384\n" \
"ds_read_b64_tr_b16 a[234:235], v222 offset:16896\n" \
"v_mfma_f32_16x16x32_bf16 v[4:7], a[196:199], v[140:143], v[4:7]\n" \
"v_accvgpr_write_b32 a152, v232\n" \
"v_accvgpr_write_b32 a153, v233\n" \
"v_mfma_f32_16x16x32_bf16 v[4:7], a[200:203], v[148:151], v[4:7]\n" \
"ds_read_b64_tr_b16 a[236:237], v222 offset:20480\n" \
"ds_read_b64_tr_b16 a[238:239], v222 offset:20992\n" \
"v_mfma_f32_16x16x32_bf16 v[4:7], a[204:207], v[156:159], v[4:7]\n" \
"buffer_load_dwordx4 v225, s[56:59], s69 offen offset:1024 lds\n" \
"v_mfma_f32_16x16x32_bf16 v[8:11], a[208:211], v[128:131], v[8:11]\n" \
"s_waitcnt lgkmcnt(6)\n" \
"ds_read_b64_tr_b16 a[240:241], v223 offset:0\n" \
"ds_read_b64_tr_b16 a[242:243], v223 offset:512\n" \
"v_mfma_f32_16x16x32_bf16 v[8:11], a[212:215], v[136:139], v[8:11]\n" \
"v_accvgpr_write_b32 a156, v236\n" \
"v_accvgpr_write_b32 a157, v237\n" \
"v_mfma_f32_16x16x32_bf16 v[8:11], a[216:219], v[144:147], v[8:11]\n" \
"ds_read_b64_tr_b16 a[244:245], v223 offset:4096\n" \
"ds_read_b64_tr_b16 a[246:247], v223 offset:4608\n" \
"v_mfma_f32_16x16x32_bf16 v[8:11], a[220:223], v[152:155], v[8:11]\n" \
"buffer_load_dwordx4 v226, s[56:59], s69 offen offset:2048 lds\n" \
"v_mfma_f32_16x16x32_bf16 v[12:15], a[208:211], v[132:135], v[12:15]\n" \
"ds_read_b64_tr_b16 a[248:249], v223 offset:16384\n" \
"ds_read_b64_tr_b16 a[250:251], v223 offset:16896\n" \
"v_mfma_f32_16x16x32_bf16 v[12:15], a[212:215], v[140:143], v[12:15]\n" \
"v_accvgpr_write_b32 a154, v234\n" \
"v_accvgpr_write_b32 a155, v235\n" \
"v_mfma_f32_16x16x32_bf16 v[12:15], a[216:219], v[148:151], v[12:15]\n" \
"s_waitcnt vmcnt(15)\n" \
"ds_read_b64_tr_b16 a[252:253], v223 offset:20480\n" \
"ds_read_b64_tr_b16 a[254:255], v223 offset:20992\n" \
"v_mfma_f32_16x16x32_bf16 v[12:15], a[220:223], v[156:159], v[12:15]\n" \
"s_barrier\n" \
"buffer_load_dwordx4 v227, s[56:59], s69 offen offset:3072 lds\n" \
"v_mfma_f32_16x16x32_bf16 v[16:19], a[224:227], v[128:131], v[16:19]\n" \
"s_waitcnt lgkmcnt(6)\n" \
"ds_read_b64_tr_b16 a[192:193], v212 offset:0\n" \
"ds_read_b64_tr_b16 a[194:195], v212 offset:512\n" \
"v_mfma_f32_16x16x32_bf16 v[16:19], a[228:231], v[136:139], v[16:19]\n" \
"s_mov_b32 s68, 2621824\n" \
"v_accvgpr_write_b32 a158, v238\n" \
"v_accvgpr_write_b32 a159, v239\n" \
"v_mfma_f32_16x16x32_bf16 v[16:19], a[232:235], v[144:147], v[16:19]\n" \
"s_add_u32 m0, s50, 98304\n" \
"ds_read_b64_tr_b16 a[196:197], v212 offset:4096\n" \
"ds_read_b64_tr_b16 a[198:199], v212 offset:4608\n" \
"v_mfma_f32_16x16x32_bf16 v[16:19], a[236:239], v[152:155], v[16:19]\n" \
"buffer_load_dwordx4 v224, s[56:59], s68 offen offset:0 lds\n" \
"v_mfma_f32_16x16x32_bf16 v[20:23], a[224:227], v[132:135], v[20:23]\n" \
"ds_read_b64_tr_b16 a[200:201], v213 offset:0\n" \
"ds_read_b64_tr_b16 a[202:203], v213 offset:512\n" \
"v_mfma_f32_16x16x32_bf16 v[20:23], a[228:231], v[140:143], v[20:23]\n" \
"v_mfma_f32_16x16x32_bf16 v[20:23], a[232:235], v[148:151], v[20:23]\n" \
"ds_read_b64_tr_b16 a[204:205], v213 offset:4096\n" \
"ds_read_b64_tr_b16 a[206:207], v213 offset:4608\n" \
"v_mfma_f32_16x16x32_bf16 v[20:23], a[236:239], v[156:159], v[20:23]\n" \
"buffer_load_dwordx4 v225, s[56:59], s68 offen offset:1024 lds\n" \
"v_mfma_f32_16x16x32_bf16 v[24:27], a[240:243], v[128:131], v[24:27]\n" \
"s_waitcnt lgkmcnt(6)\n" \
"ds_read_b64_tr_b16 a[208:209], v214 offset:0\n" \
"ds_read_b64_tr_b16 a[210:211], v214 offset:512\n" \
"v_mfma_f32_16x16x32_bf16 v[24:27], a[244:247], v[136:139], v[24:27]\n" \
"v_mfma_f32_16x16x32_bf16 v[24:27], a[248:251], v[144:147], v[24:27]\n" \
"ds_read_b64_tr_b16 a[212:213], v214 offset:4096\n" \
"ds_read_b64_tr_b16 a[214:215], v214 offset:4608\n" \
"v_mfma_f32_16x16x32_bf16 v[24:27], a[252:255], v[152:155], v[24:27]\n" \
"buffer_load_dwordx4 v226, s[56:59], s68 offen offset:2048 lds\n" \
"v_mfma_f32_16x16x32_bf16 v[28:31], a[240:243], v[132:135], v[28:31]\n" \
"ds_read_b64_tr_b16 a[216:217], v215 offset:0\n" \
"ds_read_b64_tr_b16 a[218:219], v215 offset:512\n" \
"v_mfma_f32_16x16x32_bf16 v[28:31], a[244:247], v[140:143], v[28:31]\n" \
"v_mfma_f32_16x16x32_bf16 v[28:31], a[248:251], v[148:151], v[28:31]\n" \
"ds_read_b64_tr_b16 a[220:221], v215 offset:4096\n" \
"ds_read_b64_tr_b16 a[222:223], v215 offset:4608\n" \
"v_mfma_f32_16x16x32_bf16 v[28:31], a[252:255], v[156:159], v[28:31]\n" \
"buffer_load_dwordx4 v227, s[56:59], s68 offen offset:3072 lds\n" \
"v_mfma_f32_16x16x32_bf16 v[0:3], a[192:195], v[160:163], v[0:3]\n" \
"s_waitcnt lgkmcnt(6)\n" \
"ds_read_b64_tr_b16 a[224:225], v212 offset:16384\n" \
"ds_read_b64_tr_b16 a[226:227], v212 offset:16896\n" \
"v_mfma_f32_16x16x32_bf16 v[0:3], a[196:199], v[168:171], v[0:3]\n" \
"s_mov_b32 s69, 3146112\n" \
"v_mfma_f32_16x16x32_bf16 v[4:7], a[192:195], v[164:167], v[4:7]\n" \
"s_add_u32 m0, s50, 114688\n" \
"ds_read_b64_tr_b16 a[228:229], v212 offset:20480\n" \
"ds_read_b64_tr_b16 a[230:231], v212 offset:20992\n" \
"v_mfma_f32_16x16x32_bf16 v[4:7], a[196:199], v[172:175], v[4:7]\n" \
"buffer_load_dwordx4 v224, s[56:59], s69 offen offset:0 lds\n" \
"v_mfma_f32_16x16x32_bf16 v[8:11], a[200:203], v[160:163], v[8:11]\n" \
"ds_read_b64_tr_b16 a[232:233], v213 offset:16384\n" \
"ds_read_b64_tr_b16 a[234:235], v213 offset:16896\n" \
"v_mfma_f32_16x16x32_bf16 v[8:11], a[204:207], v[168:171], v[8:11]\n" \
"v_mfma_f32_16x16x32_bf16 v[12:15], a[200:203], v[164:167], v[12:15]\n" \
"ds_read_b64_tr_b16 a[236:237], v213 offset:20480\n" \
"ds_read_b64_tr_b16 a[238:239], v213 offset:20992\n" \
"v_mfma_f32_16x16x32_bf16 v[12:15], a[204:207], v[172:175], v[12:15]\n" \
"buffer_load_dwordx4 v225, s[56:59], s69 offen offset:1024 lds\n" \
"v_mfma_f32_16x16x32_bf16 v[16:19], a[208:211], v[160:163], v[16:19]\n" \
"s_waitcnt lgkmcnt(6)\n" \
"ds_read_b64_tr_b16 a[240:241], v214 offset:16384\n" \
"ds_read_b64_tr_b16 a[242:243], v214 offset:16896\n" \
"v_mfma_f32_16x16x32_bf16 v[16:19], a[212:215], v[168:171], v[16:19]\n" \
"v_mfma_f32_16x16x32_bf16 v[20:23], a[208:211], v[164:167], v[20:23]\n" \
"ds_read_b64_tr_b16 a[244:245], v214 offset:20480\n" \
"ds_read_b64_tr_b16 a[246:247], v214 offset:20992\n" \
"v_mfma_f32_16x16x32_bf16 v[20:23], a[212:215], v[172:175], v[20:23]\n" \
"buffer_load_dwordx4 v226, s[56:59], s69 offen offset:2048 lds\n" \
"v_mfma_f32_16x16x32_bf16 v[24:27], a[216:219], v[160:163], v[24:27]\n" \
"ds_read_b64_tr_b16 a[248:249], v215 offset:16384\n" \
"ds_read_b64_tr_b16 a[250:251], v215 offset:16896\n" \
"v_mfma_f32_16x16x32_bf16 v[24:27], a[220:223], v[168:171], v[24:27]\n" \
"v_cvt_pk_bf16_f32 v176, v0, v1\n" \
"v_cvt_pk_bf16_f32 v177, v2, v3\n" \
"v_mfma_f32_16x16x32_bf16 v[28:31], a[216:219], v[164:167], v[28:31]\n" \
"s_waitcnt vmcnt(15)\n" \
"ds_read_b64_tr_b16 a[252:253], v215 offset:20480\n" \
"ds_read_b64_tr_b16 a[254:255], v215 offset:20992\n" \
"v_mfma_f32_16x16x32_bf16 v[28:31], a[220:223], v[172:175], v[28:31]\n" \
"s_barrier\n" \
"buffer_load_dwordx4 v227, s[56:59], s69 offen offset:3072 lds\n" \
"v_mfma_f32_16x16x32_bf16 v[32:35], a[224:227], v[64:67], 0\n" \
"s_waitcnt lgkmcnt(6)\n" \
"ds_read_b64_tr_b16 a[192:193], v212 offset:32768\n" \
"ds_read_b64_tr_b16 a[194:195], v212 offset:33280\n" \
"v_mfma_f32_16x16x32_bf16 v[32:35], a[228:231], v[72:75], v[32:35]\n" \
"s_mov_b32 s68, 0\n" \
"v_cvt_pk_bf16_f32 v178, v8, v9\n" \
"v_cvt_pk_bf16_f32 v179, v10, v11\n" \
"v_mfma_f32_16x16x32_bf16 v[36:39], a[224:227], v[68:71], 0\n" \
"s_add_u32 m0, s50, 131072\n" \
"ds_read_b64_tr_b16 a[196:197], v212 offset:36864\n" \
"ds_read_b64_tr_b16 a[198:199], v212 offset:37376\n" \
"v_mfma_f32_16x16x32_bf16 v[36:39], a[228:231], v[76:79], v[36:39]\n" \
"buffer_load_dwordx4 v248, s[60:63], s68 offen offset:0 lds\n" \
"v_mfma_f32_16x16x32_bf16 v[40:43], a[232:235], v[64:67], 0\n" \
"ds_read_b64_tr_b16 a[200:201], v216 offset:0\n" \
"ds_read_b64_tr_b16 a[202:203], v216 offset:512\n" \
"v_mfma_f32_16x16x32_bf16 v[40:43], a[236:239], v[72:75], v[40:43]\n" \
"v_cvt_pk_bf16_f32 v180, v4, v5\n" \
"v_cvt_pk_bf16_f32 v181, v6, v7\n" \
"v_mfma_f32_16x16x32_bf16 v[44:47], a[232:235], v[68:71], 0\n" \
"ds_read_b64_tr_b16 a[204:205], v216 offset:4096\n" \
"ds_read_b64_tr_b16 a[206:207], v216 offset:4608\n" \
"v_mfma_f32_16x16x32_bf16 v[44:47], a[236:239], v[76:79], v[44:47]\n" \
"buffer_load_dwordx4 v249, s[60:63], s68 offen offset:1024 lds\n" \
"v_mfma_f32_16x16x32_bf16 v[48:51], a[240:243], v[64:67], 0\n" \
"s_waitcnt lgkmcnt(6)\n" \
"ds_read_b64_tr_b16 a[208:209], v213 offset:32768\n" \
"ds_read_b64_tr_b16 a[210:211], v213 offset:33280\n" \
"v_mfma_f32_16x16x32_bf16 v[48:51], a[244:247], v[72:75], v[48:51]\n" \
"v_cvt_pk_bf16_f32 v182, v12, v13\n" \
"v_cvt_pk_bf16_f32 v183, v14, v15\n" \
"v_mfma_f32_16x16x32_bf16 v[52:55], a[240:243], v[68:71], 0\n" \
"ds_read_b64_tr_b16 a[212:213], v213 offset:36864\n" \
"ds_read_b64_tr_b16 a[214:215], v213 offset:37376\n" \
"v_mfma_f32_16x16x32_bf16 v[52:55], a[244:247], v[76:79], v[52:55]\n" \
"buffer_load_dwordx4 v250, s[60:63], s68 offen offset:2048 lds\n" \
"v_mfma_f32_16x16x32_bf16 v[56:59], a[248:251], v[64:67], 0\n" \
"ds_read_b64_tr_b16 a[216:217], v217 offset:0\n" \
"ds_read_b64_tr_b16 a[218:219], v217 offset:512\n" \
"v_mfma_f32_16x16x32_bf16 v[56:59], a[252:255], v[72:75], v[56:59]\n" \
"v_permlane32_swap_b32 v176, v178\n" \
"v_permlane32_swap_b32 v177, v179\n" \
"v_mfma_f32_16x16x32_bf16 v[60:63], a[248:251], v[68:71], 0\n" \
"ds_read_b64_tr_b16 a[220:221], v217 offset:4096\n" \
"ds_read_b64_tr_b16 a[222:223], v217 offset:4608\n" \
"v_mfma_f32_16x16x32_bf16 v[60:63], a[252:255], v[76:79], v[60:63]\n" \
"buffer_load_dwordx4 v251, s[60:63], s68 offen offset:3072 lds\n" \
"v_mfma_f32_16x16x32_bf16 v[32:35], a[192:195], v[80:83], v[32:35]\n" \
"s_waitcnt lgkmcnt(6)\n" \
"ds_read_b64_tr_b16 a[224:225], v214 offset:32768\n" \
"ds_read_b64_tr_b16 a[226:227], v214 offset:33280\n" \
"v_mfma_f32_16x16x32_bf16 v[32:35], a[196:199], v[88:91], v[32:35]\n" \
"s_mov_b32 s69, 114688\n" \
"v_permlane32_swap_b32 v180, v182\n" \
"v_permlane32_swap_b32 v181, v183\n" \
"v_mfma_f32_16x16x32_bf16 v[32:35], a[200:203], v[96:99], v[32:35]\n" \
"s_add_u32 m0, s50, 147456\n" \
"ds_read_b64_tr_b16 a[228:229], v214 offset:36864\n" \
"ds_read_b64_tr_b16 a[230:231], v214 offset:37376\n" \
"v_mfma_f32_16x16x32_bf16 v[32:35], a[204:207], v[104:107], v[32:35]\n" \
"buffer_load_dwordx4 v248, s[60:63], s69 offen offset:0 lds\n" \
"v_mfma_f32_16x16x32_bf16 v[36:39], a[192:195], v[84:87], v[36:39]\n" \
"ds_read_b64_tr_b16 a[232:233], v218 offset:0\n" \
"ds_read_b64_tr_b16 a[234:235], v218 offset:512\n" \
"v_mfma_f32_16x16x32_bf16 v[36:39], a[196:199], v[92:95], v[36:39]\n" \
"v_permlane16_swap_b32 v176, v178\n" \
"v_permlane16_swap_b32 v177, v179\n" \
"v_mfma_f32_16x16x32_bf16 v[36:39], a[200:203], v[100:103], v[36:39]\n" \
"ds_read_b64_tr_b16 a[236:237], v218 offset:4096\n" \
"ds_read_b64_tr_b16 a[238:239], v218 offset:4608\n" \
"v_mfma_f32_16x16x32_bf16 v[36:39], a[204:207], v[108:111], v[36:39]\n" \
"buffer_load_dwordx4 v249, s[60:63], s69 offen offset:1024 lds\n" \
"v_mfma_f32_16x16x32_bf16 v[40:43], a[208:211], v[80:83], v[40:43]\n" \
"s_waitcnt lgkmcnt(6)\n" \
"ds_read_b64_tr_b16 a[240:241], v215 offset:32768\n" \
"ds_read_b64_tr_b16 a[242:243], v215 offset:33280\n" \
"v_mfma_f32_16x16x32_bf16 v[40:43], a[212:215], v[88:91], v[40:43]\n" \
"v_permlane16_swap_b32 v180, v182\n" \
"v_permlane16_swap_b32 v181, v183\n" \
"v_mfma_f32_16x16x32_bf16 v[40:43], a[216:219], v[96:99], v[40:43]\n" \
"ds_read_b64_tr_b16 a[244:245], v215 offset:36864\n" \
"ds_read_b64_tr_b16 a[246:247], v215 offset:37376\n" \
"v_mfma_f32_16x16x32_bf16 v[40:43], a[220:223], v[104:107], v[40:43]\n" \
"buffer_load_dwordx4 v250, s[60:63], s69 offen offset:2048 lds\n" \
"v_mfma_f32_16x16x32_bf16 v[44:47], a[208:211], v[84:87], v[44:47]\n" \
"ds_read_b64_tr_b16 a[248:249], v219 offset:0\n" \
"ds_read_b64_tr_b16 a[250:251], v219 offset:512\n" \
"v_mfma_f32_16x16x32_bf16 v[44:47], a[212:215], v[92:95], v[44:47]\n" \
"v_accvgpr_write_b32 a160, v176\n" \
"v_accvgpr_write_b32 a161, v177\n" \
"v_mfma_f32_16x16x32_bf16 v[44:47], a[216:219], v[100:103], v[44:47]\n" \
"s_waitcnt vmcnt(15)\n" \
"ds_read_b64_tr_b16 a[252:253], v219 offset:4096\n" \
"ds_read_b64_tr_b16 a[254:255], v219 offset:4608\n" \
"v_mfma_f32_16x16x32_bf16 v[44:47], a[220:223], v[108:111], v[44:47]\n" \
"s_barrier\n" \
"buffer_load_dwordx4 v251, s[60:63], s69 offen offset:3072 lds\n" \
"v_mfma_f32_16x16x32_bf16 v[48:51], a[224:227], v[80:83], v[48:51]\n" \
"s_waitcnt lgkmcnt(6)\n" \
"ds_read_b64_tr_b16 a[192:193], v212 offset:49152\n" \
"ds_read_b64_tr_b16 a[194:195], v212 offset:49664\n" \
"v_mfma_f32_16x16x32_bf16 v[48:51], a[228:231], v[88:91], v[48:51]\n" \
"s_mov_b32 s68, 0\n" \
"v_accvgpr_write_b32 a164, v180\n" \
"v_accvgpr_write_b32 a165, v181\n" \
"v_mfma_f32_16x16x32_bf16 v[48:51], a[232:235], v[96:99], v[48:51]\n" \
"s_add_u32 m0, s50, 0\n" \
"ds_read_b64_tr_b16 a[196:197], v212 offset:53248\n" \
"ds_read_b64_tr_b16 a[198:199], v212 offset:53760\n" \
"v_mfma_f32_16x16x32_bf16 v[48:51], a[236:239], v[104:107], v[48:51]\n" \
"buffer_load_dwordx4 v204, s[40:43], s68 offen offset:0 lds\n" \
"v_mfma_f32_16x16x32_bf16 v[52:55], a[224:227], v[84:87], v[52:55]\n" \
"ds_read_b64_tr_b16 a[200:201], v216 offset:16384\n" \
"ds_read_b64_tr_b16 a[202:203], v216 offset:16896\n" \
"v_mfma_f32_16x16x32_bf16 v[52:55], a[228:231], v[92:95], v[52:55]\n" \
"v_accvgpr_write_b32 a162, v178\n" \
"v_accvgpr_write_b32 a163, v179\n" \
"v_mfma_f32_16x16x32_bf16 v[52:55], a[232:235], v[100:103], v[52:55]\n" \
"ds_read_b64_tr_b16 a[204:205], v216 offset:20480\n" \
"ds_read_b64_tr_b16 a[206:207], v216 offset:20992\n" \
"v_mfma_f32_16x16x32_bf16 v[52:55], a[236:239], v[108:111], v[52:55]\n" \
"buffer_load_dwordx4 v205, s[40:43], s68 offen offset:1024 lds\n" \
"v_mfma_f32_16x16x32_bf16 v[56:59], a[240:243], v[80:83], v[56:59]\n" \
"s_waitcnt lgkmcnt(6)\n" \
"ds_read_b64_tr_b16 a[208:209], v213 offset:49152\n" \
"ds_read_b64_tr_b16 a[210:211], v213 offset:49664\n" \
"v_mfma_f32_16x16x32_bf16 v[56:59], a[244:247], v[88:91], v[56:59]\n" \
"v_accvgpr_write_b32 a166, v182\n" \
"v_accvgpr_write_b32 a167, v183\n" \
"v_mfma_f32_16x16x32_bf16 v[56:59], a[248:251], v[96:99], v[56:59]\n" \
"ds_read_b64_tr_b16 a[212:213], v213 offset:53248\n" \
"ds_read_b64_tr_b16 a[214:215], v213 offset:53760\n" \
"v_mfma_f32_16x16x32_bf16 v[56:59], a[252:255], v[104:107], v[56:59]\n" \
"buffer_load_dwordx4 v206, s[40:43], s68 offen offset:2048 lds\n" \
"v_mfma_f32_16x16x32_bf16 v[60:63], a[240:243], v[84:87], v[60:63]\n" \
"ds_read_b64_tr_b16 a[216:217], v217 offset:16384\n" \
"ds_read_b64_tr_b16 a[218:219], v217 offset:16896\n" \
"v_mfma_f32_16x16x32_bf16 v[60:63], a[244:247], v[92:95], v[60:63]\n" \
"v_cvt_pk_bf16_f32 v232, v16, v17\n" \
"v_cvt_pk_bf16_f32 v233, v18, v19\n" \
"v_mfma_f32_16x16x32_bf16 v[60:63], a[248:251], v[100:103], v[60:63]\n" \
"ds_read_b64_tr_b16 a[220:221], v217 offset:20480\n" \
"ds_read_b64_tr_b16 a[222:223], v217 offset:20992\n" \
"v_mfma_f32_16x16x32_bf16 v[60:63], a[252:255], v[108:111], v[60:63]\n" \
"buffer_load_dwordx4 v207, s[40:43], s68 offen offset:3072 lds\n" \
"v_mfma_f32_16x16x32_bf16 v[32:35], a[192:195], v[112:115], v[32:35]\n" \
"s_waitcnt lgkmcnt(6)\n" \
"ds_read_b64_tr_b16 a[224:225], v214 offset:49152\n" \
"ds_read_b64_tr_b16 a[226:227], v214 offset:49664\n" \
"v_mfma_f32_16x16x32_bf16 v[32:35], a[196:199], v[120:123], v[32:35]\n" \
"s_mov_b32 s69, 16384\n" \
"v_cvt_pk_bf16_f32 v234, v24, v25\n" \
"v_cvt_pk_bf16_f32 v235, v26, v27\n" \
"v_mfma_f32_16x16x32_bf16 v[32:35], a[200:203], v[128:131], v[32:35]\n" \
"s_add_u32 m0, s50, 16384\n" \
"ds_read_b64_tr_b16 a[228:229], v214 offset:53248\n" \
"ds_read_b64_tr_b16 a[230:231], v214 offset:53760\n" \
"v_mfma_f32_16x16x32_bf16 v[32:35], a[204:207], v[136:139], v[32:35]\n" \
"buffer_load_dwordx4 v204, s[40:43], s69 offen offset:0 lds\n" \
"v_mfma_f32_16x16x32_bf16 v[36:39], a[192:195], v[116:119], v[36:39]\n" \
"ds_read_b64_tr_b16 a[232:233], v218 offset:16384\n" \
"ds_read_b64_tr_b16 a[234:235], v218 offset:16896\n" \
"v_mfma_f32_16x16x32_bf16 v[36:39], a[196:199], v[124:127], v[36:39]\n" \
"v_cvt_pk_bf16_f32 v236, v20, v21\n" \
"v_cvt_pk_bf16_f32 v237, v22, v23\n" \
"v_mfma_f32_16x16x32_bf16 v[36:39], a[200:203], v[132:135], v[36:39]\n" \
"ds_read_b64_tr_b16 a[236:237], v218 offset:20480\n" \
"ds_read_b64_tr_b16 a[238:239], v218 offset:20992\n" \
"v_mfma_f32_16x16x32_bf16 v[36:39], a[204:207], v[140:143], v[36:39]\n" \
"buffer_load_dwordx4 v205, s[40:43], s69 offen offset:1024 lds\n" \
"v_mfma_f32_16x16x32_bf16 v[40:43], a[208:211], v[112:115], v[40:43]\n" \
"s_waitcnt lgkmcnt(6)\n" \
"ds_read_b64_tr_b16 a[240:241], v215 offset:49152\n" \
"ds_read_b64_tr_b16 a[242:243], v215 offset:49664\n" \
"v_mfma_f32_16x16x32_bf16 v[40:43], a[212:215], v[120:123], v[40:43]\n" \
"v_cvt_pk_bf16_f32 v238, v28, v29\n" \
"v_cvt_pk_bf16_f32 v239, v30, v31\n" \
"v_mfma_f32_16x16x32_bf16 v[40:43], a[216:219], v[128:131], v[40:43]\n" \
"ds_read_b64_tr_b16 a[244:245], v215 offset:53248\n" \
"ds_read_b64_tr_b16 a[246:247], v215 offset:53760\n" \
"v_mfma_f32_16x16x32_bf16 v[40:43], a[220:223], v[136:139], v[40:43]\n" \
"buffer_load_dwordx4 v206, s[40:43], s69 offen offset:2048 lds\n" \
"v_mfma_f32_16x16x32_bf16 v[44:47], a[208:211], v[116:119], v[44:47]\n" \
"ds_read_b64_tr_b16 a[248:249], v219 offset:16384\n" \
"ds_read_b64_tr_b16 a[250:251], v219 offset:16896\n" \
"v_mfma_f32_16x16x32_bf16 v[44:47], a[212:215], v[124:127], v[44:47]\n" \
"v_permlane32_swap_b32 v232, v234\n" \
"v_permlane32_swap_b32 v233, v235\n" \
"v_mfma_f32_16x16x32_bf16 v[44:47], a[216:219], v[132:135], v[44:47]\n" \
"s_waitcnt vmcnt(15)\n" \
"ds_read_b64_tr_b16 a[252:253], v219 offset:20480\n" \
"ds_read_b64_tr_b16 a[254:255], v219 offset:20992\n" \
"v_mfma_f32_16x16x32_bf16 v[44:47], a[220:223], v[140:143], v[44:47]\n" \
"s_barrier\n" \
"buffer_load_dwordx4 v207, s[40:43], s69 offen offset:3072 lds\n" \
"v_mfma_f32_16x16x32_bf16 v[48:51], a[224:227], v[112:115], v[48:51]\n" \
"s_waitcnt lgkmcnt(6)\n" \
"ds_read_b64_tr_b16 a[192:193], v216 offset:32768\n" \
"ds_read_b64_tr_b16 a[194:195], v216 offset:33280\n" \
"v_mfma_f32_16x16x32_bf16 v[48:51], a[228:231], v[120:123], v[48:51]\n" \
"s_mov_b32 s68, 0\n" \
"v_permlane32_swap_b32 v236, v238\n" \
"v_permlane32_swap_b32 v237, v239\n" \
"v_mfma_f32_16x16x32_bf16 v[48:51], a[232:235], v[128:131], v[48:51]\n" \
"s_add_u32 m0, s50, 65536\n" \
"ds_read_b64_tr_b16 a[196:197], v216 offset:36864\n" \
"ds_read_b64_tr_b16 a[198:199], v216 offset:37376\n" \
"v_mfma_f32_16x16x32_bf16 v[48:51], a[236:239], v[136:139], v[48:51]\n" \
"buffer_load_dwordx4 v208, s[44:47], s68 offen offset:0 lds\n" \
"v_mfma_f32_16x16x32_bf16 v[52:55], a[224:227], v[116:119], v[52:55]\n" \
"ds_read_b64_tr_b16 a[200:201], v216 offset:49152\n" \
"ds_read_b64_tr_b16 a[202:203], v216 offset:49664\n" \
"v_mfma_f32_16x16x32_bf16 v[52:55], a[228:231], v[124:127], v[52:55]\n" \
"v_permlane16_swap_b32 v232, v234\n" \
"v_permlane16_swap_b32 v233, v235\n" \
"v_mfma_f32_16x16x32_bf16 v[52:55], a[232:235], v[132:135], v[52:55]\n" \
"ds_read_b64_tr_b16 a[204:205], v216 offset:53248\n" \
"ds_read_b64_tr_b16 a[206:207], v216 offset:53760\n" \
"v_mfma_f32_16x16x32_bf16 v[52:55], a[236:239], v[140:143], v[52:55]\n" \
"buffer_load_dwordx4 v209, s[44:47], s68 offen offset:1024 lds\n" \
"v_mfma_f32_16x16x32_bf16 v[56:59], a[240:243], v[112:115], v[56:59]\n" \
"s_waitcnt lgkmcnt(6)\n" \
"ds_read_b64_tr_b16 a[208:209], v217 offset:32768\n" \
"ds_read_b64_tr_b16 a[210:211], v217 offset:33280\n" \
"v_mfma_f32_16x16x32_bf16 v[56:59], a[244:247], v[120:123], v[56:59]\n" \
"v_permlane16_swap_b32 v236, v238\n" \
"v_permlane16_swap_b32 v237, v239\n" \
"v_mfma_f32_16x16x32_bf16 v[56:59], a[248:251], v[128:131], v[56:59]\n" \
"ds_read_b64_tr_b16 a[212:213], v217 offset:36864\n" \
"ds_read_b64_tr_b16 a[214:215], v217 offset:37376\n" \
"v_mfma_f32_16x16x32_bf16 v[56:59], a[252:255], v[136:139], v[56:59]\n" \
"buffer_load_dwordx4 v210, s[44:47], s68 offen offset:2048 lds\n" \
"v_mfma_f32_16x16x32_bf16 v[60:63], a[240:243], v[116:119], v[60:63]\n" \
"ds_read_b64_tr_b16 a[216:217], v217 offset:49152\n" \
"ds_read_b64_tr_b16 a[218:219], v217 offset:49664\n" \
"v_mfma_f32_16x16x32_bf16 v[60:63], a[244:247], v[124:127], v[60:63]\n" \
"v_accvgpr_write_b32 a168, v232\n" \
"v_accvgpr_write_b32 a169, v233\n" \
"v_mfma_f32_16x16x32_bf16 v[60:63], a[248:251], v[132:135], v[60:63]\n" \
"ds_read_b64_tr_b16 a[220:221], v217 offset:53248\n" \
"ds_read_b64_tr_b16 a[222:223], v217 offset:53760\n" \
"v_mfma_f32_16x16x32_bf16 v[60:63], a[252:255], v[140:143], v[60:63]\n" \
"buffer_load_dwordx4 v211, s[44:47], s68 offen offset:3072 lds\n" \
"v_mfma_f32_16x16x32_bf16 v[32:35], a[192:195], v[144:147], v[32:35]\n" \
"s_waitcnt lgkmcnt(6)\n" \
"ds_read_b64_tr_b16 a[224:225], v218 offset:32768\n" \
"ds_read_b64_tr_b16 a[226:227], v218 offset:33280\n" \
"v_mfma_f32_16x16x32_bf16 v[32:35], a[196:199], v[152:155], v[32:35]\n" \
"s_mov_b32 s69, 32768\n" \
"v_accvgpr_write_b32 a172, v236\n" \
"v_accvgpr_write_b32 a173, v237\n" \
"v_mfma_f32_16x16x32_bf16 v[32:35], a[200:203], v[160:163], v[32:35]\n" \
"s_add_u32 m0, s50, 32768\n" \
"ds_read_b64_tr_b16 a[228:229], v218 offset:36864\n" \
"ds_read_b64_tr_b16 a[230:231], v218 offset:37376\n" \
"v_mfma_f32_16x16x32_bf16 v[32:35], a[204:207], v[168:171], v[32:35]\n" \
"buffer_load_dwordx4 v204, s[40:43], s69 offen offset:0 lds\n" \
"v_mfma_f32_16x16x32_bf16 v[36:39], a[192:195], v[148:151], v[36:39]\n" \
"ds_read_b64_tr_b16 a[232:233], v218 offset:49152\n" \
"ds_read_b64_tr_b16 a[234:235], v218 offset:49664\n" \
"v_mfma_f32_16x16x32_bf16 v[36:39], a[196:199], v[156:159], v[36:39]\n" \
"v_accvgpr_write_b32 a170, v234\n" \
"v_accvgpr_write_b32 a171, v235\n" \
"v_mfma_f32_16x16x32_bf16 v[36:39], a[200:203], v[164:167], v[36:39]\n" \
"ds_read_b64_tr_b16 a[236:237], v218 offset:53248\n" \
"ds_read_b64_tr_b16 a[238:239], v218 offset:53760\n" \
"v_mfma_f32_16x16x32_bf16 v[36:39], a[204:207], v[172:175], v[36:39]\n" \
"buffer_load_dwordx4 v205, s[40:43], s69 offen offset:1024 lds\n" \
"v_mfma_f32_16x16x32_bf16 v[40:43], a[208:211], v[144:147], v[40:43]\n" \
"s_waitcnt lgkmcnt(6)\n" \
"ds_read_b64_tr_b16 a[240:241], v219 offset:32768\n" \
"ds_read_b64_tr_b16 a[242:243], v219 offset:33280\n" \
"v_mfma_f32_16x16x32_bf16 v[40:43], a[212:215], v[152:155], v[40:43]\n" \
"v_accvgpr_write_b32 a174, v238\n" \
"v_accvgpr_write_b32 a175, v239\n" \
"v_mfma_f32_16x16x32_bf16 v[40:43], a[216:219], v[160:163], v[40:43]\n" \
"ds_read_b64_tr_b16 a[244:245], v219 offset:36864\n" \
"ds_read_b64_tr_b16 a[246:247], v219 offset:37376\n" \
"v_mfma_f32_16x16x32_bf16 v[40:43], a[220:223], v[168:171], v[40:43]\n" \
"buffer_load_dwordx4 v206, s[40:43], s69 offen offset:2048 lds\n" \
"v_mfma_f32_16x16x32_bf16 v[44:47], a[208:211], v[148:151], v[44:47]\n" \
"ds_read_b64_tr_b16 a[248:249], v219 offset:49152\n" \
"ds_read_b64_tr_b16 a[250:251], v219 offset:49664\n" \
"v_mfma_f32_16x16x32_bf16 v[44:47], a[212:215], v[156:159], v[44:47]\n" \
"v_mfma_f32_16x16x32_bf16 v[44:47], a[216:219], v[164:167], v[44:47]\n" \
"ds_read_b64_tr_b16 a[252:253], v219 offset:53248\n" \
"ds_read_b64_tr_b16 a[254:255], v219 offset:53760\n" \
"v_mfma_f32_16x16x32_bf16 v[44:47], a[220:223], v[172:175], v[44:47]\n" \
"buffer_load_dwordx4 v207, s[40:43], s69 offen offset:3072 lds\n" \
"v_mfma_f32_16x16x32_bf16 v[48:51], a[224:227], v[144:147], v[48:51]\n" \
"s_waitcnt lgkmcnt(6)\n" \
"v_mfma_f32_16x16x32_bf16 v[48:51], a[228:231], v[152:155], v[48:51]\n" \
"v_mfma_f32_16x16x32_bf16 v[48:51], a[232:235], v[160:163], v[48:51]\n" \
"v_mfma_f32_16x16x32_bf16 v[48:51], a[236:239], v[168:171], v[48:51]\n" \
"v_cvt_pk_bf16_f32 v176, v32, v33\n" \
"v_cvt_pk_bf16_f32 v177, v34, v35\n" \
"v_mfma_f32_16x16x32_bf16 v[52:55], a[224:227], v[148:151], v[52:55]\n" \
"v_cvt_pk_bf16_f32 v178, v40, v41\n" \
"v_cvt_pk_bf16_f32 v179, v42, v43\n" \
"v_mfma_f32_16x16x32_bf16 v[52:55], a[228:231], v[156:159], v[52:55]\n" \
"v_cvt_pk_bf16_f32 v180, v36, v37\n" \
"v_cvt_pk_bf16_f32 v181, v38, v39\n" \
"v_mfma_f32_16x16x32_bf16 v[52:55], a[232:235], v[164:167], v[52:55]\n" \
"v_cvt_pk_bf16_f32 v182, v44, v45\n" \
"v_cvt_pk_bf16_f32 v183, v46, v47\n" \
"v_mfma_f32_16x16x32_bf16 v[52:55], a[236:239], v[172:175], v[52:55]\n" \
"v_permlane32_swap_b32 v176, v178\n" \
"v_permlane32_swap_b32 v177, v179\n" \
"v_mfma_f32_16x16x32_bf16 v[56:59], a[240:243], v[144:147], v[56:59]\n" \
"s_waitcnt lgkmcnt(0)\n" \
"v_permlane32_swap_b32 v180, v182\n" \
"v_permlane32_swap_b32 v181, v183\n" \
"v_mfma_f32_16x16x32_bf16 v[56:59], a[244:247], v[152:155], v[56:59]\n" \
"v_permlane16_swap_b32 v176, v178\n" \
"v_permlane16_swap_b32 v177, v179\n" \
"v_mfma_f32_16x16x32_bf16 v[56:59], a[248:251], v[160:163], v[56:59]\n" \
"v_permlane16_swap_b32 v180, v182\n" \
"v_permlane16_swap_b32 v181, v183\n" \
"v_mfma_f32_16x16x32_bf16 v[56:59], a[252:255], v[168:171], v[56:59]\n" \
"v_accvgpr_write_b32 a176, v176\n" \
"v_accvgpr_write_b32 a177, v177\n" \
"v_mfma_f32_16x16x32_bf16 v[60:63], a[240:243], v[148:151], v[60:63]\n" \
"v_accvgpr_write_b32 a180, v180\n" \
"v_accvgpr_write_b32 a181, v181\n" \
"v_mfma_f32_16x16x32_bf16 v[60:63], a[244:247], v[156:159], v[60:63]\n" \
"v_accvgpr_write_b32 a178, v178\n" \
"v_accvgpr_write_b32 a179, v179\n" \
"v_mfma_f32_16x16x32_bf16 v[60:63], a[248:251], v[164:167], v[60:63]\n" \
"v_accvgpr_write_b32 a182, v182\n" \
"v_accvgpr_write_b32 a183, v183\n" \
"v_mfma_f32_16x16x32_bf16 v[60:63], a[252:255], v[172:175], v[60:63]\n" \
"s_nop 3\n" \
"v_mov_b32 v218, 0\n" \
"v_mov_b32 v219, 0\n" \
"v_mov_b32 v220, 0\n" \
"v_mov_b32 v221, 0\n" \
"v_mov_b32 v160, 0\n" \
"v_mov_b32 v161, 0\n" \
"v_mov_b32 v162, 0\n" \
"v_mov_b32 v163, 0\n" \
"v_mov_b32 v164, 0\n" \
"v_mov_b32 v165, 0\n" \
"v_mov_b32 v166, 0\n" \
"v_mov_b32 v167, 0\n" \
"v_cvt_pk_bf16_f32 v232, v48, v49\n" \
"v_cvt_pk_bf16_f32 v233, v50, v51\n" \
"v_cvt_pk_bf16_f32 v234, v56, v57\n" \
"v_cvt_pk_bf16_f32 v235, v58, v59\n" \
"v_cvt_pk_bf16_f32 v236, v52, v53\n" \
"v_cvt_pk_bf16_f32 v237, v54, v55\n" \
"v_cvt_pk_bf16_f32 v238, v60, v61\n" \
"v_cvt_pk_bf16_f32 v239, v62, v63\n" \
"v_permlane32_swap_b32 v232, v234\n" \
"v_permlane32_swap_b32 v233, v235\n" \
"v_permlane32_swap_b32 v236, v238\n" \
"v_permlane32_swap_b32 v237, v239\n" \
"v_permlane16_swap_b32 v232, v234\n" \
"v_permlane16_swap_b32 v233, v235\n" \
"v_permlane16_swap_b32 v236, v238\n" \
"v_permlane16_swap_b32 v237, v239\n" \
"v_accvgpr_write_b32 a184, v232\n" \
"v_accvgpr_write_b32 a185, v233\n" \
"v_accvgpr_write_b32 a188, v236\n" \
"v_accvgpr_write_b32 a189, v237\n" \
"v_accvgpr_write_b32 a186, v234\n" \
"v_accvgpr_write_b32 a187, v235\n" \
"v_accvgpr_write_b32 a190, v238\n" \
"v_accvgpr_write_b32 a191, v239\n" \
"v_mov_b32 v144, 0xff800000\n" \
"v_mov_b32 v145, 0xff800000\n" \
"v_mov_b32 v146, 0xff800000\n" \
"v_mov_b32 v147, 0xff800000\n" \
"v_mov_b32 v148, 0xff800000\n" \
"v_mov_b32 v149, 0xff800000\n" \
"v_mov_b32 v150, 0xff800000\n" \
"v_mov_b32 v151, 0xff800000\n" \
"v_mov_b32 v152, 0xff800000\n" \
"v_mov_b32 v153, 0xff800000\n" \
"v_mov_b32 v154, 0xff800000\n" \
"v_mov_b32 v155, 0xff800000\n" \
"v_mov_b32 v156, 0xff800000\n" \
"v_mov_b32 v157, 0xff800000\n" \
"v_mov_b32 v158, 0xff800000\n" \
"v_mov_b32 v159, 0xff800000\n" \
"v_mov_b32 v226, 0\n" \
"v_mov_b32 v227, 0\n" \
"v_mov_b32 v228, 0\n" \
"v_mov_b32 v229, 0\n" \
"v_mov_b32 v230, 0\n" \
"v_mov_b32 v231, 0\n" \
"v_mov_b32 v232, 0\n" \
"v_mov_b32 v233, 0\n" \
"v_mov_b32 v234, 0\n" \
"v_mov_b32 v235, 0\n" \
"v_mov_b32 v236, 0\n" \
"v_mov_b32 v237, 0\n" \
"v_mov_b32 v238, 0\n" \
"v_mov_b32 v239, 0\n" \
"v_mov_b32 v240, 0\n" \
"v_mov_b32 v241, 0\n" \
"v_mov_b32 v212, 0\n" \
"v_mov_b32 v213, 0\n" \
"v_mov_b32 v214, 0\n" \
"v_mov_b32 v215, 0\n" \
"v_mov_b32 v216, 0\n" \
"v_mov_b32 v217, 0\n" \
"v_accvgpr_read_b32 v52, a6\n" \
"v_accvgpr_read_b32 v53, a7\n" \
"v_accvgpr_read_b32 v54, a8\n" \
"v_accvgpr_read_b32 v55, a9\n" \
"v_accvgpr_read_b32 v57, a10\n" \
"v_accvgpr_read_b32 v61, a11\n" \
"v_accvgpr_read_b32 v62, a12\n" \
"v_mfma_f32_32x32x16_bf16 a[0:15], v[218:221], v[218:221], 0\n" \
"v_mfma_f32_32x32x16_bf16 a[16:31], v[218:221], v[218:221], 0\n" \
"v_mfma_f32_32x32x16_bf16 a[32:47], v[218:221], v[218:221], 0\n" \
"v_mfma_f32_32x32x16_bf16 a[48:63], v[218:221], v[218:221], 0\n" \
"v_mfma_f32_32x32x16_bf16 a[64:79], v[218:221], v[218:221], 0\n" \
"v_mfma_f32_32x32x16_bf16 a[80:95], v[218:221], v[218:221], 0\n" \
"v_mfma_f32_32x32x16_bf16 a[96:111], v[218:221], v[218:221], 0\n" \
"v_mfma_f32_32x32x16_bf16 a[112:127], v[218:221], v[218:221], 0\n" \
"s_mov_b32 s53, 0xbf800000\n" \
"s_mov_b32 s54, 0xff800000\n" \
"s_mov_b32 s48, 49152\n" \
"s_mov_b32 s49, 16384\n" \
"s_waitcnt vmcnt(12)\n" \
"s_barrier\n" \
"s_getpc_b64 s[80:81]\n" \
"L_pc_%=:\n" \
"s_add_u32 s80, s80, L_exit_%=-L_pc_%=\n" \
"s_addc_u32 s81, s81, 0\n" \
"s_and_b32 s81, s81, 0xffff\n" \
"s_mov_b32 s82, L_code_end-L_exit_%=\n" \
"s_mov_b32 s83, 0x20000\n" \
"s_mov_b32 s84, s26\n" \
"s_and_b32 s85, s27, 0xffff\n" \
"s_mov_b32 s86, 57344\n" \
"s_mov_b32 s87, 0x20000\n" \
"v_mov_b32 v58, v185\n" \
"v_mov_b32 v59, v187\n" \
"ds_read_b128 a[192:195], v192 offset:0\n" \
"ds_read_b128 a[196:199], v193 offset:0\n" \
"ds_read_b128 a[200:203], v194 offset:0\n" \
"ds_read_b128 a[204:207], v195 offset:0\n" \
"ds_read_b128 a[208:211], v192 offset:256\n" \
"ds_read_b128 a[212:215], v193 offset:256\n" \
"ds_read_b128 a[216:219], v194 offset:256\n" \
"ds_read_b128 a[220:223], v195 offset:256\n" \
"ds_write_b128 v255, v[218:221] offset:0\n" \
"ds_write_b128 v255, v[218:221] offset:1024\n" \
"ds_write_b128 v255, v[218:221] offset:2048\n" \
"ds_write_b128 v255, v[218:221] offset:3072\n" \
"s_waitcnt lgkmcnt(0)\n" \
".p2align 6\n" \
"L_loop_%=:\n" \
"v_mfma_f32_16x16x32_bf16 v[128:131], a[192:195], a[128:131], v[160:163]\n" \
"s_waitcnt lgkmcnt(6)\n" \
"v_exp_f32 v239, v157\n" \
"v_mfma_f32_16x16x32_bf16 v[128:131], a[196:199], a[136:139], v[128:131]\n" \
"s_waitcnt lgkmcnt(5)\n" \
"v_exp_f32 v240, v158\n" \
"v_mfma_f32_16x16x32_bf16 v[128:131], a[200:203], a[144:147], v[128:131]\n" \
"s_waitcnt lgkmcnt(4)\n" \
"v_exp_f32 v241, v159\n" \
"v_mfma_f32_16x16x32_bf16 v[128:131], a[204:207], a[152:155], v[128:131]\n" \
"s_waitcnt lgkmcnt(3)\n" \
"v_add_f32 v216, v216, v229\n" \
"v_add_f32 v217, v217, v233\n" \
"v_mfma_f32_16x16x32_bf16 v[128:131], a[208:211], a[160:163], v[128:131]\n" \
"s_waitcnt lgkmcnt(2)\n" \
"ds_read_b128 a[224:227], v192 offset:8192\n" \
"ds_read_b128 a[228:231], v193 offset:8192\n" \
"v_mfma_f32_16x16x32_bf16 v[128:131], a[212:215], a[168:171], v[128:131]\n" \
"s_waitcnt lgkmcnt(3)\n" \
"v_add_f32 v216, v216, v234\n" \
"v_add_f32 v217, v217, v238\n" \
"v_mfma_f32_16x16x32_bf16 v[128:131], a[216:219], a[176:179], v[128:131]\n" \
"s_waitcnt lgkmcnt(2)\n" \
"ds_read_b128 a[232:235], v194 offset:8192\n" \
"ds_read_b128 a[236:239], v195 offset:8192\n" \
"v_mfma_f32_16x16x32_bf16 v[128:131], a[220:223], a[184:187], v[128:131]\n" \
"v_add_f32 v216, v216, v235\n" \
"v_add_f32 v217, v217, v239\n" \
"v_mfma_f32_16x16x32_bf16 v[132:135], a[192:195], a[132:135], v[164:167]\n" \
"ds_read_b128 a[240:243], v192 offset:8448\n" \
"ds_read_b128 a[244:247], v193 offset:8448\n" \
"v_mfma_f32_16x16x32_bf16 v[132:135], a[196:199], a[140:143], v[132:135]\n" \
"v_add_f32 v216, v216, v236\n" \
"v_add_f32 v217, v217, v240\n" \
"v_mfma_f32_16x16x32_bf16 v[132:135], a[200:203], a[148:151], v[132:135]\n" \
"ds_read_b128 a[248:251], v194 offset:8448\n" \
"ds_read_b128 a[252:255], v195 offset:8448\n" \
"v_mfma_f32_16x16x32_bf16 v[132:135], a[204:207], a[156:159], v[132:135]\n" \
"v_add_f32 v216, v216, v237\n" \
"v_add_f32 v217, v217, v241\n" \
"v_mfma_f32_16x16x32_bf16 v[132:135], a[208:211], a[164:167], v[132:135]\n" \
"s_add_u32 m0, s51, 16384\n" \
"v_cvt_pk_bf16_f32 v176, v226, v227\n" \
"v_max_f32 v184, v216, v217\n" \
"v_mfma_f32_16x16x32_bf16 v[132:135], a[212:215], a[172:175], v[132:135]\n" \
"buffer_load_dwordx4 v208, s[44:47], s49 offen offset:0 lds\n" \
"v_mfma_f32_16x16x32_bf16 v[132:135], a[216:219], a[180:183], v[132:135]\n" \
"v_cvt_pk_bf16_f32 v177, v228, v229\n" \
"v_cmp_lt_f32 vcc, s53, v184\n" \
"v_mfma_f32_16x16x32_bf16 v[132:135], a[220:223], a[188:191], v[132:135]\n" \
"s_waitcnt lgkmcnt(7)\n" \
"buffer_load_dwordx4 v209, s[44:47], s49 offen offset:1024 lds\n" \
"v_mfma_f32_16x16x32_bf16 v[136:139], a[224:227], a[128:131], v[160:163]\n" \
"s_waitcnt lgkmcnt(6)\n" \
"v_cvt_pk_bf16_f32 v180, v230, v231\n" \
"v_cvt_pk_bf16_f32 v181, v232, v233\n" \
"v_mfma_f32_16x16x32_bf16 v[136:139], a[228:231], a[136:139], v[136:139]\n" \
"s_waitcnt lgkmcnt(5)\n" \
"buffer_load_dwordx4 v210, s[44:47], s49 offen offset:2048 lds\n" \
"v_mfma_f32_16x16x32_bf16 v[136:139], a[232:235], a[144:147], v[136:139]\n" \
"s_waitcnt lgkmcnt(4)\n" \
"v_cvt_pk_bf16_f32 v178, v234, v235\n" \
"v_cvt_pk_bf16_f32 v179, v236, v237\n" \
"v_mfma_f32_16x16x32_bf16 v[136:139], a[236:239], a[152:155], v[136:139]\n" \
"s_waitcnt lgkmcnt(0)\n" \
"buffer_load_dwordx4 v211, s[44:47], s49 offen offset:3072 lds\n" \
"v_mfma_f32_16x16x32_bf16 v[136:139], a[240:243], a[160:163], v[136:139]\n" \
"s_waitcnt vmcnt(12)\n" \
"v_cvt_pk_bf16_f32 v182, v238, v239\n" \
"v_cvt_pk_bf16_f32 v183, v240, v241\n" \
"v_mfma_f32_16x16x32_bf16 v[136:139], a[244:247], a[168:171], v[136:139]\n" \
"s_barrier\n" \
"v_mfma_f32_16x16x32_bf16 v[136:139], a[248:251], a[176:179], v[136:139]\n" \
"s_add_u32 m0, s50, 49152\n" \
"ds_read_b64_tr_b16 v[64:65], v196 offset:49152\n" \
"ds_read_b64_tr_b16 v[66:67], v196 offset:57344\n" \
"v_mfma_f32_16x16x32_bf16 v[136:139], a[252:255], a[184:187], v[136:139]\n" \
"buffer_load_dwordx4 v204, s[40:43], s48 offen offset:0 lds\n" \
"v_mfma_f32_16x16x32_bf16 v[140:143], a[224:227], a[132:135], v[164:167]\n" \
"ds_read_b64_tr_b16 v[68:69], v197 offset:49152\n" \
"ds_read_b64_tr_b16 v[70:71], v197 offset:57344\n" \
"v_mfma_f32_16x16x32_bf16 v[140:143], a[228:231], a[140:143], v[140:143]\n" \
"buffer_load_dwordx4 v205, s[40:43], s48 offen offset:1024 lds\n" \
"v_mfma_f32_16x16x32_bf16 v[140:143], a[232:235], a[148:151], v[140:143]\n" \
"ds_read_b64_tr_b16 v[72:73], v198 offset:49152\n" \
"ds_read_b64_tr_b16 v[74:75], v198 offset:57344\n" \
"v_mfma_f32_16x16x32_bf16 v[140:143], a[236:239], a[156:159], v[140:143]\n" \
"buffer_load_dwordx4 v206, s[40:43], s48 offen offset:2048 lds\n" \
"v_mfma_f32_16x16x32_bf16 v[140:143], a[240:243], a[164:167], v[140:143]\n" \
"s_add_u32 s49, s49, 16384\n" \
"ds_read_b64_tr_b16 v[76:77], v199 offset:49152\n" \
"ds_read_b64_tr_b16 v[78:79], v199 offset:57344\n" \
"v_mfma_f32_16x16x32_bf16 v[140:143], a[244:247], a[172:175], v[140:143]\n" \
"buffer_load_dwordx4 v207, s[40:43], s48 offen offset:3072 lds\n" \
"v_mfma_f32_16x16x32_bf16 v[140:143], a[248:251], a[180:183], v[140:143]\n" \
"s_waitcnt lgkmcnt(6)\n" \
"ds_read_b64_tr_b16 v[80:81], v200 offset:49152\n" \
"ds_read_b64_tr_b16 v[82:83], v200 offset:57344\n" \
"v_mfma_f32_16x16x32_bf16 v[140:143], a[252:255], a[188:191], v[140:143]\n" \
"s_cbranch_vccnz L_stub0_%=\n" \
"L_ret0_%=:\n" \
"v_mfma_f32_16x16x32_bf16 a[0:3], v[64:67], v[176:179], a[0:3]\n" \
"ds_read_b64_tr_b16 v[84:85], v201 offset:49152\n" \
"ds_read_b64_tr_b16 v[86:87], v201 offset:57344\n" \
"v_mfma_f32_16x16x32_bf16 a[4:7], v[64:67], v[180:183], a[4:7]\n" \
"s_waitcnt lgkmcnt(8)\n" \
"v_add_f32 v212, v212, v216\n" \
"v_add_f32 v213, v213, v217\n" \
"v_mfma_f32_16x16x32_bf16 a[8:11], v[68:71], v[176:179], a[8:11]\n" \
"s_add_u32 s48, s48, 16384\n" \
"ds_read_b64_tr_b16 v[88:89], v202 offset:49152\n" \
"ds_read_b64_tr_b16 v[90:91], v202 offset:57344\n" \
"v_mfma_f32_16x16x32_bf16 a[12:15], v[68:71], v[180:183], a[12:15]\n" \
"s_waitcnt lgkmcnt(8)\n" \
"v_exp_f32 v226, v128\n" \
"v_mfma_f32_16x16x32_bf16 a[16:19], v[72:75], v[176:179], a[16:19]\n" \
"ds_read_b64_tr_b16 v[92:93], v203 offset:49152\n" \
"ds_read_b64_tr_b16 v[94:95], v203 offset:57344\n" \
"v_mfma_f32_16x16x32_bf16 a[20:23], v[72:75], v[180:183], a[20:23]\n" \
"s_waitcnt lgkmcnt(8)\n" \
"v_exp_f32 v227, v129\n" \
"v_mfma_f32_16x16x32_bf16 a[24:27], v[76:79], v[176:179], a[24:27]\n" \
"ds_read_b64_tr_b16 v[96:97], v196 offset:49408\n" \
"ds_read_b64_tr_b16 v[98:99], v196 offset:57600\n" \
"v_mfma_f32_16x16x32_bf16 a[28:31], v[76:79], v[180:183], a[28:31]\n" \
"s_waitcnt lgkmcnt(8)\n" \
"v_exp_f32 v228, v130\n" \
"v_mfma_f32_16x16x32_bf16 a[32:35], v[80:83], v[176:179], a[32:35]\n" \
"ds_read_b64_tr_b16 v[100:101], v197 offset:49408\n" \
"ds_read_b64_tr_b16 v[102:103], v197 offset:57600\n" \
"v_mfma_f32_16x16x32_bf16 a[36:39], v[80:83], v[180:183], a[36:39]\n" \
"s_waitcnt lgkmcnt(8)\n" \
"v_exp_f32 v229, v131\n" \
"v_mfma_f32_16x16x32_bf16 a[40:43], v[84:87], v[176:179], a[40:43]\n" \
"ds_read_b64_tr_b16 v[104:105], v198 offset:49408\n" \
"ds_read_b64_tr_b16 v[106:107], v198 offset:57600\n" \
"v_mfma_f32_16x16x32_bf16 a[44:47], v[84:87], v[180:183], a[44:47]\n" \
"s_waitcnt lgkmcnt(8)\n" \
"v_exp_f32 v230, v132\n" \
"v_mfma_f32_16x16x32_bf16 a[48:51], v[88:91], v[176:179], a[48:51]\n" \
"ds_read_b64_tr_b16 v[108:109], v199 offset:49408\n" \
"ds_read_b64_tr_b16 v[110:111], v199 offset:57600\n" \
"v_mfma_f32_16x16x32_bf16 a[52:55], v[88:91], v[180:183], a[52:55]\n" \
"s_waitcnt lgkmcnt(8)\n" \
"v_exp_f32 v231, v133\n" \
"v_mfma_f32_16x16x32_bf16 a[56:59], v[92:95], v[176:179], a[56:59]\n" \
"ds_read_b64_tr_b16 v[112:113], v200 offset:49408\n" \
"ds_read_b64_tr_b16 v[114:115], v200 offset:57600\n" \
"v_mfma_f32_16x16x32_bf16 a[60:63], v[92:95], v[180:183], a[60:63]\n" \
"s_waitcnt lgkmcnt(8)\n" \
"v_exp_f32 v232, v134\n" \
"v_mfma_f32_16x16x32_bf16 a[64:67], v[96:99], v[176:179], a[64:67]\n" \
"ds_read_b64_tr_b16 v[116:117], v201 offset:49408\n" \
"ds_read_b64_tr_b16 v[118:119], v201 offset:57600\n" \
"v_mfma_f32_16x16x32_bf16 a[68:71], v[96:99], v[180:183], a[68:71]\n" \
"s_waitcnt lgkmcnt(8)\n" \
"v_exp_f32 v233, v135\n" \
"v_mfma_f32_16x16x32_bf16 a[72:75], v[100:103], v[176:179], a[72:75]\n" \
"ds_read_b64_tr_b16 v[120:121], v202 offset:49408\n" \
"ds_read_b64_tr_b16 v[122:123], v202 offset:57600\n" \
"v_mfma_f32_16x16x32_bf16 a[76:79], v[100:103], v[180:183], a[76:79]\n" \
"s_waitcnt lgkmcnt(8)\n" \
"v_add_f32 v216, v226, v227\n" \
"v_add_f32 v217, v230, v231\n" \
"v_mfma_f32_16x16x32_bf16 a[80:83], v[104:107], v[176:179], a[80:83]\n" \
"ds_read_b64_tr_b16 v[124:125], v203 offset:49408\n" \
"ds_read_b64_tr_b16 v[126:127], v203 offset:57600\n" \
"v_mfma_f32_16x16x32_bf16 a[84:87], v[104:107], v[180:183], a[84:87]\n" \
"s_waitcnt lgkmcnt(8)\n" \
"v_exp_f32 v234, v136\n" \
"v_mfma_f32_16x16x32_bf16 a[88:91], v[108:111], v[176:179], a[88:91]\n" \
"ds_read_b128 a[192:195], v192 offset:16384\n" \
"ds_read_b128 a[196:199], v193 offset:16384\n" \
"v_mfma_f32_16x16x32_bf16 a[92:95], v[108:111], v[180:183], a[92:95]\n" \
"s_waitcnt lgkmcnt(8)\n" \
"v_exp_f32 v235, v137\n" \
"v_mfma_f32_16x16x32_bf16 a[96:99], v[112:115], v[176:179], a[96:99]\n" \
"ds_read_b128 a[200:203], v194 offset:16384\n" \
"ds_read_b128 a[204:207], v195 offset:16384\n" \
"v_mfma_f32_16x16x32_bf16 a[100:103], v[112:115], v[180:183], a[100:103]\n" \
"s_waitcnt lgkmcnt(8)\n" \
"v_exp_f32 v236, v138\n" \
"v_mfma_f32_16x16x32_bf16 a[104:107], v[116:119], v[176:179], a[104:107]\n" \
"ds_read_b128 a[208:211], v192 offset:16640\n" \
"ds_read_b128 a[212:215], v193 offset:16640\n" \
"v_mfma_f32_16x16x32_bf16 a[108:111], v[116:119], v[180:183], a[108:111]\n" \
"s_waitcnt lgkmcnt(8)\n" \
"v_exp_f32 v237, v139\n" \
"v_mfma_f32_16x16x32_bf16 a[112:115], v[120:123], v[176:179], a[112:115]\n" \
"ds_read_b128 a[216:219], v194 offset:16640\n" \
"ds_read_b128 a[220:223], v195 offset:16640\n" \
"v_mfma_f32_16x16x32_bf16 a[116:119], v[120:123], v[180:183], a[116:119]\n" \
"s_waitcnt lgkmcnt(8)\n" \
"v_add_f32 v216, v216, v228\n" \
"v_add_f32 v217, v217, v232\n" \
"v_mfma_f32_16x16x32_bf16 a[120:123], v[124:127], v[176:179], a[120:123]\n" \
"s_waitcnt lgkmcnt(7)\n" \
"s_sub_u32 s52, s52, 1\n" \
"v_exp_f32 v238, v140\n" \
"v_mfma_f32_16x16x32_bf16 a[124:127], v[124:127], v[180:183], a[124:127]\n" \
"s_cbranch_scc1 L_exit_%=\n" \
"v_mfma_f32_16x16x32_bf16 v[144:147], a[192:195], a[128:131], v[160:163]\n" \
"s_waitcnt lgkmcnt(6)\n" \
"v_exp_f32 v239, v141\n" \
"v_mfma_f32_16x16x32_bf16 v[144:147], a[196:199], a[136:139], v[144:147]\n" \
"s_waitcnt lgkmcnt(5)\n" \
"v_exp_f32 v240, v142\n" \
"v_mfma_f32_16x16x32_bf16 v[144:147], a[200:203], a[144:147], v[144:147]\n" \
"s_waitcnt lgkmcnt(4)\n" \
"v_exp_f32 v241, v143\n" \
"v_mfma_f32_16x16x32_bf16 v[144:147], a[204:207], a[152:155], v[144:147]\n" \
"s_waitcnt lgkmcnt(3)\n" \
"v_add_f32 v216, v216, v229\n" \
"v_add_f32 v217, v217, v233\n" \
"v_mfma_f32_16x16x32_bf16 v[144:147], a[208:211], a[160:163], v[144:147]\n" \
"s_waitcnt lgkmcnt(2)\n" \
"ds_read_b128 a[224:227], v192 offset:24576\n" \
"ds_read_b128 a[228:231], v193 offset:24576\n" \
"v_mfma_f32_16x16x32_bf16 v[144:147], a[212:215], a[168:171], v[144:147]\n" \
"s_waitcnt lgkmcnt(3)\n" \
"v_add_f32 v216, v216, v234\n" \
"v_add_f32 v217, v217, v238\n" \
"v_mfma_f32_16x16x32_bf16 v[144:147], a[216:219], a[176:179], v[144:147]\n" \
"s_waitcnt lgkmcnt(2)\n" \
"ds_read_b128 a[232:235], v194 offset:24576\n" \
"ds_read_b128 a[236:239], v195 offset:24576\n" \
"v_mfma_f32_16x16x32_bf16 v[144:147], a[220:223], a[184:187], v[144:147]\n" \
"v_add_f32 v216, v216, v235\n" \
"v_add_f32 v217, v217, v239\n" \
"v_mfma_f32_16x16x32_bf16 v[148:151], a[192:195], a[132:135], v[164:167]\n" \
"ds_read_b128 a[240:243], v192 offset:24832\n" \
"ds_read_b128 a[244:247], v193 offset:24832\n" \
"v_mfma_f32_16x16x32_bf16 v[148:151], a[196:199], a[140:143], v[148:151]\n" \
"v_add_f32 v216, v216, v236\n" \
"v_add_f32 v217, v217, v240\n" \
"v_mfma_f32_16x16x32_bf16 v[148:151], a[200:203], a[148:151], v[148:151]\n" \
"ds_read_b128 a[248:251], v194 offset:24832\n" \
"ds_read_b128 a[252:255], v195 offset:24832\n" \
"v_mfma_f32_16x16x32_bf16 v[148:151], a[204:207], a[156:159], v[148:151]\n" \
"v_add_f32 v216, v216, v237\n" \
"v_add_f32 v217, v217, v241\n" \
"v_mfma_f32_16x16x32_bf16 v[148:151], a[208:211], a[164:167], v[148:151]\n" \
"s_add_u32 m0, s51, 32768\n" \
"v_cvt_pk_bf16_f32 v168, v226, v227\n" \
"v_max_f32 v184, v216, v217\n" \
"v_mfma_f32_16x16x32_bf16 v[148:151], a[212:215], a[172:175], v[148:151]\n" \
"buffer_load_dwordx4 v208, s[44:47], s49 offen offset:0 lds\n" \
"v_mfma_f32_16x16x32_bf16 v[148:151], a[216:219], a[180:183], v[148:151]\n" \
"v_cvt_pk_bf16_f32 v169, v228, v229\n" \
"v_cmp_lt_f32 vcc, s53, v184\n" \
"v_mfma_f32_16x16x32_bf16 v[148:151], a[220:223], a[188:191], v[148:151]\n" \
"s_waitcnt lgkmcnt(7)\n" \
"buffer_load_dwordx4 v209, s[44:47], s49 offen offset:1024 lds\n" \
"v_mfma_f32_16x16x32_bf16 v[152:155], a[224:227], a[128:131], v[160:163]\n" \
"s_waitcnt lgkmcnt(6)\n" \
"v_cvt_pk_bf16_f32 v172, v230, v231\n" \
"v_cvt_pk_bf16_f32 v173, v232, v233\n" \
"v_mfma_f32_16x16x32_bf16 v[152:155], a[228:231], a[136:139], v[152:155]\n" \
"s_waitcnt lgkmcnt(5)\n" \
"buffer_load_dwordx4 v210, s[44:47], s49 offen offset:2048 lds\n" \
"v_mfma_f32_16x16x32_bf16 v[152:155], a[232:235], a[144:147], v[152:155]\n" \
"s_waitcnt lgkmcnt(4)\n" \
"v_cvt_pk_bf16_f32 v170, v234, v235\n" \
"v_cvt_pk_bf16_f32 v171, v236, v237\n" \
"v_mfma_f32_16x16x32_bf16 v[152:155], a[236:239], a[152:155], v[152:155]\n" \
"s_waitcnt lgkmcnt(0)\n" \
"buffer_load_dwordx4 v211, s[44:47], s49 offen offset:3072 lds\n" \
"v_mfma_f32_16x16x32_bf16 v[152:155], a[240:243], a[160:163], v[152:155]\n" \
"s_waitcnt vmcnt(12)\n" \
"v_cvt_pk_bf16_f32 v174, v238, v239\n" \
"v_cvt_pk_bf16_f32 v175, v240, v241\n" \
"v_mfma_f32_16x16x32_bf16 v[152:155], a[244:247], a[168:171], v[152:155]\n" \
"s_barrier\n" \
"v_mfma_f32_16x16x32_bf16 v[152:155], a[248:251], a[176:179], v[152:155]\n" \
"s_add_u32 m0, s50, 0\n" \
"ds_read_b64_tr_b16 v[64:65], v196 offset:0\n" \
"ds_read_b64_tr_b16 v[66:67], v196 offset:8192\n" \
"v_mfma_f32_16x16x32_bf16 v[152:155], a[252:255], a[184:187], v[152:155]\n" \
"buffer_load_dwordx4 v204, s[40:43], s48 offen offset:0 lds\n" \
"v_mfma_f32_16x16x32_bf16 v[156:159], a[224:227], a[132:135], v[164:167]\n" \
"ds_read_b64_tr_b16 v[68:69], v197 offset:0\n" \
"ds_read_b64_tr_b16 v[70:71], v197 offset:8192\n" \
"v_mfma_f32_16x16x32_bf16 v[156:159], a[228:231], a[140:143], v[156:159]\n" \
"buffer_load_dwordx4 v205, s[40:43], s48 offen offset:1024 lds\n" \
"v_mfma_f32_16x16x32_bf16 v[156:159], a[232:235], a[148:151], v[156:159]\n" \
"ds_read_b64_tr_b16 v[72:73], v198 offset:0\n" \
"ds_read_b64_tr_b16 v[74:75], v198 offset:8192\n" \
"v_mfma_f32_16x16x32_bf16 v[156:159], a[236:239], a[156:159], v[156:159]\n" \
"buffer_load_dwordx4 v206, s[40:43], s48 offen offset:2048 lds\n" \
"v_mfma_f32_16x16x32_bf16 v[156:159], a[240:243], a[164:167], v[156:159]\n" \
"s_add_u32 s49, s49, 16384\n" \
"ds_read_b64_tr_b16 v[76:77], v199 offset:0\n" \
"ds_read_b64_tr_b16 v[78:79], v199 offset:8192\n" \
"v_mfma_f32_16x16x32_bf16 v[156:159], a[244:247], a[172:175], v[156:159]\n" \
"buffer_load_dwordx4 v207, s[40:43], s48 offen offset:3072 lds\n" \
"v_mfma_f32_16x16x32_bf16 v[156:159], a[248:251], a[180:183], v[156:159]\n" \
"s_waitcnt lgkmcnt(6)\n" \
"ds_read_b64_tr_b16 v[80:81], v200 offset:0\n" \
"ds_read_b64_tr_b16 v[82:83], v200 offset:8192\n" \
"v_mfma_f32_16x16x32_bf16 v[156:159], a[252:255], a[188:191], v[156:159]\n" \
"s_cbranch_vccnz L_stub1_%=\n" \
"L_ret1_%=:\n" \
"v_mfma_f32_16x16x32_bf16 a[0:3], v[64:67], v[168:171], a[0:3]\n" \
"ds_read_b64_tr_b16 v[84:85], v201 offset:0\n" \
"ds_read_b64_tr_b16 v[86:87], v201 offset:8192\n" \
"v_mfma_f32_16x16x32_bf16 a[4:7], v[64:67], v[172:175], a[4:7]\n" \
"s_waitcnt lgkmcnt(8)\n" \
"v_add_f32 v212, v212, v216\n" \
"v_add_f32 v213, v213, v217\n" \
"v_mfma_f32_16x16x32_bf16 a[8:11], v[68:71], v[168:171], a[8:11]\n" \
"s_add_u32 s48, s48, 16384\n" \
"ds_read_b64_tr_b16 v[88:89], v202 offset:0\n" \
"ds_read_b64_tr_b16 v[90:91], v202 offset:8192\n" \
"v_mfma_f32_16x16x32_bf16 a[12:15], v[68:71], v[172:175], a[12:15]\n" \
"s_waitcnt lgkmcnt(8)\n" \
"v_exp_f32 v226, v144\n" \
"v_mfma_f32_16x16x32_bf16 a[16:19], v[72:75], v[168:171], a[16:19]\n" \
"ds_read_b64_tr_b16 v[92:93], v203 offset:0\n" \
"ds_read_b64_tr_b16 v[94:95], v203 offset:8192\n" \
"v_mfma_f32_16x16x32_bf16 a[20:23], v[72:75], v[172:175], a[20:23]\n" \
"s_waitcnt lgkmcnt(8)\n" \
"v_exp_f32 v227, v145\n" \
"v_mfma_f32_16x16x32_bf16 a[24:27], v[76:79], v[168:171], a[24:27]\n" \
"ds_read_b64_tr_b16 v[96:97], v196 offset:256\n" \
"ds_read_b64_tr_b16 v[98:99], v196 offset:8448\n" \
"v_mfma_f32_16x16x32_bf16 a[28:31], v[76:79], v[172:175], a[28:31]\n" \
"s_waitcnt lgkmcnt(8)\n" \
"v_exp_f32 v228, v146\n" \
"v_mfma_f32_16x16x32_bf16 a[32:35], v[80:83], v[168:171], a[32:35]\n" \
"ds_read_b64_tr_b16 v[100:101], v197 offset:256\n" \
"ds_read_b64_tr_b16 v[102:103], v197 offset:8448\n" \
"v_mfma_f32_16x16x32_bf16 a[36:39], v[80:83], v[172:175], a[36:39]\n" \
"s_waitcnt lgkmcnt(8)\n" \
"v_exp_f32 v229, v147\n" \
"v_mfma_f32_16x16x32_bf16 a[40:43], v[84:87], v[168:171], a[40:43]\n" \
"ds_read_b64_tr_b16 v[104:105], v198 offset:256\n" \
"ds_read_b64_tr_b16 v[106:107], v198 offset:8448\n" \
"v_mfma_f32_16x16x32_bf16 a[44:47], v[84:87], v[172:175], a[44:47]\n" \
"s_waitcnt lgkmcnt(8)\n" \
"v_exp_f32 v230, v148\n" \
"v_mfma_f32_16x16x32_bf16 a[48:51], v[88:91], v[168:171], a[48:51]\n" \
"ds_read_b64_tr_b16 v[108:109], v199 offset:256\n" \
"ds_read_b64_tr_b16 v[110:111], v199 offset:8448\n" \
"v_mfma_f32_16x16x32_bf16 a[52:55], v[88:91], v[172:175], a[52:55]\n" \
"s_waitcnt lgkmcnt(8)\n" \
"v_exp_f32 v231, v149\n" \
"v_mfma_f32_16x16x32_bf16 a[56:59], v[92:95], v[168:171], a[56:59]\n" \
"ds_read_b64_tr_b16 v[112:113], v200 offset:256\n" \
"ds_read_b64_tr_b16 v[114:115], v200 offset:8448\n" \
"v_mfma_f32_16x16x32_bf16 a[60:63], v[92:95], v[172:175], a[60:63]\n" \
"s_waitcnt lgkmcnt(8)\n" \
"v_exp_f32 v232, v150\n" \
"v_mfma_f32_16x16x32_bf16 a[64:67], v[96:99], v[168:171], a[64:67]\n" \
"ds_read_b64_tr_b16 v[116:117], v201 offset:256\n" \
"ds_read_b64_tr_b16 v[118:119], v201 offset:8448\n" \
"v_mfma_f32_16x16x32_bf16 a[68:71], v[96:99], v[172:175], a[68:71]\n" \
"s_waitcnt lgkmcnt(8)\n" \
"v_exp_f32 v233, v151\n" \
"v_mfma_f32_16x16x32_bf16 a[72:75], v[100:103], v[168:171], a[72:75]\n" \
"ds_read_b64_tr_b16 v[120:121], v202 offset:256\n" \
"ds_read_b64_tr_b16 v[122:123], v202 offset:8448\n" \
"v_mfma_f32_16x16x32_bf16 a[76:79], v[100:103], v[172:175], a[76:79]\n" \
"s_waitcnt lgkmcnt(8)\n" \
"v_add_f32 v216, v226, v227\n" \
"v_add_f32 v217, v230, v231\n" \
"v_mfma_f32_16x16x32_bf16 a[80:83], v[104:107], v[168:171], a[80:83]\n" \
"ds_read_b64_tr_b16 v[124:125], v203 offset:256\n" \
"ds_read_b64_tr_b16 v[126:127], v203 offset:8448\n" \
"v_mfma_f32_16x16x32_bf16 a[84:87], v[104:107], v[172:175], a[84:87]\n" \
"s_waitcnt lgkmcnt(8)\n" \
"v_exp_f32 v234, v152\n" \
"v_mfma_f32_16x16x32_bf16 a[88:91], v[108:111], v[168:171], a[88:91]\n" \
"ds_read_b128 a[192:195], v192 offset:32768\n" \
"ds_read_b128 a[196:199], v193 offset:32768\n" \
"v_mfma_f32_16x16x32_bf16 a[92:95], v[108:111], v[172:175], a[92:95]\n" \
"s_waitcnt lgkmcnt(8)\n" \
"v_exp_f32 v235, v153\n" \
"v_mfma_f32_16x16x32_bf16 a[96:99], v[112:115], v[168:171], a[96:99]\n" \
"ds_read_b128 a[200:203], v194 offset:32768\n" \
"ds_read_b128 a[204:207], v195 offset:32768\n" \
"v_mfma_f32_16x16x32_bf16 a[100:103], v[112:115], v[172:175], a[100:103]\n" \
"s_waitcnt lgkmcnt(8)\n" \
"v_exp_f32 v236, v154\n" \
"v_mfma_f32_16x16x32_bf16 a[104:107], v[116:119], v[168:171], a[104:107]\n" \
"ds_read_b128 a[208:211], v192 offset:33024\n" \
"ds_read_b128 a[212:215], v193 offset:33024\n" \
"v_mfma_f32_16x16x32_bf16 a[108:111], v[116:119], v[172:175], a[108:111]\n" \
"s_waitcnt lgkmcnt(8)\n" \
"v_exp_f32 v237, v155\n" \
"v_mfma_f32_16x16x32_bf16 a[112:115], v[120:123], v[168:171], a[112:115]\n" \
"ds_read_b128 a[216:219], v194 offset:33024\n" \
"ds_read_b128 a[220:223], v195 offset:33024\n" \
"v_mfma_f32_16x16x32_bf16 a[116:119], v[120:123], v[172:175], a[116:119]\n" \
"s_waitcnt lgkmcnt(8)\n" \
"v_add_f32 v216, v216, v228\n" \
"v_add_f32 v217, v217, v232\n" \
"v_mfma_f32_16x16x32_bf16 a[120:123], v[124:127], v[168:171], a[120:123]\n" \
"v_exp_f32 v238, v156\n" \
"v_mfma_f32_16x16x32_bf16 a[124:127], v[124:127], v[172:175], a[124:127]\n" \
"s_waitcnt lgkmcnt(7)\n" \
"v_mfma_f32_16x16x32_bf16 v[128:131], a[192:195], a[128:131], v[160:163]\n" \
"s_waitcnt lgkmcnt(6)\n" \
"v_exp_f32 v239, v157\n" \
"v_mfma_f32_16x16x32_bf16 v[128:131], a[196:199], a[136:139], v[128:131]\n" \
"s_waitcnt lgkmcnt(5)\n" \
"v_exp_f32 v240, v158\n" \
"v_mfma_f32_16x16x32_bf16 v[128:131], a[200:203], a[144:147], v[128:131]\n" \
"s_waitcnt lgkmcnt(4)\n" \
"v_exp_f32 v241, v159\n" \
"v_mfma_f32_16x16x32_bf16 v[128:131], a[204:207], a[152:155], v[128:131]\n" \
"s_waitcnt lgkmcnt(3)\n" \
"v_add_f32 v216, v216, v229\n" \
"v_add_f32 v217, v217, v233\n" \
"v_mfma_f32_16x16x32_bf16 v[128:131], a[208:211], a[160:163], v[128:131]\n" \
"s_waitcnt lgkmcnt(2)\n" \
"ds_read_b128 a[224:227], v192 offset:40960\n" \
"ds_read_b128 a[228:231], v193 offset:40960\n" \
"v_mfma_f32_16x16x32_bf16 v[128:131], a[212:215], a[168:171], v[128:131]\n" \
"s_waitcnt lgkmcnt(3)\n" \
"v_add_f32 v216, v216, v234\n" \
"v_add_f32 v217, v217, v238\n" \
"v_mfma_f32_16x16x32_bf16 v[128:131], a[216:219], a[176:179], v[128:131]\n" \
"s_waitcnt lgkmcnt(2)\n" \
"ds_read_b128 a[232:235], v194 offset:40960\n" \
"ds_read_b128 a[236:239], v195 offset:40960\n" \
"v_mfma_f32_16x16x32_bf16 v[128:131], a[220:223], a[184:187], v[128:131]\n" \
"v_add_f32 v216, v216, v235\n" \
"v_add_f32 v217, v217, v239\n" \
"v_mfma_f32_16x16x32_bf16 v[132:135], a[192:195], a[132:135], v[164:167]\n" \
"ds_read_b128 a[240:243], v192 offset:41216\n" \
"ds_read_b128 a[244:247], v193 offset:41216\n" \
"v_mfma_f32_16x16x32_bf16 v[132:135], a[196:199], a[140:143], v[132:135]\n" \
"v_add_f32 v216, v216, v236\n" \
"v_add_f32 v217, v217, v240\n" \
"v_mfma_f32_16x16x32_bf16 v[132:135], a[200:203], a[148:151], v[132:135]\n" \
"ds_read_b128 a[248:251], v194 offset:41216\n" \
"ds_read_b128 a[252:255], v195 offset:41216\n" \
"v_mfma_f32_16x16x32_bf16 v[132:135], a[204:207], a[156:159], v[132:135]\n" \
"v_add_f32 v216, v216, v237\n" \
"v_add_f32 v217, v217, v241\n" \
"v_mfma_f32_16x16x32_bf16 v[132:135], a[208:211], a[164:167], v[132:135]\n" \
"s_add_u32 m0, s51, 49152\n" \
"v_cvt_pk_bf16_f32 v176, v226, v227\n" \
"v_max_f32 v184, v216, v217\n" \
"v_mfma_f32_16x16x32_bf16 v[132:135], a[212:215], a[172:175], v[132:135]\n" \
"buffer_load_dwordx4 v208, s[44:47], s49 offen offset:0 lds\n" \
"v_mfma_f32_16x16x32_bf16 v[132:135], a[216:219], a[180:183], v[132:135]\n" \
"v_cvt_pk_bf16_f32 v177, v228, v229\n" \
"v_cmp_lt_f32 vcc, s53, v184\n" \
"v_mfma_f32_16x16x32_bf16 v[132:135], a[220:223], a[188:191], v[132:135]\n" \
"s_waitcnt lgkmcnt(7)\n" \
"buffer_load_dwordx4 v209, s[44:47], s49 offen offset:1024 lds\n" \
"v_mfma_f32_16x16x32_bf16 v[136:139], a[224:227], a[128:131], v[160:163]\n" \
"s_waitcnt lgkmcnt(6)\n" \
"v_cvt_pk_bf16_f32 v180, v230, v231\n" \
"v_cvt_pk_bf16_f32 v181, v232, v233\n" \
"v_mfma_f32_16x16x32_bf16 v[136:139], a[228:231], a[136:139], v[136:139]\n" \
"s_waitcnt lgkmcnt(5)\n" \
"buffer_load_dwordx4 v210, s[44:47], s49 offen offset:2048 lds\n" \
"v_mfma_f32_16x16x32_bf16 v[136:139], a[232:235], a[144:147], v[136:139]\n" \
"s_waitcnt lgkmcnt(4)\n" \
"v_cvt_pk_bf16_f32 v178, v234, v235\n" \
"v_cvt_pk_bf16_f32 v179, v236, v237\n" \
"v_mfma_f32_16x16x32_bf16 v[136:139], a[236:239], a[152:155], v[136:139]\n" \
"s_waitcnt lgkmcnt(0)\n" \
"buffer_load_dwordx4 v211, s[44:47], s49 offen offset:3072 lds\n" \
"v_mfma_f32_16x16x32_bf16 v[136:139], a[240:243], a[160:163], v[136:139]\n" \
"s_waitcnt vmcnt(12)\n" \
"v_cvt_pk_bf16_f32 v182, v238, v239\n" \
"v_cvt_pk_bf16_f32 v183, v240, v241\n" \
"v_mfma_f32_16x16x32_bf16 v[136:139], a[244:247], a[168:171], v[136:139]\n" \
"s_barrier\n" \
"v_mfma_f32_16x16x32_bf16 v[136:139], a[248:251], a[176:179], v[136:139]\n" \
"s_add_u32 m0, s50, 16384\n" \
"ds_read_b64_tr_b16 v[64:65], v196 offset:16384\n" \
"ds_read_b64_tr_b16 v[66:67], v196 offset:24576\n" \
"v_mfma_f32_16x16x32_bf16 v[136:139], a[252:255], a[184:187], v[136:139]\n" \
"buffer_load_dwordx4 v204, s[40:43], s48 offen offset:0 lds\n" \
"v_mfma_f32_16x16x32_bf16 v[140:143], a[224:227], a[132:135], v[164:167]\n" \
"ds_read_b64_tr_b16 v[68:69], v197 offset:16384\n" \
"ds_read_b64_tr_b16 v[70:71], v197 offset:24576\n" \
"v_mfma_f32_16x16x32_bf16 v[140:143], a[228:231], a[140:143], v[140:143]\n" \
"buffer_load_dwordx4 v205, s[40:43], s48 offen offset:1024 lds\n" \
"v_mfma_f32_16x16x32_bf16 v[140:143], a[232:235], a[148:151], v[140:143]\n" \
"ds_read_b64_tr_b16 v[72:73], v198 offset:16384\n" \
"ds_read_b64_tr_b16 v[74:75], v198 offset:24576\n" \
"v_mfma_f32_16x16x32_bf16 v[140:143], a[236:239], a[156:159], v[140:143]\n" \
"buffer_load_dwordx4 v206, s[40:43], s48 offen offset:2048 lds\n" \
"v_mfma_f32_16x16x32_bf16 v[140:143], a[240:243], a[164:167], v[140:143]\n" \
"s_add_u32 s49, s49, 16384\n" \
"ds_read_b64_tr_b16 v[76:77], v199 offset:16384\n" \
"ds_read_b64_tr_b16 v[78:79], v199 offset:24576\n" \
"v_mfma_f32_16x16x32_bf16 v[140:143], a[244:247], a[172:175], v[140:143]\n" \
"buffer_load_dwordx4 v207, s[40:43], s48 offen offset:3072 lds\n" \
"v_mfma_f32_16x16x32_bf16 v[140:143], a[248:251], a[180:183], v[140:143]\n" \
"s_waitcnt lgkmcnt(6)\n" \
"ds_read_b64_tr_b16 v[80:81], v200 offset:16384\n" \
"ds_read_b64_tr_b16 v[82:83], v200 offset:24576\n" \
"v_mfma_f32_16x16x32_bf16 v[140:143], a[252:255], a[188:191], v[140:143]\n" \
"s_cbranch_vccnz L_stub2_%=\n" \
"L_ret2_%=:\n" \
"v_mfma_f32_16x16x32_bf16 a[0:3], v[64:67], v[176:179], a[0:3]\n" \
"ds_read_b64_tr_b16 v[84:85], v201 offset:16384\n" \
"ds_read_b64_tr_b16 v[86:87], v201 offset:24576\n" \
"v_mfma_f32_16x16x32_bf16 a[4:7], v[64:67], v[180:183], a[4:7]\n" \
"s_waitcnt lgkmcnt(8)\n" \
"v_add_f32 v212, v212, v216\n" \
"v_add_f32 v213, v213, v217\n" \
"v_mfma_f32_16x16x32_bf16 a[8:11], v[68:71], v[176:179], a[8:11]\n" \
"s_add_u32 s48, s48, 16384\n" \
"ds_read_b64_tr_b16 v[88:89], v202 offset:16384\n" \
"ds_read_b64_tr_b16 v[90:91], v202 offset:24576\n" \
"v_mfma_f32_16x16x32_bf16 a[12:15], v[68:71], v[180:183], a[12:15]\n" \
"s_waitcnt lgkmcnt(8)\n" \
"v_exp_f32 v226, v128\n" \
"v_mfma_f32_16x16x32_bf16 a[16:19], v[72:75], v[176:179], a[16:19]\n" \
"ds_read_b64_tr_b16 v[92:93], v203 offset:16384\n" \
"ds_read_b64_tr_b16 v[94:95], v203 offset:24576\n" \
"v_mfma_f32_16x16x32_bf16 a[20:23], v[72:75], v[180:183], a[20:23]\n" \
"s_waitcnt lgkmcnt(8)\n" \
"v_exp_f32 v227, v129\n" \
"v_mfma_f32_16x16x32_bf16 a[24:27], v[76:79], v[176:179], a[24:27]\n" \
"ds_read_b64_tr_b16 v[96:97], v196 offset:16640\n" \
"ds_read_b64_tr_b16 v[98:99], v196 offset:24832\n" \
"v_mfma_f32_16x16x32_bf16 a[28:31], v[76:79], v[180:183], a[28:31]\n" \
"s_waitcnt lgkmcnt(8)\n" \
"v_exp_f32 v228, v130\n" \
"v_mfma_f32_16x16x32_bf16 a[32:35], v[80:83], v[176:179], a[32:35]\n" \
"ds_read_b64_tr_b16 v[100:101], v197 offset:16640\n" \
"ds_read_b64_tr_b16 v[102:103], v197 offset:24832\n" \
"v_mfma_f32_16x16x32_bf16 a[36:39], v[80:83], v[180:183], a[36:39]\n" \
"s_waitcnt lgkmcnt(8)\n" \
"v_exp_f32 v229, v131\n" \
"v_mfma_f32_16x16x32_bf16 a[40:43], v[84:87], v[176:179], a[40:43]\n" \
"ds_read_b64_tr_b16 v[104:105], v198 offset:16640\n" \
"ds_read_b64_tr_b16 v[106:107], v198 offset:24832\n" \
"v_mfma_f32_16x16x32_bf16 a[44:47], v[84:87], v[180:183], a[44:47]\n" \
"s_waitcnt lgkmcnt(8)\n" \
"v_exp_f32 v230, v132\n" \
"v_mfma_f32_16x16x32_bf16 a[48:51], v[88:91], v[176:179], a[48:51]\n" \
"ds_read_b64_tr_b16 v[108:109], v199 offset:16640\n" \
"ds_read_b64_tr_b16 v[110:111], v199 offset:24832\n" \
"v_mfma_f32_16x16x32_bf16 a[52:55], v[88:91], v[180:183], a[52:55]\n" \
"s_waitcnt lgkmcnt(8)\n" \
"v_exp_f32 v231, v133\n" \
"v_mfma_f32_16x16x32_bf16 a[56:59], v[92:95], v[176:179], a[56:59]\n" \
"ds_read_b64_tr_b16 v[112:113], v200 offset:16640\n" \
"ds_read_b64_tr_b16 v[114:115], v200 offset:24832\n" \
"v_mfma_f32_16x16x32_bf16 a[60:63], v[92:95], v[180:183], a[60:63]\n" \
"s_waitcnt lgkmcnt(8)\n" \
"v_exp_f32 v232, v134\n" \
"v_mfma_f32_16x16x32_bf16 a[64:67], v[96:99], v[176:179], a[64:67]\n" \
"ds_read_b64_tr_b16 v[116:117], v201 offset:16640\n" \
"ds_read_b64_tr_b16 v[118:119], v201 offset:24832\n" \
"v_mfma_f32_16x16x32_bf16 a[68:71], v[96:99], v[180:183], a[68:71]\n" \
"s_waitcnt lgkmcnt(8)\n" \
"v_exp_f32 v233, v135\n" \
"v_mfma_f32_16x16x32_bf16 a[72:75], v[100:103], v[176:179], a[72:75]\n" \
"ds_read_b64_tr_b16 v[120:121], v202 offset:16640\n" \
"ds_read_b64_tr_b16 v[122:123], v202 offset:24832\n" \
"v_mfma_f32_16x16x32_bf16 a[76:79], v[100:103], v[180:183], a[76:79]\n" \
"s_waitcnt lgkmcnt(8)\n" \
"v_add_f32 v216, v226, v227\n" \
"v_add_f32 v217, v230, v231\n" \
"v_mfma_f32_16x16x32_bf16 a[80:83], v[104:107], v[176:179], a[80:83]\n" \
"ds_read_b64_tr_b16 v[124:125], v203 offset:16640\n" \
"ds_read_b64_tr_b16 v[126:127], v203 offset:24832\n" \
"v_mfma_f32_16x16x32_bf16 a[84:87], v[104:107], v[180:183], a[84:87]\n" \
"s_waitcnt lgkmcnt(8)\n" \
"v_exp_f32 v234, v136\n" \
"v_mfma_f32_16x16x32_bf16 a[88:91], v[108:111], v[176:179], a[88:91]\n" \
"ds_read_b128 a[192:195], v192 offset:49152\n" \
"ds_read_b128 a[196:199], v193 offset:49152\n" \
"v_mfma_f32_16x16x32_bf16 a[92:95], v[108:111], v[180:183], a[92:95]\n" \
"s_waitcnt lgkmcnt(8)\n" \
"v_exp_f32 v235, v137\n" \
"v_mfma_f32_16x16x32_bf16 a[96:99], v[112:115], v[176:179], a[96:99]\n" \
"ds_read_b128 a[200:203], v194 offset:49152\n" \
"ds_read_b128 a[204:207], v195 offset:49152\n" \
"v_mfma_f32_16x16x32_bf16 a[100:103], v[112:115], v[180:183], a[100:103]\n" \
"s_waitcnt lgkmcnt(8)\n" \
"v_exp_f32 v236, v138\n" \
"v_mfma_f32_16x16x32_bf16 a[104:107], v[116:119], v[176:179], a[104:107]\n" \
"ds_read_b128 a[208:211], v192 offset:49408\n" \
"ds_read_b128 a[212:215], v193 offset:49408\n" \
"v_mfma_f32_16x16x32_bf16 a[108:111], v[116:119], v[180:183], a[108:111]\n" \
"s_waitcnt lgkmcnt(8)\n" \
"v_exp_f32 v237, v139\n" \
"v_mfma_f32_16x16x32_bf16 a[112:115], v[120:123], v[176:179], a[112:115]\n" \
"ds_read_b128 a[216:219], v194 offset:49408\n" \
"ds_read_b128 a[220:223], v195 offset:49408\n" \
"v_mfma_f32_16x16x32_bf16 a[116:119], v[120:123], v[180:183], a[116:119]\n" \
"s_waitcnt lgkmcnt(8)\n" \
"v_add_f32 v216, v216, v228\n" \
"v_add_f32 v217, v217, v232\n" \
"v_mfma_f32_16x16x32_bf16 a[120:123], v[124:127], v[176:179], a[120:123]\n" \
"v_exp_f32 v238, v140\n" \
"v_mfma_f32_16x16x32_bf16 a[124:127], v[124:127], v[180:183], a[124:127]\n" \
"s_waitcnt lgkmcnt(7)\n" \
"v_mfma_f32_16x16x32_bf16 v[144:147], a[192:195], a[128:131], v[160:163]\n" \
"s_waitcnt lgkmcnt(6)\n" \
"v_exp_f32 v239, v141\n" \
"v_mfma_f32_16x16x32_bf16 v[144:147], a[196:199], a[136:139], v[144:147]\n" \
"s_waitcnt lgkmcnt(5)\n" \
"v_exp_f32 v240, v142\n" \
"v_mfma_f32_16x16x32_bf16 v[144:147], a[200:203], a[144:147], v[144:147]\n" \
"s_waitcnt lgkmcnt(4)\n" \
"v_exp_f32 v241, v143\n" \
"v_mfma_f32_16x16x32_bf16 v[144:147], a[204:207], a[152:155], v[144:147]\n" \
"s_waitcnt lgkmcnt(3)\n" \
"v_add_f32 v216, v216, v229\n" \
"v_add_f32 v217, v217, v233\n" \
"v_mfma_f32_16x16x32_bf16 v[144:147], a[208:211], a[160:163], v[144:147]\n" \
"s_waitcnt lgkmcnt(2)\n" \
"ds_read_b128 a[224:227], v192 offset:57344\n" \
"ds_read_b128 a[228:231], v193 offset:57344\n" \
"v_mfma_f32_16x16x32_bf16 v[144:147], a[212:215], a[168:171], v[144:147]\n" \
"s_waitcnt lgkmcnt(3)\n" \
"v_add_f32 v216, v216, v234\n" \
"v_add_f32 v217, v217, v238\n" \
"v_mfma_f32_16x16x32_bf16 v[144:147], a[216:219], a[176:179], v[144:147]\n" \
"s_waitcnt lgkmcnt(2)\n" \
"ds_read_b128 a[232:235], v194 offset:57344\n" \
"ds_read_b128 a[236:239], v195 offset:57344\n" \
"v_mfma_f32_16x16x32_bf16 v[144:147], a[220:223], a[184:187], v[144:147]\n" \
"v_add_f32 v216, v216, v235\n" \
"v_add_f32 v217, v217, v239\n" \
"v_mfma_f32_16x16x32_bf16 v[148:151], a[192:195], a[132:135], v[164:167]\n" \
"ds_read_b128 a[240:243], v192 offset:57600\n" \
"ds_read_b128 a[244:247], v193 offset:57600\n" \
"v_mfma_f32_16x16x32_bf16 v[148:151], a[196:199], a[140:143], v[148:151]\n" \
"v_add_f32 v216, v216, v236\n" \
"v_add_f32 v217, v217, v240\n" \
"v_mfma_f32_16x16x32_bf16 v[148:151], a[200:203], a[148:151], v[148:151]\n" \
"ds_read_b128 a[248:251], v194 offset:57600\n" \
"ds_read_b128 a[252:255], v195 offset:57600\n" \
"v_mfma_f32_16x16x32_bf16 v[148:151], a[204:207], a[156:159], v[148:151]\n" \
"v_add_f32 v216, v216, v237\n" \
"v_add_f32 v217, v217, v241\n" \
"v_mfma_f32_16x16x32_bf16 v[148:151], a[208:211], a[164:167], v[148:151]\n" \
"s_add_u32 m0, s51, 0\n" \
"v_cvt_pk_bf16_f32 v168, v226, v227\n" \
"v_max_f32 v184, v216, v217\n" \
"v_mfma_f32_16x16x32_bf16 v[148:151], a[212:215], a[172:175], v[148:151]\n" \
"buffer_load_dwordx4 v208, s[44:47], s49 offen offset:0 lds\n" \
"v_mfma_f32_16x16x32_bf16 v[148:151], a[216:219], a[180:183], v[148:151]\n" \
"v_cvt_pk_bf16_f32 v169, v228, v229\n" \
"v_cmp_lt_f32 vcc, s53, v184\n" \
"v_mfma_f32_16x16x32_bf16 v[148:151], a[220:223], a[188:191], v[148:151]\n" \
"s_waitcnt lgkmcnt(7)\n" \
"buffer_load_dwordx4 v209, s[44:47], s49 offen offset:1024 lds\n" \
"v_mfma_f32_16x16x32_bf16 v[152:155], a[224:227], a[128:131], v[160:163]\n" \
"s_waitcnt lgkmcnt(6)\n" \
"v_cvt_pk_bf16_f32 v172, v230, v231\n" \
"v_cvt_pk_bf16_f32 v173, v232, v233\n" \
"v_mfma_f32_16x16x32_bf16 v[152:155], a[228:231], a[136:139], v[152:155]\n" \
"s_waitcnt lgkmcnt(5)\n" \
"buffer_load_dwordx4 v210, s[44:47], s49 offen offset:2048 lds\n" \
"v_mfma_f32_16x16x32_bf16 v[152:155], a[232:235], a[144:147], v[152:155]\n" \
"s_waitcnt lgkmcnt(4)\n" \
"v_cvt_pk_bf16_f32 v170, v234, v235\n" \
"v_cvt_pk_bf16_f32 v171, v236, v237\n" \
"v_mfma_f32_16x16x32_bf16 v[152:155], a[236:239], a[152:155], v[152:155]\n" \
"s_waitcnt lgkmcnt(0)\n" \
"buffer_load_dwordx4 v211, s[44:47], s49 offen offset:3072 lds\n" \
"v_mfma_f32_16x16x32_bf16 v[152:155], a[240:243], a[160:163], v[152:155]\n" \
"s_waitcnt vmcnt(12)\n" \
"v_cvt_pk_bf16_f32 v174, v238, v239\n" \
"v_cvt_pk_bf16_f32 v175, v240, v241\n" \
"v_mfma_f32_16x16x32_bf16 v[152:155], a[244:247], a[168:171], v[152:155]\n" \
"s_barrier\n" \
"v_mfma_f32_16x16x32_bf16 v[152:155], a[248:251], a[176:179], v[152:155]\n" \
"s_add_u32 m0, s50, 32768\n" \
"ds_read_b64_tr_b16 v[64:65], v196 offset:32768\n" \
"ds_read_b64_tr_b16 v[66:67], v196 offset:40960\n" \
"v_mfma_f32_16x16x32_bf16 v[152:155], a[252:255], a[184:187], v[152:155]\n" \
"buffer_load_dwordx4 v204, s[40:43], s48 offen offset:0 lds\n" \
"v_mfma_f32_16x16x32_bf16 v[156:159], a[224:227], a[132:135], v[164:167]\n" \
"ds_read_b64_tr_b16 v[68:69], v197 offset:32768\n" \
"ds_read_b64_tr_b16 v[70:71], v197 offset:40960\n" \
"v_mfma_f32_16x16x32_bf16 v[156:159], a[228:231], a[140:143], v[156:159]\n" \
"buffer_load_dwordx4 v205, s[40:43], s48 offen offset:1024 lds\n" \
"v_mfma_f32_16x16x32_bf16 v[156:159], a[232:235], a[148:151], v[156:159]\n" \
"ds_read_b64_tr_b16 v[72:73], v198 offset:32768\n" \
"ds_read_b64_tr_b16 v[74:75], v198 offset:40960\n" \
"v_mfma_f32_16x16x32_bf16 v[156:159], a[236:239], a[156:159], v[156:159]\n" \
"buffer_load_dwordx4 v206, s[40:43], s48 offen offset:2048 lds\n" \
"v_mfma_f32_16x16x32_bf16 v[156:159], a[240:243], a[164:167], v[156:159]\n" \
"s_add_u32 s49, s49, 16384\n" \
"ds_read_b64_tr_b16 v[76:77], v199 offset:32768\n" \
"ds_read_b64_tr_b16 v[78:79], v199 offset:40960\n" \
"v_mfma_f32_16x16x32_bf16 v[156:159], a[244:247], a[172:175], v[156:159]\n" \
"buffer_load_dwordx4 v207, s[40:43], s48 offen offset:3072 lds\n" \
"v_mfma_f32_16x16x32_bf16 v[156:159], a[248:251], a[180:183], v[156:159]\n" \
"s_waitcnt lgkmcnt(6)\n" \
"ds_read_b64_tr_b16 v[80:81], v200 offset:32768\n" \
"ds_read_b64_tr_b16 v[82:83], v200 offset:40960\n" \
"v_mfma_f32_16x16x32_bf16 v[156:159], a[252:255], a[188:191], v[156:159]\n" \
"s_cbranch_vccnz L_stub3_%=\n" \
"L_ret3_%=:\n" \
"v_mfma_f32_16x16x32_bf16 a[0:3], v[64:67], v[168:171], a[0:3]\n" \
"ds_read_b64_tr_b16 v[84:85], v201 offset:32768\n" \
"ds_read_b64_tr_b16 v[86:87], v201 offset:40960\n" \
"v_mfma_f32_16x16x32_bf16 a[4:7], v[64:67], v[172:175], a[4:7]\n" \
"s_waitcnt lgkmcnt(8)\n" \
"v_add_f32 v212, v212, v216\n" \
"v_add_f32 v213, v213, v217\n" \
"v_mfma_f32_16x16x32_bf16 a[8:11], v[68:71], v[168:171], a[8:11]\n" \
"s_add_u32 s48, s48, 16384\n" \
"ds_read_b64_tr_b16 v[88:89], v202 offset:32768\n" \
"ds_read_b64_tr_b16 v[90:91], v202 offset:40960\n" \
"v_mfma_f32_16x16x32_bf16 a[12:15], v[68:71], v[172:175], a[12:15]\n" \
"s_waitcnt lgkmcnt(8)\n" \
"v_exp_f32 v226, v144\n" \
"v_mfma_f32_16x16x32_bf16 a[16:19], v[72:75], v[168:171], a[16:19]\n" \
"ds_read_b64_tr_b16 v[92:93], v203 offset:32768\n" \
"ds_read_b64_tr_b16 v[94:95], v203 offset:40960\n" \
"v_mfma_f32_16x16x32_bf16 a[20:23], v[72:75], v[172:175], a[20:23]\n" \
"s_waitcnt lgkmcnt(8)\n" \
"v_exp_f32 v227, v145\n" \
"v_mfma_f32_16x16x32_bf16 a[24:27], v[76:79], v[168:171], a[24:27]\n" \
"ds_read_b64_tr_b16 v[96:97], v196 offset:33024\n" \
"ds_read_b64_tr_b16 v[98:99], v196 offset:41216\n" \
"v_mfma_f32_16x16x32_bf16 a[28:31], v[76:79], v[172:175], a[28:31]\n" \
"s_waitcnt lgkmcnt(8)\n" \
"v_exp_f32 v228, v146\n" \
"v_mfma_f32_16x16x32_bf16 a[32:35], v[80:83], v[168:171], a[32:35]\n" \
"ds_read_b64_tr_b16 v[100:101], v197 offset:33024\n" \
"ds_read_b64_tr_b16 v[102:103], v197 offset:41216\n" \
"v_mfma_f32_16x16x32_bf16 a[36:39], v[80:83], v[172:175], a[36:39]\n" \
"s_waitcnt lgkmcnt(8)\n" \
"v_exp_f32 v229, v147\n" \
"v_mfma_f32_16x16x32_bf16 a[40:43], v[84:87], v[168:171], a[40:43]\n" \
"ds_read_b64_tr_b16 v[104:105], v198 offset:33024\n" \
"ds_read_b64_tr_b16 v[106:107], v198 offset:41216\n" \
"v_mfma_f32_16x16x32_bf16 a[44:47], v[84:87], v[172:175], a[44:47]\n" \
"s_waitcnt lgkmcnt(8)\n" \
"v_exp_f32 v230, v148\n" \
"v_mfma_f32_16x16x32_bf16 a[48:51], v[88:91], v[168:171], a[48:51]\n" \
"ds_read_b64_tr_b16 v[108:109], v199 offset:33024\n" \
"ds_read_b64_tr_b16 v[110:111], v199 offset:41216\n" \
"v_mfma_f32_16x16x32_bf16 a[52:55], v[88:91], v[172:175], a[52:55]\n" \
"s_waitcnt lgkmcnt(8)\n" \
"v_exp_f32 v231, v149\n" \
"v_mfma_f32_16x16x32_bf16 a[56:59], v[92:95], v[168:171], a[56:59]\n" \
"ds_read_b64_tr_b16 v[112:113], v200 offset:33024\n" \
"ds_read_b64_tr_b16 v[114:115], v200 offset:41216\n" \
"v_mfma_f32_16x16x32_bf16 a[60:63], v[92:95], v[172:175], a[60:63]\n" \
"s_waitcnt lgkmcnt(8)\n" \
"v_exp_f32 v232, v150\n" \
"v_mfma_f32_16x16x32_bf16 a[64:67], v[96:99], v[168:171], a[64:67]\n" \
"ds_read_b64_tr_b16 v[116:117], v201 offset:33024\n" \
"ds_read_b64_tr_b16 v[118:119], v201 offset:41216\n" \
"v_mfma_f32_16x16x32_bf16 a[68:71], v[96:99], v[172:175], a[68:71]\n" \
"s_waitcnt lgkmcnt(8)\n" \
"v_exp_f32 v233, v151\n" \
"v_mfma_f32_16x16x32_bf16 a[72:75], v[100:103], v[168:171], a[72:75]\n" \
"ds_read_b64_tr_b16 v[120:121], v202 offset:33024\n" \
"ds_read_b64_tr_b16 v[122:123], v202 offset:41216\n" \
"v_mfma_f32_16x16x32_bf16 a[76:79], v[100:103], v[172:175], a[76:79]\n" \
"s_waitcnt lgkmcnt(8)\n" \
"v_add_f32 v216, v226, v227\n" \
"v_add_f32 v217, v230, v231\n" \
"v_mfma_f32_16x16x32_bf16 a[80:83], v[104:107], v[168:171], a[80:83]\n" \
"ds_read_b64_tr_b16 v[124:125], v203 offset:33024\n" \
"ds_read_b64_tr_b16 v[126:127], v203 offset:41216\n" \
"v_mfma_f32_16x16x32_bf16 a[84:87], v[104:107], v[172:175], a[84:87]\n" \
"s_waitcnt lgkmcnt(8)\n" \
"v_exp_f32 v234, v152\n" \
"v_mfma_f32_16x16x32_bf16 a[88:91], v[108:111], v[168:171], a[88:91]\n" \
"ds_read_b128 a[192:195], v192 offset:0\n" \
"ds_read_b128 a[196:199], v193 offset:0\n" \
"v_mfma_f32_16x16x32_bf16 a[92:95], v[108:111], v[172:175], a[92:95]\n" \
"s_waitcnt lgkmcnt(8)\n" \
"v_exp_f32 v235, v153\n" \
"v_mfma_f32_16x16x32_bf16 a[96:99], v[112:115], v[168:171], a[96:99]\n" \
"ds_read_b128 a[200:203], v194 offset:0\n" \
"ds_read_b128 a[204:207], v195 offset:0\n" \
"v_mfma_f32_16x16x32_bf16 a[100:103], v[112:115], v[172:175], a[100:103]\n" \
"s_waitcnt lgkmcnt(8)\n" \
"v_exp_f32 v236, v154\n" \
"v_mfma_f32_16x16x32_bf16 a[104:107], v[116:119], v[168:171], a[104:107]\n" \
"ds_read_b128 a[208:211], v192 offset:256\n" \
"ds_read_b128 a[212:215], v193 offset:256\n" \
"v_mfma_f32_16x16x32_bf16 a[108:111], v[116:119], v[172:175], a[108:111]\n" \
"s_waitcnt lgkmcnt(8)\n" \
"v_exp_f32 v237, v155\n" \
"v_mfma_f32_16x16x32_bf16 a[112:115], v[120:123], v[168:171], a[112:115]\n" \
"ds_read_b128 a[216:219], v194 offset:256\n" \
"ds_read_b128 a[220:223], v195 offset:256\n" \
"v_mfma_f32_16x16x32_bf16 a[116:119], v[120:123], v[172:175], a[116:119]\n" \
"s_waitcnt lgkmcnt(8)\n" \
"v_add_f32 v216, v216, v228\n" \
"v_add_f32 v217, v217, v232\n" \
"v_mfma_f32_16x16x32_bf16 a[120:123], v[124:127], v[168:171], a[120:123]\n" \
"s_waitcnt lgkmcnt(7)\n" \
"v_exp_f32 v238, v156\n" \
"v_mfma_f32_16x16x32_bf16 a[124:127], v[124:127], v[172:175], a[124:127]\n" \
"s_bitcmp1_b32 s24, s52\n" \
"s_cbranch_scc0 L_loop_%=\n" \
"s_bitcmp1_b32 0xccc0, s52\n" \
"s_cbranch_scc1 L_f3_%=\n" \
"s_cmp_eq_u32 s52, 2\n" \
"s_cbranch_scc1 L_f2_%=\n" \
"buffer_load_dword v186, v185, s[60:63], 0 offen\n" \
"buffer_load_dword v186, v187, s[36:39], 0 offen\n" \
"buffer_load_dword v186, v185, s[32:35], 0 offen\n" \
"s_branch L_loop_%=\n" \
"L_f2_%=:\n" \
"buffer_load_dword v186, v57, s[80:83], 0 offen\n" \
"v_add_u32 v57, 0x8000, v57\n" \
"buffer_load_dword v186, v54, s[64:67], 0 offen sc1\n" \
"buffer_load_dword v186, v54, s[84:87], 0 offen\n" \
"buffer_load_dword v186, v57, s[80:83], 0 offen\n" \
"s_branch L_loop_%=\n" \
"L_f3_%=:\n" \
"v_add_u32 v62, 0x40000, v62\n" \
"buffer_load_dword v186, v62, s[40:43], 0 offen\n" \
"buffer_load_dword v186, v62, s[44:47], 0 offen\n" \
"s_branch L_loop_%=\n" \
"L_stub0_%=:\n" \
"s_mov_b32 s55, 0\n" \
"s_branch L_rare0_%=\n" \
"L_stub1_%=:\n" \
"s_mov_b32 s55, 1\n" \
"s_branch L_rare1_%=\n" \
"L_stub2_%=:\n" \
"s_mov_b32 s55, 2\n" \
"s_branch L_rare0_%=\n" \
"L_stub3_%=:\n" \
"s_mov_b32 s55, 3\n" \
"s_branch L_rare1_%=\n" \
"L_rare0_%=:\n" \
"v_max_f32 v218, v144, v145\n" \
"v_max3_f32 v218, v218, v146, v147\n" \
"v_max3_f32 v218, v218, v152, v153\n" \
"v_max3_f32 v218, v218, v154, v155\n" \
"v_max_f32 v219, v148, v149\n" \
"v_max3_f32 v219, v219, v150, v151\n" \
"v_max3_f32 v219, v219, v156, v157\n" \
"v_max3_f32 v219, v219, v158, v159\n" \
"s_or_b32 s53, s55, s52\n" \
"s_cbranch_scc0 L_skipn_%=\n" \
"v_max3_f32 v218, v218, v128, v129\n" \
"v_max3_f32 v218, v218, v130, v131\n" \
"v_max3_f32 v218, v218, v136, v137\n" \
"v_max3_f32 v218, v218, v138, v139\n" \
"v_max3_f32 v219, v219, v132, v133\n" \
"v_max3_f32 v219, v219, v134, v135\n" \
"v_max3_f32 v219, v219, v140, v141\n" \
"v_max3_f32 v219, v219, v142, v143\n" \
"L_skipn_%=:\n" \
"v_mov_b32 v220, v218\n" \
"v_mov_b32 v221, v219\n" \
"s_nop 1\n" \
"v_permlane16_swap_b32 v220, v218\n" \
"v_permlane16_swap_b32 v221, v219\n" \
"s_nop 1\n" \
"v_max_f32 v218, v218, v220\n" \
"v_max_f32 v219, v219, v221\n" \
"v_mov_b32 v220, v218\n" \
"v_mov_b32 v221, v219\n" \
"s_nop 1\n" \
"v_permlane32_swap_b32 v220, v218\n" \
"v_permlane32_swap_b32 v221, v219\n" \
"s_nop 1\n" \
"v_max_f32 v218, v218, v220\n" \
"v_max_f32 v219, v219, v221\n" \
"v_max_f32 v218, s54, v218\n" \
"v_max_f32 v219, s54, v219\n" \
"v_sub_f32 v128, v128, v218\n" \
"v_sub_f32 v226, v144, v218\n" \
"v_sub_f32 v129, v129, v218\n" \
"v_sub_f32 v227, v145, v218\n" \
"v_sub_f32 v130, v130, v218\n" \
"v_sub_f32 v228, v146, v218\n" \
"v_sub_f32 v131, v131, v218\n" \
"v_sub_f32 v229, v147, v218\n" \
"v_sub_f32 v132, v132, v219\n" \
"v_sub_f32 v230, v148, v219\n" \
"v_sub_f32 v133, v133, v219\n" \
"v_sub_f32 v231, v149, v219\n" \
"v_sub_f32 v134, v134, v219\n" \
"v_sub_f32 v232, v150, v219\n" \
"v_sub_f32 v135, v135, v219\n" \
"v_sub_f32 v233, v151, v219\n" \
"v_sub_f32 v136, v136, v218\n" \
"v_sub_f32 v234, v152, v218\n" \
"v_sub_f32 v137, v137, v218\n" \
"v_sub_f32 v235, v153, v218\n" \
"v_sub_f32 v138, v138, v218\n" \
"v_sub_f32 v236, v154, v218\n" \
"v_sub_f32 v139, v139, v218\n" \
"v_sub_f32 v237, v155, v218\n" \
"v_sub_f32 v140, v140, v219\n" \
"v_sub_f32 v238, v156, v219\n" \
"v_sub_f32 v141, v141, v219\n" \
"v_sub_f32 v239, v157, v219\n" \
"v_sub_f32 v142, v142, v219\n" \
"v_sub_f32 v240, v158, v219\n" \
"v_sub_f32 v143, v143, v219\n" \
"v_sub_f32 v241, v159, v219\n" \
"v_exp_f32 v226, v226\n" \
"v_exp_f32 v227, v227\n" \
"v_exp_f32 v228, v228\n" \
"v_exp_f32 v229, v229\n" \
"v_exp_f32 v230, v230\n" \
"v_exp_f32 v231, v231\n" \
"v_exp_f32 v232, v232\n" \
"v_exp_f32 v233, v233\n" \
"v_exp_f32 v234, v234\n" \
"v_exp_f32 v235, v235\n" \
"v_exp_f32 v236, v236\n" \
"v_exp_f32 v237, v237\n" \
"v_exp_f32 v238, v238\n" \
"v_exp_f32 v239, v239\n" \
"v_exp_f32 v240, v240\n" \
"v_exp_f32 v241, v241\n" \
"s_nop 0\n" \
"v_cvt_pk_bf16_f32 v176, v226, v227\n" \
"v_cvt_pk_bf16_f32 v177, v228, v229\n" \
"v_cvt_pk_bf16_f32 v180, v230, v231\n" \
"v_cvt_pk_bf16_f32 v181, v232, v233\n" \
"v_cvt_pk_bf16_f32 v178, v234, v235\n" \
"v_cvt_pk_bf16_f32 v179, v236, v237\n" \
"v_cvt_pk_bf16_f32 v182, v238, v239\n" \
"v_cvt_pk_bf16_f32 v183, v240, v241\n" \
"s_branch L_rarec_%=\n" \
"L_rare1_%=:\n" \
"v_max_f32 v218, v128, v129\n" \
"v_max3_f32 v218, v218, v130, v131\n" \
"v_max3_f32 v218, v218, v136, v137\n" \
"v_max3_f32 v218, v218, v138, v139\n" \
"v_max_f32 v219, v132, v133\n" \
"v_max3_f32 v219, v219, v134, v135\n" \
"v_max3_f32 v219, v219, v140, v141\n" \
"v_max3_f32 v219, v219, v142, v143\n" \
"v_max3_f32 v218, v218, v144, v145\n" \
"v_max3_f32 v218, v218, v146, v147\n" \
"v_max3_f32 v218, v218, v152, v153\n" \
"v_max3_f32 v218, v218, v154, v155\n" \
"v_max3_f32 v219, v219, v148, v149\n" \
"v_max3_f32 v219, v219, v150, v151\n" \
"v_max3_f32 v219, v219, v156, v157\n" \
"v_max3_f32 v219, v219, v158, v159\n" \
"v_mov_b32 v220, v218\n" \
"v_mov_b32 v221, v219\n" \
"s_nop 1\n" \
"v_permlane16_swap_b32 v220, v218\n" \
"v_permlane16_swap_b32 v221, v219\n" \
"s_nop 1\n" \
"v_max_f32 v218, v218, v220\n" \
"v_max_f32 v219, v219, v221\n" \
"v_mov_b32 v220, v218\n" \
"v_mov_b32 v221, v219\n" \
"s_nop 1\n" \
"v_permlane32_swap_b32 v220, v218\n" \
"v_permlane32_swap_b32 v221, v219\n" \
"s_nop 1\n" \
"v_max_f32 v218, v218, v220\n" \
"v_max_f32 v219, v219, v221\n" \
"v_max_f32 v218, s54, v218\n" \
"v_max_f32 v219, s54, v219\n" \
"v_sub_f32 v144, v144, v218\n" \
"v_sub_f32 v226, v128, v218\n" \
"v_sub_f32 v145, v145, v218\n" \
"v_sub_f32 v227, v129, v218\n" \
"v_sub_f32 v146, v146, v218\n" \
"v_sub_f32 v228, v130, v218\n" \
"v_sub_f32 v147, v147, v218\n" \
"v_sub_f32 v229, v131, v218\n" \
"v_sub_f32 v148, v148, v219\n" \
"v_sub_f32 v230, v132, v219\n" \
"v_sub_f32 v149, v149, v219\n" \
"v_sub_f32 v231, v133, v219\n" \
"v_sub_f32 v150, v150, v219\n" \
"v_sub_f32 v232, v134, v219\n" \
"v_sub_f32 v151, v151, v219\n" \
"v_sub_f32 v233, v135, v219\n" \
"v_sub_f32 v152, v152, v218\n" \
"v_sub_f32 v234, v136, v218\n" \
"v_sub_f32 v153, v153, v218\n" \
"v_sub_f32 v235, v137, v218\n" \
"v_sub_f32 v154, v154, v218\n" \
"v_sub_f32 v236, v138, v218\n" \
"v_sub_f32 v155, v155, v218\n" \
"v_sub_f32 v237, v139, v218\n" \
"v_sub_f32 v156, v156, v219\n" \
"v_sub_f32 v238, v140, v219\n" \
"v_sub_f32 v157, v157, v219\n" \
"v_sub_f32 v239, v141, v219\n" \
"v_sub_f32 v158, v158, v219\n" \
"v_sub_f32 v240, v142, v219\n" \
"v_sub_f32 v159, v159, v219\n" \
"v_sub_f32 v241, v143, v219\n" \
"v_exp_f32 v226, v226\n" \
"v_exp_f32 v227, v227\n" \
"v_exp_f32 v228, v228\n" \
"v_exp_f32 v229, v229\n" \
"v_exp_f32 v230, v230\n" \
"v_exp_f32 v231, v231\n" \
"v_exp_f32 v232, v232\n" \
"v_exp_f32 v233, v233\n" \
"v_exp_f32 v234, v234\n" \
"v_exp_f32 v235, v235\n" \
"v_exp_f32 v236, v236\n" \
"v_exp_f32 v237, v237\n" \
"v_exp_f32 v238, v238\n" \
"v_exp_f32 v239, v239\n" \
"v_exp_f32 v240, v240\n" \
"v_exp_f32 v241, v241\n" \
"s_nop 0\n" \
"v_cvt_pk_bf16_f32 v168, v226, v227\n" \
"v_cvt_pk_bf16_f32 v169, v228, v229\n" \
"v_cvt_pk_bf16_f32 v172, v230, v231\n" \
"v_cvt_pk_bf16_f32 v173, v232, v233\n" \
"v_cvt_pk_bf16_f32 v170, v234, v235\n" \
"v_cvt_pk_bf16_f32 v171, v236, v237\n" \
"v_cvt_pk_bf16_f32 v174, v238, v239\n" \
"v_cvt_pk_bf16_f32 v175, v240, v241\n" \
"L_rarec_%=:\n" \
"v_add_f32 v216, v226, v227\n" \
"v_add_f32 v216, v216, v228\n" \
"v_add_f32 v216, v216, v229\n" \
"v_add_f32 v216, v216, v234\n" \
"v_add_f32 v216, v216, v235\n" \
"v_add_f32 v216, v216, v236\n" \
"v_add_f32 v216, v216, v237\n" \
"v_add_f32 v217, v230, v231\n" \
"v_add_f32 v217, v217, v232\n" \
"v_add_f32 v217, v217, v233\n" \
"v_add_f32 v217, v217, v238\n" \
"v_add_f32 v217, v217, v239\n" \
"v_add_f32 v217, v217, v240\n" \
"v_add_f32 v217, v217, v241\n" \
"v_add_f32 v214, v214, v218\n" \
"v_add_f32 v215, v215, v219\n" \
"v_sub_f32 v160, 0, v214\n" \
"v_sub_f32 v161, 0, v214\n" \
"v_sub_f32 v162, 0, v214\n" \
"v_sub_f32 v163, 0, v214\n" \
"v_sub_f32 v164, 0, v215\n" \
"v_sub_f32 v165, 0, v215\n" \
"v_sub_f32 v166, 0, v215\n" \
"v_sub_f32 v167, 0, v215\n" \
"v_exp_f32 v218, -v218\n" \
"v_exp_f32 v219, -v219\n" \
"s_cmp_eq_u32 s54, 0\n" \
"s_mov_b32 s54, 0\n" \
"s_mov_b32 s53, 0x45000000\n" \
"s_cbranch_scc0 L_rared_%=\n" \
"v_mul_f32 v212, v212, v218\n" \
"v_mul_f32 v213, v213, v219\n" \
"v_accvgpr_read_b32 v222, a0\n" \
"v_accvgpr_read_b32 v223, a1\n" \
"v_accvgpr_read_b32 v224, a2\n" \
"v_accvgpr_read_b32 v225, a3\n" \
"v_mul_f32 v222, v222, v218\n" \
"v_mul_f32 v223, v223, v218\n" \
"v_mul_f32 v224, v224, v218\n" \
"v_mul_f32 v225, v225, v218\n" \
"v_accvgpr_write_b32 a0, v222\n" \
"v_accvgpr_write_b32 a1, v223\n" \
"v_accvgpr_write_b32 a2, v224\n" \
"v_accvgpr_write_b32 a3, v225\n" \
"v_accvgpr_read_b32 v222, a4\n" \
"v_accvgpr_read_b32 v223, a5\n" \
"v_accvgpr_read_b32 v224, a6\n" \
"v_accvgpr_read_b32 v225, a7\n" \
"v_mul_f32 v222, v222, v219\n" \
"v_mul_f32 v223, v223, v219\n" \
"v_mul_f32 v224, v224, v219\n" \
"v_mul_f32 v225, v225, v219\n" \
"v_accvgpr_write_b32 a4, v222\n" \
"v_accvgpr_write_b32 a5, v223\n" \
"v_accvgpr_write_b32 a6, v224\n" \
"v_accvgpr_write_b32 a7, v225\n" \
"v_accvgpr_read_b32 v222, a8\n" \
"v_accvgpr_read_b32 v223, a9\n" \
"v_accvgpr_read_b32 v224, a10\n" \
"v_accvgpr_read_b32 v225, a11\n" \
"v_mul_f32 v222, v222, v218\n" \
"v_mul_f32 v223, v223, v218\n" \
"v_mul_f32 v224, v224, v218\n" \
"v_mul_f32 v225, v225, v218\n" \
"v_accvgpr_write_b32 a8, v222\n" \
"v_accvgpr_write_b32 a9, v223\n" \
"v_accvgpr_write_b32 a10, v224\n" \
"v_accvgpr_write_b32 a11, v225\n" \
"v_accvgpr_read_b32 v222, a12\n" \
"v_accvgpr_read_b32 v223, a13\n" \
"v_accvgpr_read_b32 v224, a14\n" \
"v_accvgpr_read_b32 v225, a15\n" \
"v_mul_f32 v222, v222, v219\n" \
"v_mul_f32 v223, v223, v219\n" \
"v_mul_f32 v224, v224, v219\n" \
"v_mul_f32 v225, v225, v219\n" \
"v_accvgpr_write_b32 a12, v222\n" \
"v_accvgpr_write_b32 a13, v223\n" \
"v_accvgpr_write_b32 a14, v224\n" \
"v_accvgpr_write_b32 a15, v225\n" \
"v_accvgpr_read_b32 v222, a16\n" \
"v_accvgpr_read_b32 v223, a17\n" \
"v_accvgpr_read_b32 v224, a18\n" \
"v_accvgpr_read_b32 v225, a19\n" \
"v_mul_f32 v222, v222, v218\n" \
"v_mul_f32 v223, v223, v218\n" \
"v_mul_f32 v224, v224, v218\n" \
"v_mul_f32 v225, v225, v218\n" \
"v_accvgpr_write_b32 a16, v222\n" \
"v_accvgpr_write_b32 a17, v223\n" \
"v_accvgpr_write_b32 a18, v224\n" \
"v_accvgpr_write_b32 a19, v225\n" \
"v_accvgpr_read_b32 v222, a20\n" \
"v_accvgpr_read_b32 v223, a21\n" \
"v_accvgpr_read_b32 v224, a22\n" \
"v_accvgpr_read_b32 v225, a23\n" \
"v_mul_f32 v222, v222, v219\n" \
"v_mul_f32 v223, v223, v219\n" \
"v_mul_f32 v224, v224, v219\n" \
"v_mul_f32 v225, v225, v219\n" \
"v_accvgpr_write_b32 a20, v222\n" \
"v_accvgpr_write_b32 a21, v223\n" \
"v_accvgpr_write_b32 a22, v224\n" \
"v_accvgpr_write_b32 a23, v225\n" \
"v_accvgpr_read_b32 v222, a24\n" \
"v_accvgpr_read_b32 v223, a25\n" \
"v_accvgpr_read_b32 v224, a26\n" \
"v_accvgpr_read_b32 v225, a27\n" \
"v_mul_f32 v222, v222, v218\n" \
"v_mul_f32 v223, v223, v218\n" \
"v_mul_f32 v224, v224, v218\n" \
"v_mul_f32 v225, v225, v218\n" \
"v_accvgpr_write_b32 a24, v222\n" \
"v_accvgpr_write_b32 a25, v223\n" \
"v_accvgpr_write_b32 a26, v224\n" \
"v_accvgpr_write_b32 a27, v225\n" \
"v_accvgpr_read_b32 v222, a28\n" \
"v_accvgpr_read_b32 v223, a29\n" \
"v_accvgpr_read_b32 v224, a30\n" \
"v_accvgpr_read_b32 v225, a31\n" \
"v_mul_f32 v222, v222, v219\n" \
"v_mul_f32 v223, v223, v219\n" \
"v_mul_f32 v224, v224, v219\n" \
"v_mul_f32 v225, v225, v219\n" \
"v_accvgpr_write_b32 a28, v222\n" \
"v_accvgpr_write_b32 a29, v223\n" \
"v_accvgpr_write_b32 a30, v224\n" \
"v_accvgpr_write_b32 a31, v225\n" \
"v_accvgpr_read_b32 v222, a32\n" \
"v_accvgpr_read_b32 v223, a33\n" \
"v_accvgpr_read_b32 v224, a34\n" \
"v_accvgpr_read_b32 v225, a35\n" \
"v_mul_f32 v222, v222, v218\n" \
"v_mul_f32 v223, v223, v218\n" \
"v_mul_f32 v224, v224, v218\n" \
"v_mul_f32 v225, v225, v218\n" \
"v_accvgpr_write_b32 a32, v222\n" \
"v_accvgpr_write_b32 a33, v223\n" \
"v_accvgpr_write_b32 a34, v224\n" \
"v_accvgpr_write_b32 a35, v225\n" \
"v_accvgpr_read_b32 v222, a36\n" \
"v_accvgpr_read_b32 v223, a37\n" \
"v_accvgpr_read_b32 v224, a38\n" \
"v_accvgpr_read_b32 v225, a39\n" \
"v_mul_f32 v222, v222, v219\n" \
"v_mul_f32 v223, v223, v219\n" \
"v_mul_f32 v224, v224, v219\n" \
"v_mul_f32 v225, v225, v219\n" \
"v_accvgpr_write_b32 a36, v222\n" \
"v_accvgpr_write_b32 a37, v223\n" \
"v_accvgpr_write_b32 a38, v224\n" \
"v_accvgpr_write_b32 a39, v225\n" \
"v_accvgpr_read_b32 v222, a40\n" \
"v_accvgpr_read_b32 v223, a41\n" \
"v_accvgpr_read_b32 v224, a42\n" \
"v_accvgpr_read_b32 v225, a43\n" \
"v_mul_f32 v222, v222, v218\n" \
"v_mul_f32 v223, v223, v218\n" \
"v_mul_f32 v224, v224, v218\n" \
"v_mul_f32 v225, v225, v218\n" \
"v_accvgpr_write_b32 a40, v222\n" \
"v_accvgpr_write_b32 a41, v223\n" \
"v_accvgpr_write_b32 a42, v224\n" \
"v_accvgpr_write_b32 a43, v225\n" \
"v_accvgpr_read_b32 v222, a44\n" \
"v_accvgpr_read_b32 v223, a45\n" \
"v_accvgpr_read_b32 v224, a46\n" \
"v_accvgpr_read_b32 v225, a47\n" \
"v_mul_f32 v222, v222, v219\n" \
"v_mul_f32 v223, v223, v219\n" \
"v_mul_f32 v224, v224, v219\n" \
"v_mul_f32 v225, v225, v219\n" \
"v_accvgpr_write_b32 a44, v222\n" \
"v_accvgpr_write_b32 a45, v223\n" \
"v_accvgpr_write_b32 a46, v224\n" \
"v_accvgpr_write_b32 a47, v225\n" \
"v_accvgpr_read_b32 v222, a48\n" \
"v_accvgpr_read_b32 v223, a49\n" \
"v_accvgpr_read_b32 v224, a50\n" \
"v_accvgpr_read_b32 v225, a51\n" \
"v_mul_f32 v222, v222, v218\n" \
"v_mul_f32 v223, v223, v218\n" \
"v_mul_f32 v224, v224, v218\n" \
"v_mul_f32 v225, v225, v218\n" \
"v_accvgpr_write_b32 a48, v222\n" \
"v_accvgpr_write_b32 a49, v223\n" \
"v_accvgpr_write_b32 a50, v224\n" \
"v_accvgpr_write_b32 a51, v225\n" \
"v_accvgpr_read_b32 v222, a52\n" \
"v_accvgpr_read_b32 v223, a53\n" \
"v_accvgpr_read_b32 v224, a54\n" \
"v_accvgpr_read_b32 v225, a55\n" \
"v_mul_f32 v222, v222, v219\n" \
"v_mul_f32 v223, v223, v219\n" \
"v_mul_f32 v224, v224, v219\n" \
"v_mul_f32 v225, v225, v219\n" \
"v_accvgpr_write_b32 a52, v222\n" \
"v_accvgpr_write_b32 a53, v223\n" \
"v_accvgpr_write_b32 a54, v224\n" \
"v_accvgpr_write_b32 a55, v225\n" \
"v_accvgpr_read_b32 v222, a56\n" \
"v_accvgpr_read_b32 v223, a57\n" \
"v_accvgpr_read_b32 v224, a58\n" \
"v_accvgpr_read_b32 v225, a59\n" \
"v_mul_f32 v222, v222, v218\n" \
"v_mul_f32 v223, v223, v218\n" \
"v_mul_f32 v224, v224, v218\n" \
"v_mul_f32 v225, v225, v218\n" \
"v_accvgpr_write_b32 a56, v222\n" \
"v_accvgpr_write_b32 a57, v223\n" \
"v_accvgpr_write_b32 a58, v224\n" \
"v_accvgpr_write_b32 a59, v225\n" \
"v_accvgpr_read_b32 v222, a60\n" \
"v_accvgpr_read_b32 v223, a61\n" \
"v_accvgpr_read_b32 v224, a62\n" \
"v_accvgpr_read_b32 v225, a63\n" \
"v_mul_f32 v222, v222, v219\n" \
"v_mul_f32 v223, v223, v219\n" \
"v_mul_f32 v224, v224, v219\n" \
"v_mul_f32 v225, v225, v219\n" \
"v_accvgpr_write_b32 a60, v222\n" \
"v_accvgpr_write_b32 a61, v223\n" \
"v_accvgpr_write_b32 a62, v224\n" \
"v_accvgpr_write_b32 a63, v225\n" \
"v_accvgpr_read_b32 v222, a64\n" \
"v_accvgpr_read_b32 v223, a65\n" \
"v_accvgpr_read_b32 v224, a66\n" \
"v_accvgpr_read_b32 v225, a67\n" \
"v_mul_f32 v222, v222, v218\n" \
"v_mul_f32 v223, v223, v218\n" \
"v_mul_f32 v224, v224, v218\n" \
"v_mul_f32 v225, v225, v218\n" \
"v_accvgpr_write_b32 a64, v222\n" \
"v_accvgpr_write_b32 a65, v223\n" \
"v_accvgpr_write_b32 a66, v224\n" \
"v_accvgpr_write_b32 a67, v225\n" \
"v_accvgpr_read_b32 v222, a68\n" \
"v_accvgpr_read_b32 v223, a69\n" \
"v_accvgpr_read_b32 v224, a70\n" \
"v_accvgpr_read_b32 v225, a71\n" \
"v_mul_f32 v222, v222, v219\n" \
"v_mul_f32 v223, v223, v219\n" \
"v_mul_f32 v224, v224, v219\n" \
"v_mul_f32 v225, v225, v219\n" \
"v_accvgpr_write_b32 a68, v222\n" \
"v_accvgpr_write_b32 a69, v223\n" \
"v_accvgpr_write_b32 a70, v224\n" \
"v_accvgpr_write_b32 a71, v225\n" \
"v_accvgpr_read_b32 v222, a72\n" \
"v_accvgpr_read_b32 v223, a73\n" \
"v_accvgpr_read_b32 v224, a74\n" \
"v_accvgpr_read_b32 v225, a75\n" \
"v_mul_f32 v222, v222, v218\n" \
"v_mul_f32 v223, v223, v218\n" \
"v_mul_f32 v224, v224, v218\n" \
"v_mul_f32 v225, v225, v218\n" \
"v_accvgpr_write_b32 a72, v222\n" \
"v_accvgpr_write_b32 a73, v223\n" \
"v_accvgpr_write_b32 a74, v224\n" \
"v_accvgpr_write_b32 a75, v225\n" \
"v_accvgpr_read_b32 v222, a76\n" \
"v_accvgpr_read_b32 v223, a77\n" \
"v_accvgpr_read_b32 v224, a78\n" \
"v_accvgpr_read_b32 v225, a79\n" \
"v_mul_f32 v222, v222, v219\n" \
"v_mul_f32 v223, v223, v219\n" \
"v_mul_f32 v224, v224, v219\n" \
"v_mul_f32 v225, v225, v219\n" \
"v_accvgpr_write_b32 a76, v222\n" \
"v_accvgpr_write_b32 a77, v223\n" \
"v_accvgpr_write_b32 a78, v224\n" \
"v_accvgpr_write_b32 a79, v225\n" \
"v_accvgpr_read_b32 v222, a80\n" \
"v_accvgpr_read_b32 v223, a81\n" \
"v_accvgpr_read_b32 v224, a82\n" \
"v_accvgpr_read_b32 v225, a83\n" \
"v_mul_f32 v222, v222, v218\n" \
"v_mul_f32 v223, v223, v218\n" \
"v_mul_f32 v224, v224, v218\n" \
"v_mul_f32 v225, v225, v218\n" \
"v_accvgpr_write_b32 a80, v222\n" \
"v_accvgpr_write_b32 a81, v223\n" \
"v_accvgpr_write_b32 a82, v224\n" \
"v_accvgpr_write_b32 a83, v225\n" \
"v_accvgpr_read_b32 v222, a84\n" \
"v_accvgpr_read_b32 v223, a85\n" \
"v_accvgpr_read_b32 v224, a86\n" \
"v_accvgpr_read_b32 v225, a87\n" \
"v_mul_f32 v222, v222, v219\n" \
"v_mul_f32 v223, v223, v219\n" \
"v_mul_f32 v224, v224, v219\n" \
"v_mul_f32 v225, v225, v219\n" \
"v_accvgpr_write_b32 a84, v222\n" \
"v_accvgpr_write_b32 a85, v223\n" \
"v_accvgpr_write_b32 a86, v224\n" \
"v_accvgpr_write_b32 a87, v225\n" \
"v_accvgpr_read_b32 v222, a88\n" \
"v_accvgpr_read_b32 v223, a89\n" \
"v_accvgpr_read_b32 v224, a90\n" \
"v_accvgpr_read_b32 v225, a91\n" \
"v_mul_f32 v222, v222, v218\n" \
"v_mul_f32 v223, v223, v218\n" \
"v_mul_f32 v224, v224, v218\n" \
"v_mul_f32 v225, v225, v218\n" \
"v_accvgpr_write_b32 a88, v222\n" \
"v_accvgpr_write_b32 a89, v223\n" \
"v_accvgpr_write_b32 a90, v224\n" \
"v_accvgpr_write_b32 a91, v225\n" \
"v_accvgpr_read_b32 v222, a92\n" \
"v_accvgpr_read_b32 v223, a93\n" \
"v_accvgpr_read_b32 v224, a94\n" \
"v_accvgpr_read_b32 v225, a95\n" \
"v_mul_f32 v222, v222, v219\n" \
"v_mul_f32 v223, v223, v219\n" \
"v_mul_f32 v224, v224, v219\n" \
"v_mul_f32 v225, v225, v219\n" \
"v_accvgpr_write_b32 a92, v222\n" \
"v_accvgpr_write_b32 a93, v223\n" \
"v_accvgpr_write_b32 a94, v224\n" \
"v_accvgpr_write_b32 a95, v225\n" \
"v_accvgpr_read_b32 v222, a96\n" \
"v_accvgpr_read_b32 v223, a97\n" \
"v_accvgpr_read_b32 v224, a98\n" \
"v_accvgpr_read_b32 v225, a99\n" \
"v_mul_f32 v222, v222, v218\n" \
"v_mul_f32 v223, v223, v218\n" \
"v_mul_f32 v224, v224, v218\n" \
"v_mul_f32 v225, v225, v218\n" \
"v_accvgpr_write_b32 a96, v222\n" \
"v_accvgpr_write_b32 a97, v223\n" \
"v_accvgpr_write_b32 a98, v224\n" \
"v_accvgpr_write_b32 a99, v225\n" \
"v_accvgpr_read_b32 v222, a100\n" \
"v_accvgpr_read_b32 v223, a101\n" \
"v_accvgpr_read_b32 v224, a102\n" \
"v_accvgpr_read_b32 v225, a103\n" \
"v_mul_f32 v222, v222, v219\n" \
"v_mul_f32 v223, v223, v219\n" \
"v_mul_f32 v224, v224, v219\n" \
"v_mul_f32 v225, v225, v219\n" \
"v_accvgpr_write_b32 a100, v222\n" \
"v_accvgpr_write_b32 a101, v223\n" \
"v_accvgpr_write_b32 a102, v224\n" \
"v_accvgpr_write_b32 a103, v225\n" \
"v_accvgpr_read_b32 v222, a104\n" \
"v_accvgpr_read_b32 v223, a105\n" \
"v_accvgpr_read_b32 v224, a106\n" \
"v_accvgpr_read_b32 v225, a107\n" \
"v_mul_f32 v222, v222, v218\n" \
"v_mul_f32 v223, v223, v218\n" \
"v_mul_f32 v224, v224, v218\n" \
"v_mul_f32 v225, v225, v218\n" \
"v_accvgpr_write_b32 a104, v222\n" \
"v_accvgpr_write_b32 a105, v223\n" \
"v_accvgpr_write_b32 a106, v224\n" \
"v_accvgpr_write_b32 a107, v225\n" \
"v_accvgpr_read_b32 v222, a108\n" \
"v_accvgpr_read_b32 v223, a109\n" \
"v_accvgpr_read_b32 v224, a110\n" \
"v_accvgpr_read_b32 v225, a111\n" \
"v_mul_f32 v222, v222, v219\n" \
"v_mul_f32 v223, v223, v219\n" \
"v_mul_f32 v224, v224, v219\n" \
"v_mul_f32 v225, v225, v219\n" \
"v_accvgpr_write_b32 a108, v222\n" \
"v_accvgpr_write_b32 a109, v223\n" \
"v_accvgpr_write_b32 a110, v224\n" \
"v_accvgpr_write_b32 a111, v225\n" \
"v_accvgpr_read_b32 v222, a112\n" \
"v_accvgpr_read_b32 v223, a113\n" \
"v_accvgpr_read_b32 v224, a114\n" \
"v_accvgpr_read_b32 v225, a115\n" \
"v_mul_f32 v222, v222, v218\n" \
"v_mul_f32 v223, v223, v218\n" \
"v_mul_f32 v224, v224, v218\n" \
"v_mul_f32 v225, v225, v218\n" \
"v_accvgpr_write_b32 a112, v222\n" \
"v_accvgpr_write_b32 a113, v223\n" \
"v_accvgpr_write_b32 a114, v224\n" \
"v_accvgpr_write_b32 a115, v225\n" \
"v_accvgpr_read_b32 v222, a116\n" \
"v_accvgpr_read_b32 v223, a117\n" \
"v_accvgpr_read_b32 v224, a118\n" \
"v_accvgpr_read_b32 v225, a119\n" \
"v_mul_f32 v222, v222, v219\n" \
"v_mul_f32 v223, v223, v219\n" \
"v_mul_f32 v224, v224, v219\n" \
"v_mul_f32 v225, v225, v219\n" \
"v_accvgpr_write_b32 a116, v222\n" \
"v_accvgpr_write_b32 a117, v223\n" \
"v_accvgpr_write_b32 a118, v224\n" \
"v_accvgpr_write_b32 a119, v225\n" \
"v_accvgpr_read_b32 v222, a120\n" \
"v_accvgpr_read_b32 v223, a121\n" \
"v_accvgpr_read_b32 v224, a122\n" \
"v_accvgpr_read_b32 v225, a123\n" \
"v_mul_f32 v222, v222, v218\n" \
"v_mul_f32 v223, v223, v218\n" \
"v_mul_f32 v224, v224, v218\n" \
"v_mul_f32 v225, v225, v218\n" \
"v_accvgpr_write_b32 a120, v222\n" \
"v_accvgpr_write_b32 a121, v223\n" \
"v_accvgpr_write_b32 a122, v224\n" \
"v_accvgpr_write_b32 a123, v225\n" \
"v_accvgpr_read_b32 v222, a124\n" \
"v_accvgpr_read_b32 v223, a125\n" \
"v_accvgpr_read_b32 v224, a126\n" \
"v_accvgpr_read_b32 v225, a127\n" \
"v_mul_f32 v222, v222, v219\n" \
"v_mul_f32 v223, v223, v219\n" \
"v_mul_f32 v224, v224, v219\n" \
"v_mul_f32 v225, v225, v219\n" \
"v_accvgpr_write_b32 a124, v222\n" \
"v_accvgpr_write_b32 a125, v223\n" \
"v_accvgpr_write_b32 a126, v224\n" \
"v_accvgpr_write_b32 a127, v225\n" \
"L_rared_%=:\n" \
"s_nop 1\n" \
"s_cmp_eq_u32 s55, 0\n" \
"s_cbranch_scc1 L_ret0_%=\n" \
"s_cmp_eq_u32 s55, 1\n" \
"s_cbranch_scc1 L_ret1_%=\n" \
"s_cmp_eq_u32 s55, 2\n" \
"s_cbranch_scc1 L_ret2_%=\n" \
"s_branch L_ret3_%=\n" \
"L_exit_%=:\n" \
"s_waitcnt vmcnt(0) lgkmcnt(0)\n" \
"s_barrier\n" \
"v_mov_b32 v216, v212\n" \
"v_mov_b32 v217, v213\n" \
"s_nop 1\n" \
"v_permlane16_swap_b32 v216, v212\n" \
"v_permlane16_swap_b32 v217, v213\n" \
"s_nop 1\n" \
"v_add_f32 v212, v212, v216\n" \
"v_add_f32 v213, v213, v217\n" \
"v_mov_b32 v216, v212\n" \
"v_mov_b32 v217, v213\n" \
"s_nop 1\n" \
"v_permlane32_swap_b32 v216, v212\n" \
"v_permlane32_swap_b32 v217, v213\n" \
"s_nop 1\n" \
"v_add_f32 v212, v212, v216\n" \
"v_add_f32 v213, v213, v217\n" \
"v_rcp_f32 v212, v212\n" \
"v_rcp_f32 v213, v213\n" \
"v_mov_b32 v48, v188\n" \
"v_mov_b32 v49, v189\n" \
"v_mov_b32 v50, v190\n" \
"v_mov_b32 v51, v191\n" \
"s_add_u32 m0, s50, 0\n" \
"s_mov_b32 s68, 229376\n" \
"v_accvgpr_read_b32 v0, a0\n" \
"v_accvgpr_read_b32 v1, a1\n" \
"v_accvgpr_read_b32 v2, a2\n" \
"v_accvgpr_read_b32 v3, a3\n" \
"v_accvgpr_read_b32 v4, a8\n" \
"v_accvgpr_read_b32 v5, a9\n" \
"v_accvgpr_read_b32 v6, a10\n" \
"v_accvgpr_read_b32 v7, a11\n" \
"buffer_load_dwordx4 v248, s[60:63], s68 offen offset:0 lds\n" \
"v_pk_mul_f32 v[0:1], v[0:1], v[212:213] op_sel:[0,0] op_sel_hi:[1,0]\n" \
"v_pk_mul_f32 v[2:3], v[2:3], v[212:213] op_sel:[0,0] op_sel_hi:[1,0]\n" \
"v_pk_mul_f32 v[4:5], v[4:5], v[212:213] op_sel:[0,0] op_sel_hi:[1,0]\n" \
"v_pk_mul_f32 v[6:7], v[6:7], v[212:213] op_sel:[0,0] op_sel_hi:[1,0]\n" \
"v_cvt_pk_bf16_f32 v8, v0, v1\n" \
"v_cvt_pk_bf16_f32 v9, v2, v3\n" \
"v_cvt_pk_bf16_f32 v10, v4, v5\n" \
"v_cvt_pk_bf16_f32 v11, v6, v7\n" \
"ds_write_b128 v253, v[8:11] offset:0\n" \
"v_accvgpr_read_b32 v16, a4\n" \
"v_accvgpr_read_b32 v17, a5\n" \
"v_accvgpr_read_b32 v18, a6\n" \
"v_accvgpr_read_b32 v19, a7\n" \
"v_accvgpr_read_b32 v20, a12\n" \
"v_accvgpr_read_b32 v21, a13\n" \
"v_accvgpr_read_b32 v22, a14\n" \
"v_accvgpr_read_b32 v23, a15\n" \
"buffer_load_dwordx4 v249, s[60:63], s68 offen offset:1024 lds\n" \
"v_pk_mul_f32 v[16:17], v[16:17], v[212:213] op_sel:[0,1] op_sel_hi:[1,1]\n" \
"v_pk_mul_f32 v[18:19], v[18:19], v[212:213] op_sel:[0,1] op_sel_hi:[1,1]\n" \
"v_pk_mul_f32 v[20:21], v[20:21], v[212:213] op_sel:[0,1] op_sel_hi:[1,1]\n" \
"v_pk_mul_f32 v[22:23], v[22:23], v[212:213] op_sel:[0,1] op_sel_hi:[1,1]\n" \
"v_cvt_pk_bf16_f32 v24, v16, v17\n" \
"v_cvt_pk_bf16_f32 v25, v18, v19\n" \
"v_cvt_pk_bf16_f32 v26, v20, v21\n" \
"v_cvt_pk_bf16_f32 v27, v22, v23\n" \
"ds_write_b128 v253, v[24:27] offset:1024\n" \
"v_accvgpr_read_b32 v32, a16\n" \
"v_accvgpr_read_b32 v33, a17\n" \
"v_accvgpr_read_b32 v34, a18\n" \
"v_accvgpr_read_b32 v35, a19\n" \
"v_accvgpr_read_b32 v36, a24\n" \
"v_accvgpr_read_b32 v37, a25\n" \
"v_accvgpr_read_b32 v38, a26\n" \
"v_accvgpr_read_b32 v39, a27\n" \
"buffer_load_dwordx4 v250, s[60:63], s68 offen offset:2048 lds\n" \
"v_pk_mul_f32 v[32:33], v[32:33], v[212:213] op_sel:[0,0] op_sel_hi:[1,0]\n" \
"v_pk_mul_f32 v[34:35], v[34:35], v[212:213] op_sel:[0,0] op_sel_hi:[1,0]\n" \
"v_pk_mul_f32 v[36:37], v[36:37], v[212:213] op_sel:[0,0] op_sel_hi:[1,0]\n" \
"v_pk_mul_f32 v[38:39], v[38:39], v[212:213] op_sel:[0,0] op_sel_hi:[1,0]\n" \
"v_cvt_pk_bf16_f32 v40, v32, v33\n" \
"v_cvt_pk_bf16_f32 v41, v34, v35\n" \
"v_cvt_pk_bf16_f32 v42, v36, v37\n" \
"v_cvt_pk_bf16_f32 v43, v38, v39\n" \
"ds_write_b128 v253, v[40:43] offset:2048\n" \
"v_accvgpr_read_b32 v0, a20\n" \
"v_accvgpr_read_b32 v1, a21\n" \
"v_accvgpr_read_b32 v2, a22\n" \
"v_accvgpr_read_b32 v3, a23\n" \
"v_accvgpr_read_b32 v4, a28\n" \
"v_accvgpr_read_b32 v5, a29\n" \
"v_accvgpr_read_b32 v6, a30\n" \
"v_accvgpr_read_b32 v7, a31\n" \
"buffer_load_dwordx4 v251, s[60:63], s68 offen offset:3072 lds\n" \
"v_pk_mul_f32 v[0:1], v[0:1], v[212:213] op_sel:[0,1] op_sel_hi:[1,1]\n" \
"v_pk_mul_f32 v[2:3], v[2:3], v[212:213] op_sel:[0,1] op_sel_hi:[1,1]\n" \
"v_pk_mul_f32 v[4:5], v[4:5], v[212:213] op_sel:[0,1] op_sel_hi:[1,1]\n" \
"v_pk_mul_f32 v[6:7], v[6:7], v[212:213] op_sel:[0,1] op_sel_hi:[1,1]\n" \
"v_cvt_pk_bf16_f32 v8, v0, v1\n" \
"v_cvt_pk_bf16_f32 v9, v2, v3\n" \
"v_cvt_pk_bf16_f32 v10, v4, v5\n" \
"v_cvt_pk_bf16_f32 v11, v6, v7\n" \
"ds_write_b128 v253, v[8:11] offset:3072\n" \
"s_add_u32 m0, s50, 16384\n" \
"s_mov_b32 s69, 344064\n" \
"v_accvgpr_read_b32 v16, a32\n" \
"v_accvgpr_read_b32 v17, a33\n" \
"v_accvgpr_read_b32 v18, a34\n" \
"v_accvgpr_read_b32 v19, a35\n" \
"v_accvgpr_read_b32 v20, a40\n" \
"v_accvgpr_read_b32 v21, a41\n" \
"v_accvgpr_read_b32 v22, a42\n" \
"v_accvgpr_read_b32 v23, a43\n" \
"buffer_load_dwordx4 v248, s[60:63], s69 offen offset:0 lds\n" \
"v_pk_mul_f32 v[16:17], v[16:17], v[212:213] op_sel:[0,0] op_sel_hi:[1,0]\n" \
"v_pk_mul_f32 v[18:19], v[18:19], v[212:213] op_sel:[0,0] op_sel_hi:[1,0]\n" \
"v_pk_mul_f32 v[20:21], v[20:21], v[212:213] op_sel:[0,0] op_sel_hi:[1,0]\n" \
"v_pk_mul_f32 v[22:23], v[22:23], v[212:213] op_sel:[0,0] op_sel_hi:[1,0]\n" \
"v_cvt_pk_bf16_f32 v24, v16, v17\n" \
"v_cvt_pk_bf16_f32 v25, v18, v19\n" \
"v_cvt_pk_bf16_f32 v26, v20, v21\n" \
"v_cvt_pk_bf16_f32 v27, v22, v23\n" \
"ds_write_b128 v253, v[24:27] offset:4096\n" \
"v_accvgpr_read_b32 v32, a36\n" \
"v_accvgpr_read_b32 v33, a37\n" \
"v_accvgpr_read_b32 v34, a38\n" \
"v_accvgpr_read_b32 v35, a39\n" \
"v_accvgpr_read_b32 v36, a44\n" \
"v_accvgpr_read_b32 v37, a45\n" \
"v_accvgpr_read_b32 v38, a46\n" \
"v_accvgpr_read_b32 v39, a47\n" \
"buffer_load_dwordx4 v249, s[60:63], s69 offen offset:1024 lds\n" \
"v_pk_mul_f32 v[32:33], v[32:33], v[212:213] op_sel:[0,1] op_sel_hi:[1,1]\n" \
"v_pk_mul_f32 v[34:35], v[34:35], v[212:213] op_sel:[0,1] op_sel_hi:[1,1]\n" \
"v_pk_mul_f32 v[36:37], v[36:37], v[212:213] op_sel:[0,1] op_sel_hi:[1,1]\n" \
"v_pk_mul_f32 v[38:39], v[38:39], v[212:213] op_sel:[0,1] op_sel_hi:[1,1]\n" \
"v_cvt_pk_bf16_f32 v40, v32, v33\n" \
"v_cvt_pk_bf16_f32 v41, v34, v35\n" \
"v_cvt_pk_bf16_f32 v42, v36, v37\n" \
"v_cvt_pk_bf16_f32 v43, v38, v39\n" \
"ds_write_b128 v253, v[40:43] offset:5120\n" \
"v_accvgpr_read_b32 v0, a48\n" \
"v_accvgpr_read_b32 v1, a49\n" \
"v_accvgpr_read_b32 v2, a50\n" \
"v_accvgpr_read_b32 v3, a51\n" \
"v_accvgpr_read_b32 v4, a56\n" \
"v_accvgpr_read_b32 v5, a57\n" \
"v_accvgpr_read_b32 v6, a58\n" \
"v_accvgpr_read_b32 v7, a59\n" \
"buffer_load_dwordx4 v250, s[60:63], s69 offen offset:2048 lds\n" \
"v_pk_mul_f32 v[0:1], v[0:1], v[212:213] op_sel:[0,0] op_sel_hi:[1,0]\n" \
"v_pk_mul_f32 v[2:3], v[2:3], v[212:213] op_sel:[0,0] op_sel_hi:[1,0]\n" \
"v_pk_mul_f32 v[4:5], v[4:5], v[212:213] op_sel:[0,0] op_sel_hi:[1,0]\n" \
"v_pk_mul_f32 v[6:7], v[6:7], v[212:213] op_sel:[0,0] op_sel_hi:[1,0]\n" \
"v_cvt_pk_bf16_f32 v8, v0, v1\n" \
"v_cvt_pk_bf16_f32 v9, v2, v3\n" \
"v_cvt_pk_bf16_f32 v10, v4, v5\n" \
"v_cvt_pk_bf16_f32 v11, v6, v7\n" \
"ds_write_b128 v253, v[8:11] offset:6144\n" \
"v_accvgpr_read_b32 v16, a52\n" \
"v_accvgpr_read_b32 v17, a53\n" \
"v_accvgpr_read_b32 v18, a54\n" \
"v_accvgpr_read_b32 v19, a55\n" \
"v_accvgpr_read_b32 v20, a60\n" \
"v_accvgpr_read_b32 v21, a61\n" \
"v_accvgpr_read_b32 v22, a62\n" \
"v_accvgpr_read_b32 v23, a63\n" \
"buffer_load_dwordx4 v251, s[60:63], s69 offen offset:3072 lds\n" \
"v_pk_mul_f32 v[16:17], v[16:17], v[212:213] op_sel:[0,1] op_sel_hi:[1,1]\n" \
"v_pk_mul_f32 v[18:19], v[18:19], v[212:213] op_sel:[0,1] op_sel_hi:[1,1]\n" \
"v_pk_mul_f32 v[20:21], v[20:21], v[212:213] op_sel:[0,1] op_sel_hi:[1,1]\n" \
"v_pk_mul_f32 v[22:23], v[22:23], v[212:213] op_sel:[0,1] op_sel_hi:[1,1]\n" \
"v_cvt_pk_bf16_f32 v24, v16, v17\n" \
"v_cvt_pk_bf16_f32 v25, v18, v19\n" \
"v_cvt_pk_bf16_f32 v26, v20, v21\n" \
"v_cvt_pk_bf16_f32 v27, v22, v23\n" \
"ds_write_b128 v253, v[24:27] offset:7168\n" \
"s_add_u32 m0, s50, 32768\n" \
"s_mov_b32 s68, 128\n" \
"v_accvgpr_read_b32 v32, a64\n" \
"v_accvgpr_read_b32 v33, a65\n" \
"v_accvgpr_read_b32 v34, a66\n" \
"v_accvgpr_read_b32 v35, a67\n" \
"v_accvgpr_read_b32 v36, a72\n" \
"v_accvgpr_read_b32 v37, a73\n" \
"v_accvgpr_read_b32 v38, a74\n" \
"v_accvgpr_read_b32 v39, a75\n" \
"buffer_load_dwordx4 v248, s[60:63], s68 offen offset:0 lds\n" \
"v_pk_mul_f32 v[32:33], v[32:33], v[212:213] op_sel:[0,0] op_sel_hi:[1,0]\n" \
"v_pk_mul_f32 v[34:35], v[34:35], v[212:213] op_sel:[0,0] op_sel_hi:[1,0]\n" \
"v_pk_mul_f32 v[36:37], v[36:37], v[212:213] op_sel:[0,0] op_sel_hi:[1,0]\n" \
"v_pk_mul_f32 v[38:39], v[38:39], v[212:213] op_sel:[0,0] op_sel_hi:[1,0]\n" \
"v_cvt_pk_bf16_f32 v40, v32, v33\n" \
"v_cvt_pk_bf16_f32 v41, v34, v35\n" \
"v_cvt_pk_bf16_f32 v42, v36, v37\n" \
"v_cvt_pk_bf16_f32 v43, v38, v39\n" \
"ds_write_b128 v253, v[40:43] offset:8192\n" \
"v_accvgpr_read_b32 v0, a68\n" \
"v_accvgpr_read_b32 v1, a69\n" \
"v_accvgpr_read_b32 v2, a70\n" \
"v_accvgpr_read_b32 v3, a71\n" \
"v_accvgpr_read_b32 v4, a76\n" \
"v_accvgpr_read_b32 v5, a77\n" \
"v_accvgpr_read_b32 v6, a78\n" \
"v_accvgpr_read_b32 v7, a79\n" \
"buffer_load_dwordx4 v249, s[60:63], s68 offen offset:1024 lds\n" \
"v_pk_mul_f32 v[0:1], v[0:1], v[212:213] op_sel:[0,1] op_sel_hi:[1,1]\n" \
"v_pk_mul_f32 v[2:3], v[2:3], v[212:213] op_sel:[0,1] op_sel_hi:[1,1]\n" \
"v_pk_mul_f32 v[4:5], v[4:5], v[212:213] op_sel:[0,1] op_sel_hi:[1,1]\n" \
"v_pk_mul_f32 v[6:7], v[6:7], v[212:213] op_sel:[0,1] op_sel_hi:[1,1]\n" \
"v_cvt_pk_bf16_f32 v8, v0, v1\n" \
"v_cvt_pk_bf16_f32 v9, v2, v3\n" \
"v_cvt_pk_bf16_f32 v10, v4, v5\n" \
"v_cvt_pk_bf16_f32 v11, v6, v7\n" \
"ds_write_b128 v253, v[8:11] offset:9216\n" \
"v_accvgpr_read_b32 v16, a80\n" \
"v_accvgpr_read_b32 v17, a81\n" \
"v_accvgpr_read_b32 v18, a82\n" \
"v_accvgpr_read_b32 v19, a83\n" \
"v_accvgpr_read_b32 v20, a88\n" \
"v_accvgpr_read_b32 v21, a89\n" \
"v_accvgpr_read_b32 v22, a90\n" \
"v_accvgpr_read_b32 v23, a91\n" \
"buffer_load_dwordx4 v250, s[60:63], s68 offen offset:2048 lds\n" \
"v_pk_mul_f32 v[16:17], v[16:17], v[212:213] op_sel:[0,0] op_sel_hi:[1,0]\n" \
"v_pk_mul_f32 v[18:19], v[18:19], v[212:213] op_sel:[0,0] op_sel_hi:[1,0]\n" \
"v_pk_mul_f32 v[20:21], v[20:21], v[212:213] op_sel:[0,0] op_sel_hi:[1,0]\n" \
"v_pk_mul_f32 v[22:23], v[22:23], v[212:213] op_sel:[0,0] op_sel_hi:[1,0]\n" \
"v_cvt_pk_bf16_f32 v24, v16, v17\n" \
"v_cvt_pk_bf16_f32 v25, v18, v19\n" \
"v_cvt_pk_bf16_f32 v26, v20, v21\n" \
"v_cvt_pk_bf16_f32 v27, v22, v23\n" \
"ds_write_b128 v253, v[24:27] offset:10240\n" \
"v_accvgpr_read_b32 v32, a84\n" \
"v_accvgpr_read_b32 v33, a85\n" \
"v_accvgpr_read_b32 v34, a86\n" \
"v_accvgpr_read_b32 v35, a87\n" \
"v_accvgpr_read_b32 v36, a92\n" \
"v_accvgpr_read_b32 v37, a93\n" \
"v_accvgpr_read_b32 v38, a94\n" \
"v_accvgpr_read_b32 v39, a95\n" \
"buffer_load_dwordx4 v251, s[60:63], s68 offen offset:3072 lds\n" \
"v_pk_mul_f32 v[32:33], v[32:33], v[212:213] op_sel:[0,1] op_sel_hi:[1,1]\n" \
"v_pk_mul_f32 v[34:35], v[34:35], v[212:213] op_sel:[0,1] op_sel_hi:[1,1]\n" \
"v_pk_mul_f32 v[36:37], v[36:37], v[212:213] op_sel:[0,1] op_sel_hi:[1,1]\n" \
"v_pk_mul_f32 v[38:39], v[38:39], v[212:213] op_sel:[0,1] op_sel_hi:[1,1]\n" \
"v_cvt_pk_bf16_f32 v40, v32, v33\n" \
"v_cvt_pk_bf16_f32 v41, v34, v35\n" \
"v_cvt_pk_bf16_f32 v42, v36, v37\n" \
"v_cvt_pk_bf16_f32 v43, v38, v39\n" \
"ds_write_b128 v253, v[40:43] offset:11264\n" \
"s_add_u32 m0, s50, 49152\n" \
"s_mov_b32 s69, 114816\n" \
"v_accvgpr_read_b32 v0, a96\n" \
"v_accvgpr_read_b32 v1, a97\n" \
"v_accvgpr_read_b32 v2, a98\n" \
"v_accvgpr_read_b32 v3, a99\n" \
"v_accvgpr_read_b32 v4, a104\n" \
"v_accvgpr_read_b32 v5, a105\n" \
"v_accvgpr_read_b32 v6, a106\n" \
"v_accvgpr_read_b32 v7, a107\n" \
"buffer_load_dwordx4 v248, s[60:63], s69 offen offset:0 lds\n" \
"v_pk_mul_f32 v[0:1], v[0:1], v[212:213] op_sel:[0,0] op_sel_hi:[1,0]\n" \
"v_pk_mul_f32 v[2:3], v[2:3], v[212:213] op_sel:[0,0] op_sel_hi:[1,0]\n" \
"v_pk_mul_f32 v[4:5], v[4:5], v[212:213] op_sel:[0,0] op_sel_hi:[1,0]\n" \
"v_pk_mul_f32 v[6:7], v[6:7], v[212:213] op_sel:[0,0] op_sel_hi:[1,0]\n" \
"v_cvt_pk_bf16_f32 v8, v0, v1\n" \
"v_cvt_pk_bf16_f32 v9, v2, v3\n" \
"v_cvt_pk_bf16_f32 v10, v4, v5\n" \
"v_cvt_pk_bf16_f32 v11, v6, v7\n" \
"ds_write_b128 v253, v[8:11] offset:12288\n" \
"v_accvgpr_read_b32 v16, a100\n" \
"v_accvgpr_read_b32 v17, a101\n" \
"v_accvgpr_read_b32 v18, a102\n" \
"v_accvgpr_read_b32 v19, a103\n" \
"v_accvgpr_read_b32 v20, a108\n" \
"v_accvgpr_read_b32 v21, a109\n" \
"v_accvgpr_read_b32 v22, a110\n" \
"v_accvgpr_read_b32 v23, a111\n" \
"buffer_load_dwordx4 v249, s[60:63], s69 offen offset:1024 lds\n" \
"v_pk_mul_f32 v[16:17], v[16:17], v[212:213] op_sel:[0,1] op_sel_hi:[1,1]\n" \
"v_pk_mul_f32 v[18:19], v[18:19], v[212:213] op_sel:[0,1] op_sel_hi:[1,1]\n" \
"v_pk_mul_f32 v[20:21], v[20:21], v[212:213] op_sel:[0,1] op_sel_hi:[1,1]\n" \
"v_pk_mul_f32 v[22:23], v[22:23], v[212:213] op_sel:[0,1] op_sel_hi:[1,1]\n" \
"v_cvt_pk_bf16_f32 v24, v16, v17\n" \
"v_cvt_pk_bf16_f32 v25, v18, v19\n" \
"v_cvt_pk_bf16_f32 v26, v20, v21\n" \
"v_cvt_pk_bf16_f32 v27, v22, v23\n" \
"ds_write_b128 v253, v[24:27] offset:13312\n" \
"v_accvgpr_read_b32 v32, a112\n" \
"v_accvgpr_read_b32 v33, a113\n" \
"v_accvgpr_read_b32 v34, a114\n" \
"v_accvgpr_read_b32 v35, a115\n" \
"v_accvgpr_read_b32 v36, a120\n" \
"v_accvgpr_read_b32 v37, a121\n" \
"v_accvgpr_read_b32 v38, a122\n" \
"v_accvgpr_read_b32 v39, a123\n" \
"buffer_load_dwordx4 v250, s[60:63], s69 offen offset:2048 lds\n" \
"v_pk_mul_f32 v[32:33], v[32:33], v[212:213] op_sel:[0,0] op_sel_hi:[1,0]\n" \
"v_pk_mul_f32 v[34:35], v[34:35], v[212:213] op_sel:[0,0] op_sel_hi:[1,0]\n" \
"v_pk_mul_f32 v[36:37], v[36:37], v[212:213] op_sel:[0,0] op_sel_hi:[1,0]\n" \
"v_pk_mul_f32 v[38:39], v[38:39], v[212:213] op_sel:[0,0] op_sel_hi:[1,0]\n" \
"v_cvt_pk_bf16_f32 v40, v32, v33\n" \
"v_cvt_pk_bf16_f32 v41, v34, v35\n" \
"v_cvt_pk_bf16_f32 v42, v36, v37\n" \
"v_cvt_pk_bf16_f32 v43, v38, v39\n" \
"ds_write_b128 v253, v[40:43] offset:14336\n" \
"v_accvgpr_read_b32 v0, a116\n" \
"v_accvgpr_read_b32 v1, a117\n" \
"v_accvgpr_read_b32 v2, a118\n" \
"v_accvgpr_read_b32 v3, a119\n" \
"v_accvgpr_read_b32 v4, a124\n" \
"v_accvgpr_read_b32 v5, a125\n" \
"v_accvgpr_read_b32 v6, a126\n" \
"v_accvgpr_read_b32 v7, a127\n" \
"buffer_load_dwordx4 v251, s[60:63], s69 offen offset:3072 lds\n" \
"v_pk_mul_f32 v[0:1], v[0:1], v[212:213] op_sel:[0,1] op_sel_hi:[1,1]\n" \
"v_pk_mul_f32 v[2:3], v[2:3], v[212:213] op_sel:[0,1] op_sel_hi:[1,1]\n" \
"v_pk_mul_f32 v[4:5], v[4:5], v[212:213] op_sel:[0,1] op_sel_hi:[1,1]\n" \
"v_pk_mul_f32 v[6:7], v[6:7], v[212:213] op_sel:[0,1] op_sel_hi:[1,1]\n" \
"v_cvt_pk_bf16_f32 v8, v0, v1\n" \
"v_cvt_pk_bf16_f32 v9, v2, v3\n" \
"v_cvt_pk_bf16_f32 v10, v4, v5\n" \
"v_cvt_pk_bf16_f32 v11, v6, v7\n" \
"ds_write_b128 v253, v[8:11] offset:15360\n" \
"buffer_load_dwordx4 a[128:131], v52, s[76:79], 0 offen offset:0 sc1\n" \
"buffer_load_dwordx4 a[132:135], v52, s[76:79], 0 offen offset:3584 sc1\n" \
"v_add_u32 v52, 7168, v52\n" \
"buffer_load_dwordx4 a[136:139], v52, s[76:79], 0 offen offset:0 sc1\n" \
"buffer_load_dwordx4 a[140:143], v52, s[76:79], 0 offen offset:3584 sc1\n" \
"v_add_u32 v52, 7168, v52\n" \
"buffer_load_dwordx4 a[144:147], v52, s[76:79], 0 offen offset:0 sc1\n" \
"buffer_load_dwordx4 a[148:151], v52, s[76:79], 0 offen offset:3584 sc1\n" \
"v_add_u32 v52, 7168, v52\n" \
"buffer_load_dwordx4 a[152:155], v52, s[76:79], 0 offen offset:0 sc1\n" \
"buffer_load_dwordx4 a[156:159], v52, s[76:79], 0 offen offset:3584 sc1\n" \
"v_add_u32 v52, 7168, v52\n" \
"buffer_load_dwordx2 a[160:161], v53, s[72:75], 0 offen offset:0\n" \
"buffer_load_dwordx2 a[162:163], v53, s[72:75], 0 offen offset:32\n" \
"buffer_load_dwordx2 a[164:165], v53, s[72:75], 0 offen offset:128\n" \
"buffer_load_dwordx2 a[166:167], v53, s[72:75], 0 offen offset:160\n" \
"buffer_load_dword a168, v59, s[36:39], 0 offen\n" \
"buffer_load_dword a168, v58, s[32:35], 0 offen\n" \
"s_waitcnt vmcnt(30) lgkmcnt(0)\n" \
"s_barrier\n" \
"ds_read_b128 v[64:67], v254 offset:0\n" \
"ds_read_b128 v[72:75], v254 offset:2048\n" \
"ds_read_b128 v[80:83], v254 offset:4096\n" \
"ds_read_b128 v[88:91], v254 offset:6144\n" \
"ds_read_b128 v[96:99], v254 offset:8192\n" \
"ds_read_b128 v[104:107], v254 offset:10240\n" \
"ds_read_b128 v[112:115], v254 offset:12288\n" \
"ds_read_b128 v[120:123], v254 offset:14336\n" \
"ds_read_b128 v[68:71], v254 offset:1024\n" \
"ds_read_b128 v[76:79], v254 offset:3072\n" \
"ds_read_b128 v[84:87], v254 offset:5120\n" \
"ds_read_b128 v[92:95], v254 offset:7168\n" \
"ds_read_b128 v[100:103], v254 offset:9216\n" \
"ds_read_b128 v[108:111], v254 offset:11264\n" \
"ds_read_b128 v[116:119], v254 offset:13312\n" \
"ds_read_b128 v[124:127], v254 offset:15360\n" \
"ds_read_b64_tr_b16 a[192:193], v246 offset:0\n" \
"ds_read_b64_tr_b16 a[194:195], v246 offset:2048\n" \
"ds_read_b64_tr_b16 a[196:197], v246 offset:4096\n" \
"ds_read_b64_tr_b16 a[198:199], v246 offset:6144\n" \
"ds_read_b64_tr_b16 a[200:201], v246 offset:8192\n" \
"ds_read_b64_tr_b16 a[202:203], v246 offset:10240\n" \
"ds_read_b64_tr_b16 a[204:205], v246 offset:12288\n" \
"ds_read_b64_tr_b16 a[206:207], v246 offset:14336\n" \
"ds_read_b64_tr_b16 a[208:209], v246 offset:16384\n" \
"ds_read_b64_tr_b16 a[210:211], v246 offset:18432\n" \
"ds_read_b64_tr_b16 a[212:213], v246 offset:20480\n" \
"ds_read_b64_tr_b16 a[214:215], v246 offset:22528\n" \
"ds_read_b64_tr_b16 a[216:217], v246 offset:24576\n" \
"ds_read_b64_tr_b16 a[218:219], v246 offset:26624\n" \
"ds_read_b64_tr_b16 a[220:221], v246 offset:28672\n" \
"ds_read_b64_tr_b16 a[222:223], v246 offset:30720\n" \
"s_waitcnt lgkmcnt(14)\n" \
"v_mfma_f32_16x16x32_bf16 v[0:3], a[192:195], v[64:67], 0\n" \
"s_waitcnt lgkmcnt(12)\n" \
"ds_read_b64_tr_b16 a[224:225], v247 offset:0\n" \
"ds_read_b64_tr_b16 a[226:227], v247 offset:2048\n" \
"v_mfma_f32_16x16x32_bf16 v[0:3], a[196:199], v[72:75], v[0:3]\n" \
"s_waitcnt lgkmcnt(12)\n" \
"ds_read_b128 v[128:131], v254 offset:32768\n" \
"ds_read_b128 v[136:139], v254 offset:34816\n" \
"v_mfma_f32_16x16x32_bf16 v[0:3], a[200:203], v[80:83], v[0:3]\n" \
"s_waitcnt lgkmcnt(12)\n" \
"ds_read_b64_tr_b16 a[228:229], v247 offset:4096\n" \
"ds_read_b64_tr_b16 a[230:231], v247 offset:6144\n" \
"v_mfma_f32_16x16x32_bf16 v[0:3], a[204:207], v[88:91], v[0:3]\n" \
"s_waitcnt lgkmcnt(12)\n" \
"ds_read_b128 v[144:147], v254 offset:36864\n" \
"ds_read_b128 v[152:155], v254 offset:38912\n" \
"v_mfma_f32_16x16x32_bf16 v[0:3], a[208:211], v[96:99], v[0:3]\n" \
"s_waitcnt lgkmcnt(12)\n" \
"ds_read_b64_tr_b16 a[232:233], v247 offset:8192\n" \
"ds_read_b64_tr_b16 a[234:235], v247 offset:10240\n" \
"v_mfma_f32_16x16x32_bf16 v[0:3], a[212:215], v[104:107], v[0:3]\n" \
"s_waitcnt lgkmcnt(12)\n" \
"ds_read_b128 v[160:163], v254 offset:40960\n" \
"ds_read_b128 v[168:171], v254 offset:43008\n" \
"v_mfma_f32_16x16x32_bf16 v[0:3], a[216:219], v[112:115], v[0:3]\n" \
"s_waitcnt lgkmcnt(12)\n" \
"ds_read_b64_tr_b16 a[236:237], v247 offset:12288\n" \
"ds_read_b64_tr_b16 a[238:239], v247 offset:14336\n" \
"v_mfma_f32_16x16x32_bf16 v[0:3], a[220:223], v[120:123], v[0:3]\n" \
"ds_read_b128 v[176:179], v254 offset:45056\n" \
"ds_read_b128 v[184:187], v254 offset:47104\n" \
"v_mfma_f32_16x16x32_bf16 v[4:7], a[192:195], v[68:71], 0\n" \
"ds_read_b64_tr_b16 a[240:241], v247 offset:16384\n" \
"ds_read_b64_tr_b16 a[242:243], v247 offset:18432\n" \
"v_mfma_f32_16x16x32_bf16 v[4:7], a[196:199], v[76:79], v[4:7]\n" \
"ds_read_b128 v[132:135], v254 offset:33792\n" \
"ds_read_b128 v[140:143], v254 offset:35840\n" \
"v_mfma_f32_16x16x32_bf16 v[4:7], a[200:203], v[84:87], v[4:7]\n" \
"ds_read_b64_tr_b16 a[244:245], v247 offset:20480\n" \
"ds_read_b64_tr_b16 a[246:247], v247 offset:22528\n" \
"v_mfma_f32_16x16x32_bf16 v[4:7], a[204:207], v[92:95], v[4:7]\n" \
"ds_read_b128 v[148:151], v254 offset:37888\n" \
"ds_read_b128 v[156:159], v254 offset:39936\n" \
"v_mfma_f32_16x16x32_bf16 v[4:7], a[208:211], v[100:103], v[4:7]\n" \
"ds_read_b64_tr_b16 a[248:249], v247 offset:24576\n" \
"ds_read_b64_tr_b16 a[250:251], v247 offset:26624\n" \
"v_mfma_f32_16x16x32_bf16 v[4:7], a[212:215], v[108:111], v[4:7]\n" \
"s_waitcnt lgkmcnt(12)\n" \
"ds_read_b128 v[164:167], v254 offset:41984\n" \
"ds_read_b128 v[172:175], v254 offset:44032\n" \
"v_mfma_f32_16x16x32_bf16 v[4:7], a[216:219], v[116:119], v[4:7]\n" \
"s_waitcnt vmcnt(22)\n" \
"ds_read_b64_tr_b16 a[252:253], v247 offset:28672\n" \
"ds_read_b64_tr_b16 a[254:255], v247 offset:30720\n" \
"v_mfma_f32_16x16x32_bf16 v[4:7], a[220:223], v[124:127], v[4:7]\n" \
"s_barrier\n" \
"ds_read_b128 v[180:183], v254 offset:46080\n" \
"ds_read_b128 v[188:191], v254 offset:48128\n" \
"v_mfma_f32_16x16x32_bf16 v[8:11], a[224:227], v[64:67], 0\n" \
"ds_read_b64_tr_b16 a[192:193], v242 offset:0\n" \
"ds_read_b64_tr_b16 a[194:195], v242 offset:2048\n" \
"v_mfma_f32_16x16x32_bf16 v[8:11], a[228:231], v[72:75], v[8:11]\n" \
"s_mov_b32 s68, 229504\n" \
"v_mfma_f32_16x16x32_bf16 v[8:11], a[232:235], v[80:83], v[8:11]\n" \
"s_add_u32 m0, s50, 65536\n" \
"ds_read_b64_tr_b16 a[196:197], v242 offset:4096\n" \
"ds_read_b64_tr_b16 a[198:199], v242 offset:6144\n" \
"v_mfma_f32_16x16x32_bf16 v[8:11], a[236:239], v[88:91], v[8:11]\n" \
"s_waitcnt lgkmcnt(10)\n" \
"buffer_load_dwordx4 v248, s[60:63], s68 offen offset:0 lds\n" \
"v_mfma_f32_16x16x32_bf16 v[8:11], a[240:243], v[96:99], v[8:11]\n" \
"ds_read_b64_tr_b16 a[200:201], v242 offset:8192\n" \
"ds_read_b64_tr_b16 a[202:203], v242 offset:10240\n" \
"v_mfma_f32_16x16x32_bf16 v[8:11], a[244:247], v[104:107], v[8:11]\n" \
"v_mfma_f32_16x16x32_bf16 v[8:11], a[248:251], v[112:115], v[8:11]\n" \
"s_waitcnt lgkmcnt(6)\n" \
"ds_read_b64_tr_b16 a[204:205], v242 offset:12288\n" \
"ds_read_b64_tr_b16 a[206:207], v242 offset:14336\n" \
"v_mfma_f32_16x16x32_bf16 v[8:11], a[252:255], v[120:123], v[8:11]\n" \
"buffer_load_dwordx4 v249, s[60:63], s68 offen offset:1024 lds\n" \
"v_mfma_f32_16x16x32_bf16 v[12:15], a[224:227], v[68:71], 0\n" \
"ds_read_b64_tr_b16 a[208:209], v242 offset:16384\n" \
"ds_read_b64_tr_b16 a[210:211], v242 offset:18432\n" \
"v_mfma_f32_16x16x32_bf16 v[12:15], a[228:231], v[76:79], v[12:15]\n" \
"v_mfma_f32_16x16x32_bf16 v[12:15], a[232:235], v[84:87], v[12:15]\n" \
"ds_read_b64_tr_b16 a[212:213], v242 offset:20480\n" \
"ds_read_b64_tr_b16 a[214:215], v242 offset:22528\n" \
"v_mfma_f32_16x16x32_bf16 v[12:15], a[236:239], v[92:95], v[12:15]\n" \
"buffer_load_dwordx4 v250, s[60:63], s68 offen offset:2048 lds\n" \
"v_mfma_f32_16x16x32_bf16 v[12:15], a[240:243], v[100:103], v[12:15]\n" \
"ds_read_b64_tr_b16 a[216:217], v242 offset:24576\n" \
"ds_read_b64_tr_b16 a[218:219], v242 offset:26624\n" \
"v_mfma_f32_16x16x32_bf16 v[12:15], a[244:247], v[108:111], v[12:15]\n" \
"v_mfma_f32_16x16x32_bf16 v[12:15], a[248:251], v[116:119], v[12:15]\n" \
"ds_read_b64_tr_b16 a[220:221], v242 offset:28672\n" \
"ds_read_b64_tr_b16 a[222:223], v242 offset:30720\n" \
"v_mfma_f32_16x16x32_bf16 v[12:15], a[252:255], v[124:127], v[12:15]\n" \
"s_waitcnt lgkmcnt(6)\n" \
"buffer_load_dwordx4 v251, s[60:63], s68 offen offset:3072 lds\n" \
"v_mfma_f32_16x16x32_bf16 v[0:3], a[192:195], v[128:131], v[0:3]\n" \
"ds_read_b64_tr_b16 a[224:225], v243 offset:0\n" \
"ds_read_b64_tr_b16 a[226:227], v243 offset:2048\n" \
"v_mfma_f32_16x16x32_bf16 v[0:3], a[196:199], v[136:139], v[0:3]\n" \
"s_mov_b32 s69, 344192\n" \
"v_mfma_f32_16x16x32_bf16 v[0:3], a[200:203], v[144:147], v[0:3]\n" \
"s_add_u32 m0, s50, 81920\n" \
"ds_read_b64_tr_b16 a[228:229], v243 offset:4096\n" \
"ds_read_b64_tr_b16 a[230:231], v243 offset:6144\n" \
"v_mfma_f32_16x16x32_bf16 v[0:3], a[204:207], v[152:155], v[0:3]\n" \
"buffer_load_dwordx4 v248, s[60:63], s69 offen offset:0 lds\n" \
"v_mfma_f32_16x16x32_bf16 v[0:3], a[208:211], v[160:163], v[0:3]\n" \
"s_waitcnt lgkmcnt(6)\n" \
"ds_read_b64_tr_b16 a[232:233], v243 offset:8192\n" \
"ds_read_b64_tr_b16 a[234:235], v243 offset:10240\n" \
"v_mfma_f32_16x16x32_bf16 v[0:3], a[212:215], v[168:171], v[0:3]\n" \
"v_mfma_f32_16x16x32_bf16 v[0:3], a[216:219], v[176:179], v[0:3]\n" \
"s_waitcnt lgkmcnt(6)\n" \
"ds_read_b64_tr_b16 a[236:237], v243 offset:12288\n" \
"ds_read_b64_tr_b16 a[238:239], v243 offset:14336\n" \
"v_mfma_f32_16x16x32_bf16 v[0:3], a[220:223], v[184:187], v[0:3]\n" \
"buffer_load_dwordx4 v249, s[60:63], s69 offen offset:1024 lds\n" \
"v_mfma_f32_16x16x32_bf16 v[4:7], a[192:195], v[132:135], v[4:7]\n" \
"ds_read_b64_tr_b16 a[240:241], v243 offset:16384\n" \
"ds_read_b64_tr_b16 a[242:243], v243 offset:18432\n" \
"v_mfma_f32_16x16x32_bf16 v[4:7], a[196:199], v[140:143], v[4:7]\n" \
"v_mfma_f32_16x16x32_bf16 v[4:7], a[200:203], v[148:151], v[4:7]\n" \
"ds_read_b64_tr_b16 a[244:245], v243 offset:20480\n" \
"ds_read_b64_tr_b16 a[246:247], v243 offset:22528\n" \
"v_mfma_f32_16x16x32_bf16 v[4:7], a[204:207], v[156:159], v[4:7]\n" \
"buffer_load_dwordx4 v250, s[60:63], s69 offen offset:2048 lds\n" \
"v_mfma_f32_16x16x32_bf16 v[4:7], a[208:211], v[164:167], v[4:7]\n" \
"ds_read_b64_tr_b16 a[248:249], v243 offset:24576\n" \
"ds_read_b64_tr_b16 a[250:251], v243 offset:26624\n" \
"v_mfma_f32_16x16x32_bf16 v[4:7], a[212:215], v[172:175], v[4:7]\n" \
"s_waitcnt lgkmcnt(6)\n" \
"v_mfma_f32_16x16x32_bf16 v[4:7], a[216:219], v[180:183], v[4:7]\n" \
"s_waitcnt vmcnt(21)\n" \
"ds_read_b64_tr_b16 a[252:253], v243 offset:28672\n" \
"ds_read_b64_tr_b16 a[254:255], v243 offset:30720\n" \
"v_mfma_f32_16x16x32_bf16 v[4:7], a[220:223], v[188:191], v[4:7]\n" \
"s_barrier\n" \
"buffer_load_dwordx4 v251, s[60:63], s69 offen offset:3072 lds\n" \
"v_mfma_f32_16x16x32_bf16 v[8:11], a[224:227], v[128:131], v[8:11]\n" \
"ds_read_b64_tr_b16 a[192:193], v242 offset:32768\n" \
"ds_read_b64_tr_b16 a[194:195], v242 offset:34816\n" \
"v_mfma_f32_16x16x32_bf16 v[8:11], a[228:231], v[136:139], v[8:11]\n" \
"s_mov_b32 s68, 256\n" \
"v_mfma_f32_16x16x32_bf16 v[8:11], a[232:235], v[144:147], v[8:11]\n" \
"s_add_u32 m0, s50, 98304\n" \
"ds_read_b64_tr_b16 a[196:197], v242 offset:36864\n" \
"ds_read_b64_tr_b16 a[198:199], v242 offset:38912\n" \
"v_mfma_f32_16x16x32_bf16 v[8:11], a[236:239], v[152:155], v[8:11]\n" \
"s_waitcnt lgkmcnt(6)\n" \
"buffer_load_dwordx4 v248, s[60:63], s68 offen offset:0 lds\n" \
"v_mfma_f32_16x16x32_bf16 v[8:11], a[240:243], v[160:163], v[8:11]\n" \
"ds_read_b64_tr_b16 a[200:201], v242 offset:40960\n" \
"ds_read_b64_tr_b16 a[202:203], v242 offset:43008\n" \
"v_mfma_f32_16x16x32_bf16 v[8:11], a[244:247], v[168:171], v[8:11]\n" \
"buffer_load_dwordx4 v249, s[60:63], s68 offen offset:1024 lds\n" \
"v_mfma_f32_16x16x32_bf16 v[8:11], a[248:251], v[176:179], v[8:11]\n" \
"s_waitcnt lgkmcnt(6)\n" \
"ds_read_b64_tr_b16 a[204:205], v242 offset:45056\n" \
"ds_read_b64_tr_b16 a[206:207], v242 offset:47104\n" \
"v_mfma_f32_16x16x32_bf16 v[8:11], a[252:255], v[184:187], v[8:11]\n" \
"buffer_load_dwordx4 v250, s[60:63], s68 offen offset:2048 lds\n" \
"v_mfma_f32_16x16x32_bf16 v[12:15], a[224:227], v[132:135], v[12:15]\n" \
"ds_read_b64_tr_b16 a[208:209], v242 offset:49152\n" \
"ds_read_b64_tr_b16 a[210:211], v242 offset:51200\n" \
"v_mfma_f32_16x16x32_bf16 v[12:15], a[228:231], v[140:143], v[12:15]\n" \
"s_mov_b32 s69, 114944\n" \
"buffer_load_dwordx4 v251, s[60:63], s68 offen offset:3072 lds\n" \
"v_mfma_f32_16x16x32_bf16 v[12:15], a[232:235], v[148:151], v[12:15]\n" \
"s_add_u32 m0, s50, 114688\n" \
"ds_read_b64_tr_b16 a[212:213], v242 offset:53248\n" \
"ds_read_b64_tr_b16 a[214:215], v242 offset:55296\n" \
"v_mfma_f32_16x16x32_bf16 v[12:15], a[236:239], v[156:159], v[12:15]\n" \
"buffer_load_dwordx4 v248, s[60:63], s69 offen offset:0 lds\n" \
"v_mfma_f32_16x16x32_bf16 v[12:15], a[240:243], v[164:167], v[12:15]\n" \
"ds_read_b64_tr_b16 a[216:217], v242 offset:57344\n" \
"ds_read_b64_tr_b16 a[218:219], v242 offset:59392\n" \
"v_mfma_f32_16x16x32_bf16 v[12:15], a[244:247], v[172:175], v[12:15]\n" \
"buffer_load_dwordx4 v249, s[60:63], s69 offen offset:1024 lds\n" \
"v_mfma_f32_16x16x32_bf16 v[12:15], a[248:251], v[180:183], v[12:15]\n" \
"ds_read_b64_tr_b16 a[220:221], v242 offset:61440\n" \
"ds_read_b64_tr_b16 a[222:223], v242 offset:63488\n" \
"v_mfma_f32_16x16x32_bf16 v[12:15], a[252:255], v[188:191], v[12:15]\n" \
"s_waitcnt lgkmcnt(6)\n" \
"buffer_load_dwordx4 v250, s[60:63], s69 offen offset:2048 lds\n" \
"v_mfma_f32_16x16x32_bf16 v[16:19], a[192:195], v[64:67], 0\n" \
"ds_read_b64_tr_b16 a[224:225], v243 offset:32768\n" \
"ds_read_b64_tr_b16 a[226:227], v243 offset:34816\n" \
"v_mfma_f32_16x16x32_bf16 v[16:19], a[196:199], v[72:75], v[16:19]\n" \
"s_mov_b32 s68, 229632\n" \
"buffer_load_dwordx4 v251, s[60:63], s69 offen offset:3072 lds\n" \
"v_mfma_f32_16x16x32_bf16 v[16:19], a[200:203], v[80:83], v[16:19]\n" \
"s_add_u32 m0, s50, 131072\n" \
"ds_read_b64_tr_b16 a[228:229], v243 offset:36864\n" \
"ds_read_b64_tr_b16 a[230:231], v243 offset:38912\n" \
"v_mfma_f32_16x16x32_bf16 v[16:19], a[204:207], v[88:91], v[16:19]\n" \
"buffer_load_dwordx4 v248, s[60:63], s68 offen offset:0 lds\n" \
"v_mfma_f32_16x16x32_bf16 v[16:19], a[208:211], v[96:99], v[16:19]\n" \
"s_waitcnt lgkmcnt(6)\n" \
"ds_read_b64_tr_b16 a[232:233], v243 offset:40960\n" \
"ds_read_b64_tr_b16 a[234:235], v243 offset:43008\n" \
"v_mfma_f32_16x16x32_bf16 v[16:19], a[212:215], v[104:107], v[16:19]\n" \
"buffer_load_dwordx4 v249, s[60:63], s68 offen offset:1024 lds\n" \
"v_mfma_f32_16x16x32_bf16 v[16:19], a[216:219], v[112:115], v[16:19]\n" \
"s_waitcnt lgkmcnt(6)\n" \
"ds_read_b64_tr_b16 a[236:237], v243 offset:45056\n" \
"ds_read_b64_tr_b16 a[238:239], v243 offset:47104\n" \
"v_mfma_f32_16x16x32_bf16 v[16:19], a[220:223], v[120:123], v[16:19]\n" \
"buffer_load_dwordx4 v250, s[60:63], s68 offen offset:2048 lds\n" \
"v_mfma_f32_16x16x32_bf16 v[20:23], a[192:195], v[68:71], 0\n" \
"ds_read_b64_tr_b16 a[240:241], v243 offset:49152\n" \
"ds_read_b64_tr_b16 a[242:243], v243 offset:51200\n" \
"v_mfma_f32_16x16x32_bf16 v[20:23], a[196:199], v[76:79], v[20:23]\n" \
"s_mov_b32 s69, 344320\n" \
"buffer_load_dwordx4 v251, s[60:63], s68 offen offset:3072 lds\n" \
"v_mfma_f32_16x16x32_bf16 v[20:23], a[200:203], v[84:87], v[20:23]\n" \
"s_add_u32 m0, s50, 147456\n" \
"ds_read_b64_tr_b16 a[244:245], v243 offset:53248\n" \
"ds_read_b64_tr_b16 a[246:247], v243 offset:55296\n" \
"v_mfma_f32_16x16x32_bf16 v[20:23], a[204:207], v[92:95], v[20:23]\n" \
"buffer_load_dwordx4 v248, s[60:63], s69 offen offset:0 lds\n" \
"v_mfma_f32_16x16x32_bf16 v[20:23], a[208:211], v[100:103], v[20:23]\n" \
"ds_read_b64_tr_b16 a[248:249], v243 offset:57344\n" \
"ds_read_b64_tr_b16 a[250:251], v243 offset:59392\n" \
"v_mfma_f32_16x16x32_bf16 v[20:23], a[212:215], v[108:111], v[20:23]\n" \
"s_waitcnt lgkmcnt(6)\n" \
"buffer_load_dwordx4 v249, s[60:63], s69 offen offset:1024 lds\n" \
"v_mfma_f32_16x16x32_bf16 v[20:23], a[216:219], v[116:119], v[20:23]\n" \
"s_waitcnt vmcnt(14)\n" \
"ds_read_b64_tr_b16 a[252:253], v243 offset:61440\n" \
"ds_read_b64_tr_b16 a[254:255], v243 offset:63488\n" \
"v_mfma_f32_16x16x32_bf16 v[20:23], a[220:223], v[124:127], v[20:23]\n" \
"s_barrier\n" \
"buffer_load_dwordx4 v250, s[60:63], s69 offen offset:2048 lds\n" \
"v_mfma_f32_16x16x32_bf16 v[24:27], a[224:227], v[64:67], 0\n" \
"ds_read_b64_tr_b16 a[192:193], v244 offset:0\n" \
"ds_read_b64_tr_b16 a[194:195], v244 offset:2048\n" \
"v_mfma_f32_16x16x32_bf16 v[24:27], a[228:231], v[72:75], v[24:27]\n" \
"s_mov_b32 s68, 384\n" \
"buffer_load_dwordx4 v251, s[60:63], s69 offen offset:3072 lds\n" \
"v_mfma_f32_16x16x32_bf16 v[24:27], a[232:235], v[80:83], v[24:27]\n" \
"s_add_u32 m0, s50, 0\n" \
"ds_read_b64_tr_b16 a[196:197], v244 offset:4096\n" \
"ds_read_b64_tr_b16 a[198:199], v244 offset:6144\n" \
"v_mfma_f32_16x16x32_bf16 v[24:27], a[236:239], v[88:91], v[24:27]\n" \
"s_waitcnt lgkmcnt(6)\n" \
"buffer_load_dwordx4 v248, s[60:63], s68 offen offset:0 lds\n" \
"v_mfma_f32_16x16x32_bf16 v[24:27], a[240:243], v[96:99], v[24:27]\n" \
"ds_read_b64_tr_b16 a[200:201], v244 offset:8192\n" \
"ds_read_b64_tr_b16 a[202:203], v244 offset:10240\n" \
"v_mfma_f32_16x16x32_bf16 v[24:27], a[244:247], v[104:107], v[24:27]\n" \
"v_cvt_pk_bf16_f32 v192, v0, v1\n" \
"v_cvt_pk_bf16_f32 v193, v2, v3\n" \
"v_mfma_f32_16x16x32_bf16 v[24:27], a[248:251], v[112:115], v[24:27]\n" \
"s_waitcnt lgkmcnt(6)\n" \
"ds_read_b64_tr_b16 a[204:205], v244 offset:12288\n" \
"ds_read_b64_tr_b16 a[206:207], v244 offset:14336\n" \
"v_mfma_f32_16x16x32_bf16 v[24:27], a[252:255], v[120:123], v[24:27]\n" \
"buffer_load_dwordx4 v249, s[60:63], s68 offen offset:1024 lds\n" \
"v_mfma_f32_16x16x32_bf16 v[28:31], a[224:227], v[68:71], 0\n" \
"ds_read_b64_tr_b16 a[208:209], v244 offset:16384\n" \
"ds_read_b64_tr_b16 a[210:211], v244 offset:18432\n" \
"v_mfma_f32_16x16x32_bf16 v[28:31], a[228:231], v[76:79], v[28:31]\n" \
"v_cvt_pk_bf16_f32 v194, v8, v9\n" \
"v_cvt_pk_bf16_f32 v195, v10, v11\n" \
"v_mfma_f32_16x16x32_bf16 v[28:31], a[232:235], v[84:87], v[28:31]\n" \
"ds_read_b64_tr_b16 a[212:213], v244 offset:20480\n" \
"ds_read_b64_tr_b16 a[214:215], v244 offset:22528\n" \
"v_mfma_f32_16x16x32_bf16 v[28:31], a[236:239], v[92:95], v[28:31]\n" \
"buffer_load_dwordx4 v250, s[60:63], s68 offen offset:2048 lds\n" \
"v_mfma_f32_16x16x32_bf16 v[28:31], a[240:243], v[100:103], v[28:31]\n" \
"ds_read_b64_tr_b16 a[216:217], v244 offset:24576\n" \
"ds_read_b64_tr_b16 a[218:219], v244 offset:26624\n" \
"v_mfma_f32_16x16x32_bf16 v[28:31], a[244:247], v[108:111], v[28:31]\n" \
"v_cvt_pk_bf16_f32 v196, v4, v5\n" \
"v_cvt_pk_bf16_f32 v197, v6, v7\n" \
"v_mfma_f32_16x16x32_bf16 v[28:31], a[248:251], v[116:119], v[28:31]\n" \
"ds_read_b64_tr_b16 a[220:221], v244 offset:28672\n" \
"ds_read_b64_tr_b16 a[222:223], v244 offset:30720\n" \
"v_mfma_f32_16x16x32_bf16 v[28:31], a[252:255], v[124:127], v[28:31]\n" \
"s_waitcnt lgkmcnt(6)\n" \
"buffer_load_dwordx4 v251, s[60:63], s68 offen offset:3072 lds\n" \
"v_mfma_f32_16x16x32_bf16 v[16:19], a[192:195], v[128:131], v[16:19]\n" \
"ds_read_b64_tr_b16 a[224:225], v245 offset:0\n" \
"ds_read_b64_tr_b16 a[226:227], v245 offset:2048\n" \
"v_mfma_f32_16x16x32_bf16 v[16:19], a[196:199], v[136:139], v[16:19]\n" \
"s_mov_b32 s69, 115072\n" \
"v_cvt_pk_bf16_f32 v198, v12, v13\n" \
"v_cvt_pk_bf16_f32 v199, v14, v15\n" \
"v_mfma_f32_16x16x32_bf16 v[16:19], a[200:203], v[144:147], v[16:19]\n" \
"s_add_u32 m0, s50, 16384\n" \
"ds_read_b64_tr_b16 a[228:229], v245 offset:4096\n" \
"ds_read_b64_tr_b16 a[230:231], v245 offset:6144\n" \
"v_mfma_f32_16x16x32_bf16 v[16:19], a[204:207], v[152:155], v[16:19]\n" \
"buffer_load_dwordx4 v248, s[60:63], s69 offen offset:0 lds\n" \
"v_mfma_f32_16x16x32_bf16 v[16:19], a[208:211], v[160:163], v[16:19]\n" \
"s_waitcnt lgkmcnt(6)\n" \
"ds_read_b64_tr_b16 a[232:233], v245 offset:8192\n" \
"ds_read_b64_tr_b16 a[234:235], v245 offset:10240\n" \
"v_mfma_f32_16x16x32_bf16 v[16:19], a[212:215], v[168:171], v[16:19]\n" \
"buffer_store_dwordx4 v[192:195], v252, s[64:67], 0 offen offset:0 sc1\n" \
"v_mfma_f32_16x16x32_bf16 v[16:19], a[216:219], v[176:179], v[16:19]\n" \
"s_waitcnt lgkmcnt(6)\n" \
"ds_read_b64_tr_b16 a[236:237], v245 offset:12288\n" \
"ds_read_b64_tr_b16 a[238:239], v245 offset:14336\n" \
"v_mfma_f32_16x16x32_bf16 v[16:19], a[220:223], v[184:187], v[16:19]\n" \
"buffer_load_dwordx4 v249, s[60:63], s69 offen offset:1024 lds\n" \
"v_mfma_f32_16x16x32_bf16 v[20:23], a[192:195], v[132:135], v[20:23]\n" \
"ds_read_b64_tr_b16 a[240:241], v245 offset:16384\n" \
"ds_read_b64_tr_b16 a[242:243], v245 offset:18432\n" \
"v_mfma_f32_16x16x32_bf16 v[20:23], a[196:199], v[140:143], v[20:23]\n" \
"buffer_store_dwordx4 v[196:199], v252, s[64:67], 0 offen offset:1024 sc1\n" \
"v_mfma_f32_16x16x32_bf16 v[20:23], a[200:203], v[148:151], v[20:23]\n" \
"ds_read_b64_tr_b16 a[244:245], v245 offset:20480\n" \
"ds_read_b64_tr_b16 a[246:247], v245 offset:22528\n" \
"v_mfma_f32_16x16x32_bf16 v[20:23], a[204:207], v[156:159], v[20:23]\n" \
"buffer_load_dwordx4 v250, s[60:63], s69 offen offset:2048 lds\n" \
"v_mfma_f32_16x16x32_bf16 v[20:23], a[208:211], v[164:167], v[20:23]\n" \
"ds_read_b64_tr_b16 a[248:249], v245 offset:24576\n" \
"ds_read_b64_tr_b16 a[250:251], v245 offset:26624\n" \
"v_mfma_f32_16x16x32_bf16 v[20:23], a[212:215], v[172:175], v[20:23]\n" \
"s_waitcnt lgkmcnt(6)\n" \
"v_add_u32 v252, 0x2000, v252\n" \
"v_mfma_f32_16x16x32_bf16 v[20:23], a[216:219], v[180:183], v[20:23]\n" \
"s_waitcnt vmcnt(17)\n" \
"ds_read_b64_tr_b16 a[252:253], v245 offset:28672\n" \
"ds_read_b64_tr_b16 a[254:255], v245 offset:30720\n" \
"v_mfma_f32_16x16x32_bf16 v[20:23], a[220:223], v[188:191], v[20:23]\n" \
"s_barrier\n" \
"buffer_load_dwordx4 v251, s[60:63], s69 offen offset:3072 lds\n" \
"v_mfma_f32_16x16x32_bf16 v[24:27], a[224:227], v[128:131], v[24:27]\n" \
"ds_read_b64_tr_b16 a[192:193], v244 offset:32768\n" \
"ds_read_b64_tr_b16 a[194:195], v244 offset:34816\n" \
"v_mfma_f32_16x16x32_bf16 v[24:27], a[228:231], v[136:139], v[24:27]\n" \
"s_mov_b32 s68, 229760\n" \
"v_mfma_f32_16x16x32_bf16 v[24:27], a[232:235], v[144:147], v[24:27]\n" \
"s_add_u32 m0, s50, 32768\n" \
"ds_read_b64_tr_b16 a[196:197], v244 offset:36864\n" \
"ds_read_b64_tr_b16 a[198:199], v244 offset:38912\n" \
"v_mfma_f32_16x16x32_bf16 v[24:27], a[236:239], v[152:155], v[24:27]\n" \
"s_waitcnt lgkmcnt(6)\n" \
"buffer_load_dwordx4 v248, s[60:63], s68 offen offset:0 lds\n" \
"v_mfma_f32_16x16x32_bf16 v[24:27], a[240:243], v[160:163], v[24:27]\n" \
"ds_read_b64_tr_b16 a[200:201], v244 offset:40960\n" \
"ds_read_b64_tr_b16 a[202:203], v244 offset:43008\n" \
"v_mfma_f32_16x16x32_bf16 v[24:27], a[244:247], v[168:171], v[24:27]\n" \
"v_mfma_f32_16x16x32_bf16 v[24:27], a[248:251], v[176:179], v[24:27]\n" \
"s_waitcnt lgkmcnt(6)\n" \
"ds_read_b64_tr_b16 a[204:205], v244 offset:45056\n" \
"ds_read_b64_tr_b16 a[206:207], v244 offset:47104\n" \
"v_mfma_f32_16x16x32_bf16 v[24:27], a[252:255], v[184:187], v[24:27]\n" \
"buffer_load_dwordx4 v249, s[60:63], s68 offen offset:1024 lds\n" \
"v_mfma_f32_16x16x32_bf16 v[28:31], a[224:227], v[132:135], v[28:31]\n" \
"ds_read_b64_tr_b16 a[208:209], v244 offset:49152\n" \
"ds_read_b64_tr_b16 a[210:211], v244 offset:51200\n" \
"v_mfma_f32_16x16x32_bf16 v[28:31], a[228:231], v[140:143], v[28:31]\n" \
"v_mfma_f32_16x16x32_bf16 v[28:31], a[232:235], v[148:151], v[28:31]\n" \
"ds_read_b64_tr_b16 a[212:213], v244 offset:53248\n" \
"ds_read_b64_tr_b16 a[214:215], v244 offset:55296\n" \
"v_mfma_f32_16x16x32_bf16 v[28:31], a[236:239], v[156:159], v[28:31]\n" \
"buffer_load_dwordx4 v250, s[60:63], s68 offen offset:2048 lds\n" \
"v_mfma_f32_16x16x32_bf16 v[28:31], a[240:243], v[164:167], v[28:31]\n" \
"ds_read_b64_tr_b16 a[216:217], v244 offset:57344\n" \
"ds_read_b64_tr_b16 a[218:219], v244 offset:59392\n" \
"v_mfma_f32_16x16x32_bf16 v[28:31], a[244:247], v[172:175], v[28:31]\n" \
"v_mfma_f32_16x16x32_bf16 v[28:31], a[248:251], v[180:183], v[28:31]\n" \
"ds_read_b64_tr_b16 a[220:221], v244 offset:61440\n" \
"ds_read_b64_tr_b16 a[222:223], v244 offset:63488\n" \
"v_mfma_f32_16x16x32_bf16 v[28:31], a[252:255], v[188:191], v[28:31]\n" \
"s_waitcnt lgkmcnt(6)\n" \
"buffer_load_dwordx4 v251, s[60:63], s68 offen offset:3072 lds\n" \
"v_mfma_f32_16x16x32_bf16 v[0:3], a[192:195], v[64:67], 0\n" \
"ds_read_b64_tr_b16 a[224:225], v245 offset:32768\n" \
"ds_read_b64_tr_b16 a[226:227], v245 offset:34816\n" \
"v_mfma_f32_16x16x32_bf16 v[0:3], a[196:199], v[72:75], v[0:3]\n" \
"s_mov_b32 s69, 344448\n" \
"v_mfma_f32_16x16x32_bf16 v[0:3], a[200:203], v[80:83], v[0:3]\n" \
"s_add_u32 m0, s50, 49152\n" \
"ds_read_b64_tr_b16 a[228:229], v245 offset:36864\n" \
"ds_read_b64_tr_b16 a[230:231], v245 offset:38912\n" \
"v_mfma_f32_16x16x32_bf16 v[0:3], a[204:207], v[88:91], v[0:3]\n" \
"buffer_load_dwordx4 v248, s[60:63], s69 offen offset:0 lds\n" \
"v_mfma_f32_16x16x32_bf16 v[0:3], a[208:211], v[96:99], v[0:3]\n" \
"s_waitcnt lgkmcnt(6)\n" \
"ds_read_b64_tr_b16 a[232:233], v245 offset:40960\n" \
"ds_read_b64_tr_b16 a[234:235], v245 offset:43008\n" \
"v_mfma_f32_16x16x32_bf16 v[0:3], a[212:215], v[104:107], v[0:3]\n" \
"v_cvt_pk_bf16_f32 v200, v16, v17\n" \
"v_cvt_pk_bf16_f32 v201, v18, v19\n" \
"v_mfma_f32_16x16x32_bf16 v[0:3], a[216:219], v[112:115], v[0:3]\n" \
"s_waitcnt lgkmcnt(6)\n" \
"ds_read_b64_tr_b16 a[236:237], v245 offset:45056\n" \
"ds_read_b64_tr_b16 a[238:239], v245 offset:47104\n" \
"v_mfma_f32_16x16x32_bf16 v[0:3], a[220:223], v[120:123], v[0:3]\n" \
"buffer_load_dwordx4 v249, s[60:63], s69 offen offset:1024 lds\n" \
"v_mfma_f32_16x16x32_bf16 v[4:7], a[192:195], v[68:71], 0\n" \
"ds_read_b64_tr_b16 a[240:241], v245 offset:49152\n" \
"ds_read_b64_tr_b16 a[242:243], v245 offset:51200\n" \
"v_mfma_f32_16x16x32_bf16 v[4:7], a[196:199], v[76:79], v[4:7]\n" \
"v_cvt_pk_bf16_f32 v202, v24, v25\n" \
"v_cvt_pk_bf16_f32 v203, v26, v27\n" \
"v_mfma_f32_16x16x32_bf16 v[4:7], a[200:203], v[84:87], v[4:7]\n" \
"ds_read_b64_tr_b16 a[244:245], v245 offset:53248\n" \
"ds_read_b64_tr_b16 a[246:247], v245 offset:55296\n" \
"v_mfma_f32_16x16x32_bf16 v[4:7], a[204:207], v[92:95], v[4:7]\n" \
"buffer_load_dwordx4 v250, s[60:63], s69 offen offset:2048 lds\n" \
"v_mfma_f32_16x16x32_bf16 v[4:7], a[208:211], v[100:103], v[4:7]\n" \
"ds_read_b64_tr_b16 a[248:249], v245 offset:57344\n" \
"ds_read_b64_tr_b16 a[250:251], v245 offset:59392\n" \
"v_mfma_f32_16x16x32_bf16 v[4:7], a[212:215], v[108:111], v[4:7]\n" \
"s_waitcnt lgkmcnt(6)\n" \
"v_cvt_pk_bf16_f32 v204, v20, v21\n" \
"v_cvt_pk_bf16_f32 v205, v22, v23\n" \
"v_mfma_f32_16x16x32_bf16 v[4:7], a[216:219], v[116:119], v[4:7]\n" \
"s_waitcnt vmcnt(17)\n" \
"ds_read_b64_tr_b16 a[252:253], v245 offset:61440\n" \
"ds_read_b64_tr_b16 a[254:255], v245 offset:63488\n" \
"v_mfma_f32_16x16x32_bf16 v[4:7], a[220:223], v[124:127], v[4:7]\n" \
"s_barrier\n" \
"buffer_load_dwordx4 v251, s[60:63], s69 offen offset:3072 lds\n" \
"v_mfma_f32_16x16x32_bf16 v[8:11], a[224:227], v[64:67], 0\n" \
"ds_read_b64_tr_b16 a[192:193], v246 offset:0\n" \
"ds_read_b64_tr_b16 a[194:195], v246 offset:2048\n" \
"v_mfma_f32_16x16x32_bf16 v[8:11], a[228:231], v[72:75], v[8:11]\n" \
"s_mov_b32 s68, 512\n" \
"v_cvt_pk_bf16_f32 v206, v28, v29\n" \
"v_cvt_pk_bf16_f32 v207, v30, v31\n" \
"v_mfma_f32_16x16x32_bf16 v[8:11], a[232:235], v[80:83], v[8:11]\n" \
"s_add_u32 m0, s50, 65536\n" \
"ds_read_b64_tr_b16 a[196:197], v246 offset:4096\n" \
"ds_read_b64_tr_b16 a[198:199], v246 offset:6144\n" \
"v_mfma_f32_16x16x32_bf16 v[8:11], a[236:239], v[88:91], v[8:11]\n" \
"s_waitcnt lgkmcnt(6)\n" \
"buffer_load_dwordx4 v248, s[60:63], s68 offen offset:0 lds\n" \
"v_mfma_f32_16x16x32_bf16 v[8:11], a[240:243], v[96:99], v[8:11]\n" \
"ds_read_b64_tr_b16 a[200:201], v246 offset:8192\n" \
"ds_read_b64_tr_b16 a[202:203], v246 offset:10240\n" \
"v_mfma_f32_16x16x32_bf16 v[8:11], a[244:247], v[104:107], v[8:11]\n" \
"buffer_store_dwordx4 v[200:203], v252, s[64:67], 0 offen offset:0 sc1\n" \
"v_mfma_f32_16x16x32_bf16 v[8:11], a[248:251], v[112:115], v[8:11]\n" \
"s_waitcnt lgkmcnt(6)\n" \
"ds_read_b64_tr_b16 a[204:205], v246 offset:12288\n" \
"ds_read_b64_tr_b16 a[206:207], v246 offset:14336\n" \
"v_mfma_f32_16x16x32_bf16 v[8:11], a[252:255], v[120:123], v[8:11]\n" \
"buffer_load_dwordx4 v249, s[60:63], s68 offen offset:1024 lds\n" \
"v_mfma_f32_16x16x32_bf16 v[12:15], a[224:227], v[68:71], 0\n" \
"ds_read_b64_tr_b16 a[208:209], v246 offset:16384\n" \
"ds_read_b64_tr_b16 a[210:211], v246 offset:18432\n" \
"v_mfma_f32_16x16x32_bf16 v[12:15], a[228:231], v[76:79], v[12:15]\n" \
"buffer_store_dwordx4 v[204:207], v252, s[64:67], 0 offen offset:1024 sc1\n" \
"v_mfma_f32_16x16x32_bf16 v[12:15], a[232:235], v[84:87], v[12:15]\n" \
"ds_read_b64_tr_b16 a[212:213], v246 offset:20480\n" \
"ds_read_b64_tr_b16 a[214:215], v246 offset:22528\n" \
"v_mfma_f32_16x16x32_bf16 v[12:15], a[236:239], v[92:95], v[12:15]\n" \
"buffer_load_dwordx4 v250, s[60:63], s68 offen offset:2048 lds\n" \
"v_mfma_f32_16x16x32_bf16 v[12:15], a[240:243], v[100:103], v[12:15]\n" \
"ds_read_b64_tr_b16 a[216:217], v246 offset:24576\n" \
"ds_read_b64_tr_b16 a[218:219], v246 offset:26624\n" \
"v_mfma_f32_16x16x32_bf16 v[12:15], a[244:247], v[108:111], v[12:15]\n" \
"v_add_u32 v252, 0x2000, v252\n" \
"v_mfma_f32_16x16x32_bf16 v[12:15], a[248:251], v[116:119], v[12:15]\n" \
"ds_read_b64_tr_b16 a[220:221], v246 offset:28672\n" \
"ds_read_b64_tr_b16 a[222:223], v246 offset:30720\n" \
"v_mfma_f32_16x16x32_bf16 v[12:15], a[252:255], v[124:127], v[12:15]\n" \
"s_waitcnt lgkmcnt(6)\n" \
"buffer_load_dwordx4 v251, s[60:63], s68 offen offset:3072 lds\n" \
"v_mfma_f32_16x16x32_bf16 v[0:3], a[192:195], v[128:131], v[0:3]\n" \
"ds_read_b64_tr_b16 a[224:225], v247 offset:0\n" \
"ds_read_b64_tr_b16 a[226:227], v247 offset:2048\n" \
"v_mfma_f32_16x16x32_bf16 v[0:3], a[196:199], v[136:139], v[0:3]\n" \
"s_mov_b32 s69, 115200\n" \
"v_mfma_f32_16x16x32_bf16 v[0:3], a[200:203], v[144:147], v[0:3]\n" \
"s_add_u32 m0, s50, 81920\n" \
"ds_read_b64_tr_b16 a[228:229], v247 offset:4096\n" \
"ds_read_b64_tr_b16 a[230:231], v247 offset:6144\n" \
"v_mfma_f32_16x16x32_bf16 v[0:3], a[204:207], v[152:155], v[0:3]\n" \
"buffer_load_dwordx4 v248, s[60:63], s69 offen offset:0 lds\n" \
"v_mfma_f32_16x16x32_bf16 v[0:3], a[208:211], v[160:163], v[0:3]\n" \
"s_waitcnt lgkmcnt(6)\n" \
"ds_read_b64_tr_b16 a[232:233], v247 offset:8192\n" \
"ds_read_b64_tr_b16 a[234:235], v247 offset:10240\n" \
"v_mfma_f32_16x16x32_bf16 v[0:3], a[212:215], v[168:171], v[0:3]\n" \
"v_mfma_f32_16x16x32_bf16 v[0:3], a[216:219], v[176:179], v[0:3]\n" \
"s_waitcnt lgkmcnt(6)\n" \
"ds_read_b64_tr_b16 a[236:237], v247 offset:12288\n" \
"ds_read_b64_tr_b16 a[238:239], v247 offset:14336\n" \
"v_mfma_f32_16x16x32_bf16 v[0:3], a[220:223], v[184:187], v[0:3]\n" \
"buffer_load_dwordx4 v249, s[60:63], s69 offen offset:1024 lds\n" \
"v_mfma_f32_16x16x32_bf16 v[4:7], a[192:195], v[132:135], v[4:7]\n" \
"ds_read_b64_tr_b16 a[240:241], v247 offset:16384\n" \
"ds_read_b64_tr_b16 a[242:243], v247 offset:18432\n" \
"v_mfma_f32_16x16x32_bf16 v[4:7], a[196:199], v[140:143], v[4:7]\n" \
"v_mfma_f32_16x16x32_bf16 v[4:7], a[200:203], v[148:151], v[4:7]\n" \
"ds_read_b64_tr_b16 a[244:245], v247 offset:20480\n" \
"ds_read_b64_tr_b16 a[246:247], v247 offset:22528\n" \
"v_mfma_f32_16x16x32_bf16 v[4:7], a[204:207], v[156:159], v[4:7]\n" \
"buffer_load_dwordx4 v250, s[60:63], s69 offen offset:2048 lds\n" \
"v_mfma_f32_16x16x32_bf16 v[4:7], a[208:211], v[164:167], v[4:7]\n" \
"ds_read_b64_tr_b16 a[248:249], v247 offset:24576\n" \
"ds_read_b64_tr_b16 a[250:251], v247 offset:26624\n" \
"v_mfma_f32_16x16x32_bf16 v[4:7], a[212:215], v[172:175], v[4:7]\n" \
"s_waitcnt lgkmcnt(6)\n" \
"v_mfma_f32_16x16x32_bf16 v[4:7], a[216:219], v[180:183], v[4:7]\n" \
"s_waitcnt vmcnt(17)\n" \
"ds_read_b64_tr_b16 a[252:253], v247 offset:28672\n" \
"ds_read_b64_tr_b16 a[254:255], v247 offset:30720\n" \
"v_mfma_f32_16x16x32_bf16 v[4:7], a[220:223], v[188:191], v[4:7]\n" \
"s_barrier\n" \
"buffer_load_dwordx4 v251, s[60:63], s69 offen offset:3072 lds\n" \
"v_mfma_f32_16x16x32_bf16 v[8:11], a[224:227], v[128:131], v[8:11]\n" \
"ds_read_b64_tr_b16 a[192:193], v242 offset:0\n" \
"ds_read_b64_tr_b16 a[194:195], v242 offset:2048\n" \
"v_mfma_f32_16x16x32_bf16 v[8:11], a[228:231], v[136:139], v[8:11]\n" \
"s_mov_b32 s68, 229888\n" \
"v_mfma_f32_16x16x32_bf16 v[8:11], a[232:235], v[144:147], v[8:11]\n" \
"s_add_u32 m0, s50, 98304\n" \
"ds_read_b64_tr_b16 a[196:197], v242 offset:4096\n" \
"ds_read_b64_tr_b16 a[198:199], v242 offset:6144\n" \
"v_mfma_f32_16x16x32_bf16 v[8:11], a[236:239], v[152:155], v[8:11]\n" \
"s_waitcnt lgkmcnt(6)\n" \
"buffer_load_dwordx4 v248, s[60:63], s68 offen offset:0 lds\n" \
"v_mfma_f32_16x16x32_bf16 v[8:11], a[240:243], v[160:163], v[8:11]\n" \
"ds_read_b64_tr_b16 a[200:201], v242 offset:8192\n" \
"ds_read_b64_tr_b16 a[202:203], v242 offset:10240\n" \
"v_mfma_f32_16x16x32_bf16 v[8:11], a[244:247], v[168:171], v[8:11]\n" \
"v_mfma_f32_16x16x32_bf16 v[8:11], a[248:251], v[176:179], v[8:11]\n" \
"s_waitcnt lgkmcnt(6)\n" \
"ds_read_b64_tr_b16 a[204:205], v242 offset:12288\n" \
"ds_read_b64_tr_b16 a[206:207], v242 offset:14336\n" \
"v_mfma_f32_16x16x32_bf16 v[8:11], a[252:255], v[184:187], v[8:11]\n" \
"buffer_load_dwordx4 v249, s[60:63], s68 offen offset:1024 lds\n" \
"v_mfma_f32_16x16x32_bf16 v[12:15], a[224:227], v[132:135], v[12:15]\n" \
"ds_read_b64_tr_b16 a[208:209], v242 offset:16384\n" \
"ds_read_b64_tr_b16 a[210:211], v242 offset:18432\n" \
"v_mfma_f32_16x16x32_bf16 v[12:15], a[228:231], v[140:143], v[12:15]\n" \
"v_mfma_f32_16x16x32_bf16 v[12:15], a[232:235], v[148:151], v[12:15]\n" \
"ds_read_b64_tr_b16 a[212:213], v242 offset:20480\n" \
"ds_read_b64_tr_b16 a[214:215], v242 offset:22528\n" \
"v_mfma_f32_16x16x32_bf16 v[12:15], a[236:239], v[156:159], v[12:15]\n" \
"buffer_load_dwordx4 v250, s[60:63], s68 offen offset:2048 lds\n" \
"v_mfma_f32_16x16x32_bf16 v[12:15], a[240:243], v[164:167], v[12:15]\n" \
"ds_read_b64_tr_b16 a[216:217], v242 offset:24576\n" \
"ds_read_b64_tr_b16 a[218:219], v242 offset:26624\n" \
"v_mfma_f32_16x16x32_bf16 v[12:15], a[244:247], v[172:175], v[12:15]\n" \
"v_mfma_f32_16x16x32_bf16 v[12:15], a[248:251], v[180:183], v[12:15]\n" \
"ds_read_b64_tr_b16 a[220:221], v242 offset:28672\n" \
"ds_read_b64_tr_b16 a[222:223], v242 offset:30720\n" \
"v_mfma_f32_16x16x32_bf16 v[12:15], a[252:255], v[188:191], v[12:15]\n" \
"s_waitcnt lgkmcnt(6)\n" \
"buffer_load_dwordx4 v251, s[60:63], s68 offen offset:3072 lds\n" \
"v_mfma_f32_16x16x32_bf16 v[16:19], a[192:195], v[64:67], 0\n" \
"ds_read_b64_tr_b16 a[224:225], v243 offset:0\n" \
"ds_read_b64_tr_b16 a[226:227], v243 offset:2048\n" \
"v_mfma_f32_16x16x32_bf16 v[16:19], a[196:199], v[72:75], v[16:19]\n" \
"s_mov_b32 s69, 344576\n" \
"v_mfma_f32_16x16x32_bf16 v[16:19], a[200:203], v[80:83], v[16:19]\n" \
"s_add_u32 m0, s50, 114688\n" \
"ds_read_b64_tr_b16 a[228:229], v243 offset:4096\n" \
"ds_read_b64_tr_b16 a[230:231], v243 offset:6144\n" \
"v_mfma_f32_16x16x32_bf16 v[16:19], a[204:207], v[88:91], v[16:19]\n" \
"buffer_load_dwordx4 v248, s[60:63], s69 offen offset:0 lds\n" \
"v_mfma_f32_16x16x32_bf16 v[16:19], a[208:211], v[96:99], v[16:19]\n" \
"s_waitcnt lgkmcnt(6)\n" \
"ds_read_b64_tr_b16 a[232:233], v243 offset:8192\n" \
"ds_read_b64_tr_b16 a[234:235], v243 offset:10240\n" \
"v_mfma_f32_16x16x32_bf16 v[16:19], a[212:215], v[104:107], v[16:19]\n" \
"v_cvt_pk_bf16_f32 v208, v0, v1\n" \
"v_cvt_pk_bf16_f32 v209, v2, v3\n" \
"v_mfma_f32_16x16x32_bf16 v[16:19], a[216:219], v[112:115], v[16:19]\n" \
"s_waitcnt lgkmcnt(6)\n" \
"ds_read_b64_tr_b16 a[236:237], v243 offset:12288\n" \
"ds_read_b64_tr_b16 a[238:239], v243 offset:14336\n" \
"v_mfma_f32_16x16x32_bf16 v[16:19], a[220:223], v[120:123], v[16:19]\n" \
"buffer_load_dwordx4 v249, s[60:63], s69 offen offset:1024 lds\n" \
"v_mfma_f32_16x16x32_bf16 v[20:23], a[192:195], v[68:71], 0\n" \
"ds_read_b64_tr_b16 a[240:241], v243 offset:16384\n" \
"ds_read_b64_tr_b16 a[242:243], v243 offset:18432\n" \
"v_mfma_f32_16x16x32_bf16 v[20:23], a[196:199], v[76:79], v[20:23]\n" \
"v_cvt_pk_bf16_f32 v210, v8, v9\n" \
"v_cvt_pk_bf16_f32 v211, v10, v11\n" \
"v_mfma_f32_16x16x32_bf16 v[20:23], a[200:203], v[84:87], v[20:23]\n" \
"ds_read_b64_tr_b16 a[244:245], v243 offset:20480\n" \
"ds_read_b64_tr_b16 a[246:247], v243 offset:22528\n" \
"v_mfma_f32_16x16x32_bf16 v[20:23], a[204:207], v[92:95], v[20:23]\n" \
"buffer_load_dwordx4 v250, s[60:63], s69 offen offset:2048 lds\n" \
"v_mfma_f32_16x16x32_bf16 v[20:23], a[208:211], v[100:103], v[20:23]\n" \
"ds_read_b64_tr_b16 a[248:249], v243 offset:24576\n" \
"ds_read_b64_tr_b16 a[250:251], v243 offset:26624\n" \
"v_mfma_f32_16x16x32_bf16 v[20:23], a[212:215], v[108:111], v[20:23]\n" \
"s_waitcnt lgkmcnt(6)\n" \
"v_cvt_pk_bf16_f32 v212, v4, v5\n" \
"v_cvt_pk_bf16_f32 v213, v6, v7\n" \
"v_mfma_f32_16x16x32_bf16 v[20:23], a[216:219], v[116:119], v[20:23]\n" \
"s_waitcnt vmcnt(17)\n" \
"ds_read_b64_tr_b16 a[252:253], v243 offset:28672\n" \
"ds_read_b64_tr_b16 a[254:255], v243 offset:30720\n" \
"v_mfma_f32_16x16x32_bf16 v[20:23], a[220:223], v[124:127], v[20:23]\n" \
"s_barrier\n" \
"buffer_load_dwordx4 v251, s[60:63], s69 offen offset:3072 lds\n" \
"v_mfma_f32_16x16x32_bf16 v[24:27], a[224:227], v[64:67], 0\n" \
"ds_read_b64_tr_b16 a[192:193], v242 offset:32768\n" \
"ds_read_b64_tr_b16 a[194:195], v242 offset:34816\n" \
"v_mfma_f32_16x16x32_bf16 v[24:27], a[228:231], v[72:75], v[24:27]\n" \
"s_mov_b32 s68, 640\n" \
"v_cvt_pk_bf16_f32 v214, v12, v13\n" \
"v_cvt_pk_bf16_f32 v215, v14, v15\n" \
"v_mfma_f32_16x16x32_bf16 v[24:27], a[232:235], v[80:83], v[24:27]\n" \
"s_add_u32 m0, s50, 131072\n" \
"ds_read_b64_tr_b16 a[196:197], v242 offset:36864\n" \
"ds_read_b64_tr_b16 a[198:199], v242 offset:38912\n" \
"v_mfma_f32_16x16x32_bf16 v[24:27], a[236:239], v[88:91], v[24:27]\n" \
"s_waitcnt lgkmcnt(6)\n" \
"buffer_load_dwordx4 v248, s[60:63], s68 offen offset:0 lds\n" \
"v_mfma_f32_16x16x32_bf16 v[24:27], a[240:243], v[96:99], v[24:27]\n" \
"ds_read_b64_tr_b16 a[200:201], v242 offset:40960\n" \
"ds_read_b64_tr_b16 a[202:203], v242 offset:43008\n" \
"v_mfma_f32_16x16x32_bf16 v[24:27], a[244:247], v[104:107], v[24:27]\n" \
"buffer_store_dwordx4 v[208:211], v252, s[64:67], 0 offen offset:0 sc1\n" \
"v_mfma_f32_16x16x32_bf16 v[24:27], a[248:251], v[112:115], v[24:27]\n" \
"s_waitcnt lgkmcnt(6)\n" \
"ds_read_b64_tr_b16 a[204:205], v242 offset:45056\n" \
"ds_read_b64_tr_b16 a[206:207], v242 offset:47104\n" \
"v_mfma_f32_16x16x32_bf16 v[24:27], a[252:255], v[120:123], v[24:27]\n" \
"buffer_load_dwordx4 v249, s[60:63], s68 offen offset:1024 lds\n" \
"v_mfma_f32_16x16x32_bf16 v[28:31], a[224:227], v[68:71], 0\n" \
"ds_read_b64_tr_b16 a[208:209], v242 offset:49152\n" \
"ds_read_b64_tr_b16 a[210:211], v242 offset:51200\n" \
"v_mfma_f32_16x16x32_bf16 v[28:31], a[228:231], v[76:79], v[28:31]\n" \
"buffer_store_dwordx4 v[212:215], v252, s[64:67], 0 offen offset:1024 sc1\n" \
"v_mfma_f32_16x16x32_bf16 v[28:31], a[232:235], v[84:87], v[28:31]\n" \
"ds_read_b64_tr_b16 a[212:213], v242 offset:53248\n" \
"ds_read_b64_tr_b16 a[214:215], v242 offset:55296\n" \
"v_mfma_f32_16x16x32_bf16 v[28:31], a[236:239], v[92:95], v[28:31]\n" \
"buffer_load_dwordx4 v250, s[60:63], s68 offen offset:2048 lds\n" \
"v_mfma_f32_16x16x32_bf16 v[28:31], a[240:243], v[100:103], v[28:31]\n" \
"ds_read_b64_tr_b16 a[216:217], v242 offset:57344\n" \
"ds_read_b64_tr_b16 a[218:219], v242 offset:59392\n" \
"v_mfma_f32_16x16x32_bf16 v[28:31], a[244:247], v[108:111], v[28:31]\n" \
"v_add_u32 v252, 0x2000, v252\n" \
"v_mfma_f32_16x16x32_bf16 v[28:31], a[248:251], v[116:119], v[28:31]\n" \
"ds_read_b64_tr_b16 a[220:221], v242 offset:61440\n" \
"ds_read_b64_tr_b16 a[222:223], v242 offset:63488\n" \
"v_mfma_f32_16x16x32_bf16 v[28:31], a[252:255], v[124:127], v[28:31]\n" \
"s_waitcnt lgkmcnt(6)\n" \
"buffer_load_dwordx4 v251, s[60:63], s68 offen offset:3072 lds\n" \
"v_mfma_f32_16x16x32_bf16 v[16:19], a[192:195], v[128:131], v[16:19]\n" \
"ds_read_b64_tr_b16 a[224:225], v243 offset:32768\n" \
"ds_read_b64_tr_b16 a[226:227], v243 offset:34816\n" \
"v_mfma_f32_16x16x32_bf16 v[16:19], a[196:199], v[136:139], v[16:19]\n" \
"s_mov_b32 s69, 115328\n" \
"v_mfma_f32_16x16x32_bf16 v[16:19], a[200:203], v[144:147], v[16:19]\n" \
"s_add_u32 m0, s50, 147456\n" \
"ds_read_b64_tr_b16 a[228:229], v243 offset:36864\n" \
"ds_read_b64_tr_b16 a[230:231], v243 offset:38912\n" \
"v_mfma_f32_16x16x32_bf16 v[16:19], a[204:207], v[152:155], v[16:19]\n" \
"buffer_load_dwordx4 v248, s[60:63], s69 offen offset:0 lds\n" \
"v_mfma_f32_16x16x32_bf16 v[16:19], a[208:211], v[160:163], v[16:19]\n" \
"s_waitcnt lgkmcnt(6)\n" \
"ds_read_b64_tr_b16 a[232:233], v243 offset:40960\n" \
"ds_read_b64_tr_b16 a[234:235], v243 offset:43008\n" \
"v_mfma_f32_16x16x32_bf16 v[16:19], a[212:215], v[168:171], v[16:19]\n" \
"v_mfma_f32_16x16x32_bf16 v[16:19], a[216:219], v[176:179], v[16:19]\n" \
"s_waitcnt lgkmcnt(6)\n" \
"ds_read_b64_tr_b16 a[236:237], v243 offset:45056\n" \
"ds_read_b64_tr_b16 a[238:239], v243 offset:47104\n" \
"v_mfma_f32_16x16x32_bf16 v[16:19], a[220:223], v[184:187], v[16:19]\n" \
"buffer_load_dwordx4 v249, s[60:63], s69 offen offset:1024 lds\n" \
"v_mfma_f32_16x16x32_bf16 v[20:23], a[192:195], v[132:135], v[20:23]\n" \
"ds_read_b64_tr_b16 a[240:241], v243 offset:49152\n" \
"ds_read_b64_tr_b16 a[242:243], v243 offset:51200\n" \
"v_mfma_f32_16x16x32_bf16 v[20:23], a[196:199], v[140:143], v[20:23]\n" \
"v_mfma_f32_16x16x32_bf16 v[20:23], a[200:203], v[148:151], v[20:23]\n" \
"ds_read_b64_tr_b16 a[244:245], v243 offset:53248\n" \
"ds_read_b64_tr_b16 a[246:247], v243 offset:55296\n" \
"v_mfma_f32_16x16x32_bf16 v[20:23], a[204:207], v[156:159], v[20:23]\n" \
"buffer_load_dwordx4 v250, s[60:63], s69 offen offset:2048 lds\n" \
"v_mfma_f32_16x16x32_bf16 v[20:23], a[208:211], v[164:167], v[20:23]\n" \
"ds_read_b64_tr_b16 a[248:249], v243 offset:57344\n" \
"ds_read_b64_tr_b16 a[250:251], v243 offset:59392\n" \
"v_mfma_f32_16x16x32_bf16 v[20:23], a[212:215], v[172:175], v[20:23]\n" \
"s_waitcnt lgkmcnt(6)\n" \
"v_mfma_f32_16x16x32_bf16 v[20:23], a[216:219], v[180:183], v[20:23]\n" \
"s_waitcnt vmcnt(17)\n" \
"ds_read_b64_tr_b16 a[252:253], v243 offset:61440\n" \
"ds_read_b64_tr_b16 a[254:255], v243 offset:63488\n" \
"v_mfma_f32_16x16x32_bf16 v[20:23], a[220:223], v[188:191], v[20:23]\n" \
"s_barrier\n" \
"buffer_load_dwordx4 v251, s[60:63], s69 offen offset:3072 lds\n" \
"v_mfma_f32_16x16x32_bf16 v[24:27], a[224:227], v[128:131], v[24:27]\n" \
"ds_read_b64_tr_b16 a[192:193], v244 offset:0\n" \
"ds_read_b64_tr_b16 a[194:195], v244 offset:2048\n" \
"v_mfma_f32_16x16x32_bf16 v[24:27], a[228:231], v[136:139], v[24:27]\n" \
"s_mov_b32 s68, 230016\n" \
"v_mfma_f32_16x16x32_bf16 v[24:27], a[232:235], v[144:147], v[24:27]\n" \
"s_add_u32 m0, s50, 0\n" \
"ds_read_b64_tr_b16 a[196:197], v244 offset:4096\n" \
"ds_read_b64_tr_b16 a[198:199], v244 offset:6144\n" \
"v_mfma_f32_16x16x32_bf16 v[24:27], a[236:239], v[152:155], v[24:27]\n" \
"s_waitcnt lgkmcnt(6)\n" \
"buffer_load_dwordx4 v248, s[60:63], s68 offen offset:0 lds\n" \
"v_mfma_f32_16x16x32_bf16 v[24:27], a[240:243], v[160:163], v[24:27]\n" \
"ds_read_b64_tr_b16 a[200:201], v244 offset:8192\n" \
"ds_read_b64_tr_b16 a[202:203], v244 offset:10240\n" \
"v_mfma_f32_16x16x32_bf16 v[24:27], a[244:247], v[168:171], v[24:27]\n" \
"v_mfma_f32_16x16x32_bf16 v[24:27], a[248:251], v[176:179], v[24:27]\n" \
"s_waitcnt lgkmcnt(6)\n" \
"ds_read_b64_tr_b16 a[204:205], v244 offset:12288\n" \
"ds_read_b64_tr_b16 a[206:207], v244 offset:14336\n" \
"v_mfma_f32_16x16x32_bf16 v[24:27], a[252:255], v[184:187], v[24:27]\n" \
"buffer_load_dwordx4 v249, s[60:63], s68 offen offset:1024 lds\n" \
"v_mfma_f32_16x16x32_bf16 v[28:31], a[224:227], v[132:135], v[28:31]\n" \
"ds_read_b64_tr_b16 a[208:209], v244 offset:16384\n" \
"ds_read_b64_tr_b16 a[210:211], v244 offset:18432\n" \
"v_mfma_f32_16x16x32_bf16 v[28:31], a[228:231], v[140:143], v[28:31]\n" \
"v_mfma_f32_16x16x32_bf16 v[28:31], a[232:235], v[148:151], v[28:31]\n" \
"ds_read_b64_tr_b16 a[212:213], v244 offset:20480\n" \
"ds_read_b64_tr_b16 a[214:215], v244 offset:22528\n" \
"v_mfma_f32_16x16x32_bf16 v[28:31], a[236:239], v[156:159], v[28:31]\n" \
"buffer_load_dwordx4 v250, s[60:63], s68 offen offset:2048 lds\n" \
"v_mfma_f32_16x16x32_bf16 v[28:31], a[240:243], v[164:167], v[28:31]\n" \
"ds_read_b64_tr_b16 a[216:217], v244 offset:24576\n" \
"ds_read_b64_tr_b16 a[218:219], v244 offset:26624\n" \
"v_mfma_f32_16x16x32_bf16 v[28:31], a[244:247], v[172:175], v[28:31]\n" \
"v_mfma_f32_16x16x32_bf16 v[28:31], a[248:251], v[180:183], v[28:31]\n" \
"ds_read_b64_tr_b16 a[220:221], v244 offset:28672\n" \
"ds_read_b64_tr_b16 a[222:223], v244 offset:30720\n" \
"v_mfma_f32_16x16x32_bf16 v[28:31], a[252:255], v[188:191], v[28:31]\n" \
"s_waitcnt lgkmcnt(6)\n" \
"buffer_load_dwordx4 v251, s[60:63], s68 offen offset:3072 lds\n" \
"v_mfma_f32_16x16x32_bf16 v[0:3], a[192:195], v[64:67], 0\n" \
"ds_read_b64_tr_b16 a[224:225], v245 offset:0\n" \
"ds_read_b64_tr_b16 a[226:227], v245 offset:2048\n" \
"v_mfma_f32_16x16x32_bf16 v[0:3], a[196:199], v[72:75], v[0:3]\n" \
"s_mov_b32 s69, 344704\n" \
"v_mfma_f32_16x16x32_bf16 v[0:3], a[200:203], v[80:83], v[0:3]\n" \
"s_add_u32 m0, s50, 16384\n" \
"ds_read_b64_tr_b16 a[228:229], v245 offset:4096\n" \
"ds_read_b64_tr_b16 a[230:231], v245 offset:6144\n" \
"v_mfma_f32_16x16x32_bf16 v[0:3], a[204:207], v[88:91], v[0:3]\n" \
"buffer_load_dwordx4 v248, s[60:63], s69 offen offset:0 lds\n" \
"v_mfma_f32_16x16x32_bf16 v[0:3], a[208:211], v[96:99], v[0:3]\n" \
"s_waitcnt lgkmcnt(6)\n" \
"ds_read_b64_tr_b16 a[232:233], v245 offset:8192\n" \
"ds_read_b64_tr_b16 a[234:235], v245 offset:10240\n" \
"v_mfma_f32_16x16x32_bf16 v[0:3], a[212:215], v[104:107], v[0:3]\n" \
"v_cvt_pk_bf16_f32 v216, v16, v17\n" \
"v_cvt_pk_bf16_f32 v217, v18, v19\n" \
"v_mfma_f32_16x16x32_bf16 v[0:3], a[216:219], v[112:115], v[0:3]\n" \
"s_waitcnt lgkmcnt(6)\n" \
"ds_read_b64_tr_b16 a[236:237], v245 offset:12288\n" \
"ds_read_b64_tr_b16 a[238:239], v245 offset:14336\n" \
"v_mfma_f32_16x16x32_bf16 v[0:3], a[220:223], v[120:123], v[0:3]\n" \
"buffer_load_dwordx4 v249, s[60:63], s69 offen offset:1024 lds\n" \
"v_mfma_f32_16x16x32_bf16 v[4:7], a[192:195], v[68:71], 0\n" \
"ds_read_b64_tr_b16 a[240:241], v245 offset:16384\n" \
"ds_read_b64_tr_b16 a[242:243], v245 offset:18432\n" \
"v_mfma_f32_16x16x32_bf16 v[4:7], a[196:199], v[76:79], v[4:7]\n" \
"v_cvt_pk_bf16_f32 v218, v24, v25\n" \
"v_cvt_pk_bf16_f32 v219, v26, v27\n" \
"v_mfma_f32_16x16x32_bf16 v[4:7], a[200:203], v[84:87], v[4:7]\n" \
"ds_read_b64_tr_b16 a[244:245], v245 offset:20480\n" \
"ds_read_b64_tr_b16 a[246:247], v245 offset:22528\n" \
"v_mfma_f32_16x16x32_bf16 v[4:7], a[204:207], v[92:95], v[4:7]\n" \
"buffer_load_dwordx4 v250, s[60:63], s69 offen offset:2048 lds\n" \
"v_mfma_f32_16x16x32_bf16 v[4:7], a[208:211], v[100:103], v[4:7]\n" \
"ds_read_b64_tr_b16 a[248:249], v245 offset:24576\n" \
"ds_read_b64_tr_b16 a[250:251], v245 offset:26624\n" \
"v_mfma_f32_16x16x32_bf16 v[4:7], a[212:215], v[108:111], v[4:7]\n" \
"s_waitcnt lgkmcnt(6)\n" \
"v_cvt_pk_bf16_f32 v220, v20, v21\n" \
"v_cvt_pk_bf16_f32 v221, v22, v23\n" \
"v_mfma_f32_16x16x32_bf16 v[4:7], a[216:219], v[116:119], v[4:7]\n" \
"s_waitcnt vmcnt(17)\n" \
"ds_read_b64_tr_b16 a[252:253], v245 offset:28672\n" \
"ds_read_b64_tr_b16 a[254:255], v245 offset:30720\n" \
"v_mfma_f32_16x16x32_bf16 v[4:7], a[220:223], v[124:127], v[4:7]\n" \
"s_barrier\n" \
"buffer_load_dwordx4 v251, s[60:63], s69 offen offset:3072 lds\n" \
"v_mfma_f32_16x16x32_bf16 v[8:11], a[224:227], v[64:67], 0\n" \
"ds_read_b64_tr_b16 a[192:193], v244 offset:32768\n" \
"ds_read_b64_tr_b16 a[194:195], v244 offset:34816\n" \
"v_mfma_f32_16x16x32_bf16 v[8:11], a[228:231], v[72:75], v[8:11]\n" \
"s_mov_b32 s68, 768\n" \
"v_cvt_pk_bf16_f32 v222, v28, v29\n" \
"v_cvt_pk_bf16_f32 v223, v30, v31\n" \
"v_mfma_f32_16x16x32_bf16 v[8:11], a[232:235], v[80:83], v[8:11]\n" \
"s_add_u32 m0, s50, 32768\n" \
"ds_read_b64_tr_b16 a[196:197], v244 offset:36864\n" \
"ds_read_b64_tr_b16 a[198:199], v244 offset:38912\n" \
"v_mfma_f32_16x16x32_bf16 v[8:11], a[236:239], v[88:91], v[8:11]\n" \
"s_waitcnt lgkmcnt(6)\n" \
"buffer_load_dwordx4 v248, s[60:63], s68 offen offset:0 lds\n" \
"v_mfma_f32_16x16x32_bf16 v[8:11], a[240:243], v[96:99], v[8:11]\n" \
"ds_read_b64_tr_b16 a[200:201], v244 offset:40960\n" \
"ds_read_b64_tr_b16 a[202:203], v244 offset:43008\n" \
"v_mfma_f32_16x16x32_bf16 v[8:11], a[244:247], v[104:107], v[8:11]\n" \
"v_mfma_f32_16x16x32_bf16 v[8:11], a[248:251], v[112:115], v[8:11]\n" \
"s_waitcnt lgkmcnt(6)\n" \
"ds_read_b64_tr_b16 a[204:205], v244 offset:45056\n" \
"ds_read_b64_tr_b16 a[206:207], v244 offset:47104\n" \
"v_mfma_f32_16x16x32_bf16 v[8:11], a[252:255], v[120:123], v[8:11]\n" \
"buffer_load_dwordx4 v249, s[60:63], s68 offen offset:1024 lds\n" \
"v_mfma_f32_16x16x32_bf16 v[12:15], a[224:227], v[68:71], 0\n" \
"ds_read_b64_tr_b16 a[208:209], v244 offset:49152\n" \
"ds_read_b64_tr_b16 a[210:211], v244 offset:51200\n" \
"v_mfma_f32_16x16x32_bf16 v[12:15], a[228:231], v[76:79], v[12:15]\n" \
"v_mfma_f32_16x16x32_bf16 v[12:15], a[232:235], v[84:87], v[12:15]\n" \
"ds_read_b64_tr_b16 a[212:213], v244 offset:53248\n" \
"ds_read_b64_tr_b16 a[214:215], v244 offset:55296\n" \
"v_mfma_f32_16x16x32_bf16 v[12:15], a[236:239], v[92:95], v[12:15]\n" \
"buffer_load_dwordx4 v250, s[60:63], s68 offen offset:2048 lds\n" \
"v_mfma_f32_16x16x32_bf16 v[12:15], a[240:243], v[100:103], v[12:15]\n" \
"ds_read_b64_tr_b16 a[216:217], v244 offset:57344\n" \
"ds_read_b64_tr_b16 a[218:219], v244 offset:59392\n" \
"v_mfma_f32_16x16x32_bf16 v[12:15], a[244:247], v[108:111], v[12:15]\n" \
"v_mfma_f32_16x16x32_bf16 v[12:15], a[248:251], v[116:119], v[12:15]\n" \
"ds_read_b64_tr_b16 a[220:221], v244 offset:61440\n" \
"ds_read_b64_tr_b16 a[222:223], v244 offset:63488\n" \
"v_mfma_f32_16x16x32_bf16 v[12:15], a[252:255], v[124:127], v[12:15]\n" \
"s_waitcnt lgkmcnt(6)\n" \
"buffer_load_dwordx4 v251, s[60:63], s68 offen offset:3072 lds\n" \
"v_mfma_f32_16x16x32_bf16 v[0:3], a[192:195], v[128:131], v[0:3]\n" \
"ds_read_b64_tr_b16 a[224:225], v245 offset:32768\n" \
"ds_read_b64_tr_b16 a[226:227], v245 offset:34816\n" \
"v_mfma_f32_16x16x32_bf16 v[0:3], a[196:199], v[136:139], v[0:3]\n" \
"s_mov_b32 s69, 115456\n" \
"v_mfma_f32_16x16x32_bf16 v[0:3], a[200:203], v[144:147], v[0:3]\n" \
"s_add_u32 m0, s50, 49152\n" \
"ds_read_b64_tr_b16 a[228:229], v245 offset:36864\n" \
"ds_read_b64_tr_b16 a[230:231], v245 offset:38912\n" \
"v_mfma_f32_16x16x32_bf16 v[0:3], a[204:207], v[152:155], v[0:3]\n" \
"buffer_load_dwordx4 v248, s[60:63], s69 offen offset:0 lds\n" \
"v_mfma_f32_16x16x32_bf16 v[0:3], a[208:211], v[160:163], v[0:3]\n" \
"s_waitcnt lgkmcnt(6)\n" \
"ds_read_b64_tr_b16 a[232:233], v245 offset:40960\n" \
"ds_read_b64_tr_b16 a[234:235], v245 offset:43008\n" \
"v_mfma_f32_16x16x32_bf16 v[0:3], a[212:215], v[168:171], v[0:3]\n" \
"v_mfma_f32_16x16x32_bf16 v[0:3], a[216:219], v[176:179], v[0:3]\n" \
"s_waitcnt lgkmcnt(6)\n" \
"ds_read_b64_tr_b16 a[236:237], v245 offset:45056\n" \
"ds_read_b64_tr_b16 a[238:239], v245 offset:47104\n" \
"v_mfma_f32_16x16x32_bf16 v[0:3], a[220:223], v[184:187], v[0:3]\n" \
"buffer_load_dwordx4 v249, s[60:63], s69 offen offset:1024 lds\n" \
"v_mfma_f32_16x16x32_bf16 v[4:7], a[192:195], v[132:135], v[4:7]\n" \
"ds_read_b64_tr_b16 a[240:241], v245 offset:49152\n" \
"ds_read_b64_tr_b16 a[242:243], v245 offset:51200\n" \
"v_mfma_f32_16x16x32_bf16 v[4:7], a[196:199], v[140:143], v[4:7]\n" \
"v_mfma_f32_16x16x32_bf16 v[4:7], a[200:203], v[148:151], v[4:7]\n" \
"ds_read_b64_tr_b16 a[244:245], v245 offset:53248\n" \
"ds_read_b64_tr_b16 a[246:247], v245 offset:55296\n" \
"v_mfma_f32_16x16x32_bf16 v[4:7], a[204:207], v[156:159], v[4:7]\n" \
"buffer_load_dwordx4 v250, s[60:63], s69 offen offset:2048 lds\n" \
"v_mfma_f32_16x16x32_bf16 v[4:7], a[208:211], v[164:167], v[4:7]\n" \
"ds_read_b64_tr_b16 a[248:249], v245 offset:57344\n" \
"ds_read_b64_tr_b16 a[250:251], v245 offset:59392\n" \
"v_mfma_f32_16x16x32_bf16 v[4:7], a[212:215], v[172:175], v[4:7]\n" \
"s_waitcnt lgkmcnt(6)\n" \
"v_mfma_f32_16x16x32_bf16 v[4:7], a[216:219], v[180:183], v[4:7]\n" \
"s_waitcnt vmcnt(15)\n" \
"ds_read_b64_tr_b16 a[252:253], v245 offset:61440\n" \
"ds_read_b64_tr_b16 a[254:255], v245 offset:63488\n" \
"v_mfma_f32_16x16x32_bf16 v[4:7], a[220:223], v[188:191], v[4:7]\n" \
"s_barrier\n" \
"buffer_load_dwordx4 v251, s[60:63], s69 offen offset:3072 lds\n" \
"v_mfma_f32_16x16x32_bf16 v[8:11], a[224:227], v[128:131], v[8:11]\n" \
"ds_read_b64_tr_b16 a[192:193], v246 offset:0\n" \
"ds_read_b64_tr_b16 a[194:195], v246 offset:2048\n" \
"v_mfma_f32_16x16x32_bf16 v[8:11], a[228:231], v[136:139], v[8:11]\n" \
"s_mov_b32 s68, 230144\n" \
"v_mfma_f32_16x16x32_bf16 v[8:11], a[232:235], v[144:147], v[8:11]\n" \
"s_add_u32 m0, s50, 65536\n" \
"ds_read_b64_tr_b16 a[196:197], v246 offset:4096\n" \
"ds_read_b64_tr_b16 a[198:199], v246 offset:6144\n" \
"v_mfma_f32_16x16x32_bf16 v[8:11], a[236:239], v[152:155], v[8:11]\n" \
"s_waitcnt lgkmcnt(6)\n" \
"buffer_load_dwordx4 v248, s[60:63], s68 offen offset:0 lds\n" \
"v_mfma_f32_16x16x32_bf16 v[8:11], a[240:243], v[160:163], v[8:11]\n" \
"ds_read_b64_tr_b16 a[200:201], v246 offset:8192\n" \
"ds_read_b64_tr_b16 a[202:203], v246 offset:10240\n" \
"v_mfma_f32_16x16x32_bf16 v[8:11], a[244:247], v[168:171], v[8:11]\n" \
"v_mfma_f32_16x16x32_bf16 v[8:11], a[248:251], v[176:179], v[8:11]\n" \
"s_waitcnt lgkmcnt(6)\n" \
"ds_read_b64_tr_b16 a[204:205], v246 offset:12288\n" \
"ds_read_b64_tr_b16 a[206:207], v246 offset:14336\n" \
"v_mfma_f32_16x16x32_bf16 v[8:11], a[252:255], v[184:187], v[8:11]\n" \
"buffer_load_dwordx4 v249, s[60:63], s68 offen offset:1024 lds\n" \
"v_mfma_f32_16x16x32_bf16 v[12:15], a[224:227], v[132:135], v[12:15]\n" \
"ds_read_b64_tr_b16 a[208:209], v246 offset:16384\n" \
"ds_read_b64_tr_b16 a[210:211], v246 offset:18432\n" \
"v_mfma_f32_16x16x32_bf16 v[12:15], a[228:231], v[140:143], v[12:15]\n" \
"v_mfma_f32_16x16x32_bf16 v[12:15], a[232:235], v[148:151], v[12:15]\n" \
"ds_read_b64_tr_b16 a[212:213], v246 offset:20480\n" \
"ds_read_b64_tr_b16 a[214:215], v246 offset:22528\n" \
"v_mfma_f32_16x16x32_bf16 v[12:15], a[236:239], v[156:159], v[12:15]\n" \
"buffer_load_dwordx4 v250, s[60:63], s68 offen offset:2048 lds\n" \
"v_mfma_f32_16x16x32_bf16 v[12:15], a[240:243], v[164:167], v[12:15]\n" \
"ds_read_b64_tr_b16 a[216:217], v246 offset:24576\n" \
"ds_read_b64_tr_b16 a[218:219], v246 offset:26624\n" \
"v_mfma_f32_16x16x32_bf16 v[12:15], a[244:247], v[172:175], v[12:15]\n" \
"v_mfma_f32_16x16x32_bf16 v[12:15], a[248:251], v[180:183], v[12:15]\n" \
"ds_read_b64_tr_b16 a[220:221], v246 offset:28672\n" \
"ds_read_b64_tr_b16 a[222:223], v246 offset:30720\n" \
"v_mfma_f32_16x16x32_bf16 v[12:15], a[252:255], v[188:191], v[12:15]\n" \
"s_waitcnt lgkmcnt(6)\n" \
"buffer_load_dwordx4 v251, s[60:63], s68 offen offset:3072 lds\n" \
"v_mfma_f32_16x16x32_bf16 v[16:19], a[192:195], v[64:67], 0\n" \
"ds_read_b64_tr_b16 a[224:225], v247 offset:0\n" \
"ds_read_b64_tr_b16 a[226:227], v247 offset:2048\n" \
"v_mfma_f32_16x16x32_bf16 v[16:19], a[196:199], v[72:75], v[16:19]\n" \
"s_mov_b32 s69, 344832\n" \
"v_mfma_f32_16x16x32_bf16 v[16:19], a[200:203], v[80:83], v[16:19]\n" \
"s_add_u32 m0, s50, 81920\n" \
"ds_read_b64_tr_b16 a[228:229], v247 offset:4096\n" \
"ds_read_b64_tr_b16 a[230:231], v247 offset:6144\n" \
"v_mfma_f32_16x16x32_bf16 v[16:19], a[204:207], v[88:91], v[16:19]\n" \
"buffer_load_dwordx4 v248, s[60:63], s69 offen offset:0 lds\n" \
"v_mfma_f32_16x16x32_bf16 v[16:19], a[208:211], v[96:99], v[16:19]\n" \
"s_waitcnt lgkmcnt(6)\n" \
"ds_read_b64_tr_b16 a[232:233], v247 offset:8192\n" \
"ds_read_b64_tr_b16 a[234:235], v247 offset:10240\n" \
"v_mfma_f32_16x16x32_bf16 v[16:19], a[212:215], v[104:107], v[16:19]\n" \
"v_cvt_pk_bf16_f32 v224, v0, v1\n" \
"v_cvt_pk_bf16_f32 v225, v2, v3\n" \
"v_mfma_f32_16x16x32_bf16 v[16:19], a[216:219], v[112:115], v[16:19]\n" \
"s_waitcnt lgkmcnt(6)\n" \
"ds_read_b64_tr_b16 a[236:237], v247 offset:12288\n" \
"ds_read_b64_tr_b16 a[238:239], v247 offset:14336\n" \
"v_mfma_f32_16x16x32_bf16 v[16:19], a[220:223], v[120:123], v[16:19]\n" \
"buffer_load_dwordx4 v249, s[60:63], s69 offen offset:1024 lds\n" \
"v_mfma_f32_16x16x32_bf16 v[20:23], a[192:195], v[68:71], 0\n" \
"ds_read_b64_tr_b16 a[240:241], v247 offset:16384\n" \
"ds_read_b64_tr_b16 a[242:243], v247 offset:18432\n" \
"v_mfma_f32_16x16x32_bf16 v[20:23], a[196:199], v[76:79], v[20:23]\n" \
"v_cvt_pk_bf16_f32 v226, v8, v9\n" \
"v_cvt_pk_bf16_f32 v227, v10, v11\n" \
"v_mfma_f32_16x16x32_bf16 v[20:23], a[200:203], v[84:87], v[20:23]\n" \
"ds_read_b64_tr_b16 a[244:245], v247 offset:20480\n" \
"ds_read_b64_tr_b16 a[246:247], v247 offset:22528\n" \
"v_mfma_f32_16x16x32_bf16 v[20:23], a[204:207], v[92:95], v[20:23]\n" \
"buffer_load_dwordx4 v250, s[60:63], s69 offen offset:2048 lds\n" \
"v_mfma_f32_16x16x32_bf16 v[20:23], a[208:211], v[100:103], v[20:23]\n" \
"ds_read_b64_tr_b16 a[248:249], v247 offset:24576\n" \
"ds_read_b64_tr_b16 a[250:251], v247 offset:26624\n" \
"v_mfma_f32_16x16x32_bf16 v[20:23], a[212:215], v[108:111], v[20:23]\n" \
"s_waitcnt lgkmcnt(6)\n" \
"v_cvt_pk_bf16_f32 v228, v4, v5\n" \
"v_cvt_pk_bf16_f32 v229, v6, v7\n" \
"v_mfma_f32_16x16x32_bf16 v[20:23], a[216:219], v[116:119], v[20:23]\n" \
"s_waitcnt vmcnt(15)\n" \
"ds_read_b64_tr_b16 a[252:253], v247 offset:28672\n" \
"ds_read_b64_tr_b16 a[254:255], v247 offset:30720\n" \
"v_mfma_f32_16x16x32_bf16 v[20:23], a[220:223], v[124:127], v[20:23]\n" \
"s_barrier\n" \
"buffer_load_dwordx4 v251, s[60:63], s69 offen offset:3072 lds\n" \
"v_mfma_f32_16x16x32_bf16 v[24:27], a[224:227], v[64:67], 0\n" \
"ds_read_b64_tr_b16 a[192:193], v242 offset:0\n" \
"ds_read_b64_tr_b16 a[194:195], v242 offset:2048\n" \
"v_mfma_f32_16x16x32_bf16 v[24:27], a[228:231], v[72:75], v[24:27]\n" \
"s_mov_b32 s68, 0\n" \
"v_cvt_pk_bf16_f32 v230, v12, v13\n" \
"v_cvt_pk_bf16_f32 v231, v14, v15\n" \
"v_mfma_f32_16x16x32_bf16 v[24:27], a[232:235], v[80:83], v[24:27]\n" \
"s_add_u32 m0, s50, 98304\n" \
"ds_read_b64_tr_b16 a[196:197], v242 offset:4096\n" \
"ds_read_b64_tr_b16 a[198:199], v242 offset:6144\n" \
"v_mfma_f32_16x16x32_bf16 v[24:27], a[236:239], v[88:91], v[24:27]\n" \
"s_waitcnt lgkmcnt(6)\n" \
"buffer_load_dwordx4 v48, s[36:39], s68 offen offset:0 lds\n" \
"v_mfma_f32_16x16x32_bf16 v[24:27], a[240:243], v[96:99], v[24:27]\n" \
"ds_read_b64_tr_b16 a[200:201], v242 offset:8192\n" \
"ds_read_b64_tr_b16 a[202:203], v242 offset:10240\n" \
"v_mfma_f32_16x16x32_bf16 v[24:27], a[244:247], v[104:107], v[24:27]\n" \
"v_mfma_f32_16x16x32_bf16 v[24:27], a[248:251], v[112:115], v[24:27]\n" \
"s_waitcnt lgkmcnt(6)\n" \
"ds_read_b64_tr_b16 a[204:205], v242 offset:12288\n" \
"ds_read_b64_tr_b16 a[206:207], v242 offset:14336\n" \
"v_mfma_f32_16x16x32_bf16 v[24:27], a[252:255], v[120:123], v[24:27]\n" \
"buffer_load_dwordx4 v49, s[36:39], s68 offen offset:1024 lds\n" \
"v_mfma_f32_16x16x32_bf16 v[28:31], a[224:227], v[68:71], 0\n" \
"ds_read_b64_tr_b16 a[208:209], v242 offset:16384\n" \
"ds_read_b64_tr_b16 a[210:211], v242 offset:18432\n" \
"v_mfma_f32_16x16x32_bf16 v[28:31], a[228:231], v[76:79], v[28:31]\n" \
"v_mfma_f32_16x16x32_bf16 v[28:31], a[232:235], v[84:87], v[28:31]\n" \
"ds_read_b64_tr_b16 a[212:213], v242 offset:20480\n" \
"ds_read_b64_tr_b16 a[214:215], v242 offset:22528\n" \
"v_mfma_f32_16x16x32_bf16 v[28:31], a[236:239], v[92:95], v[28:31]\n" \
"buffer_load_dwordx4 v50, s[36:39], s68 offen offset:2048 lds\n" \
"v_mfma_f32_16x16x32_bf16 v[28:31], a[240:243], v[100:103], v[28:31]\n" \
"ds_read_b64_tr_b16 a[216:217], v242 offset:24576\n" \
"ds_read_b64_tr_b16 a[218:219], v242 offset:26624\n" \
"v_mfma_f32_16x16x32_bf16 v[28:31], a[244:247], v[108:111], v[28:31]\n" \
"v_mfma_f32_16x16x32_bf16 v[28:31], a[248:251], v[116:119], v[28:31]\n" \
"ds_read_b64_tr_b16 a[220:221], v242 offset:28672\n" \
"ds_read_b64_tr_b16 a[222:223], v242 offset:30720\n" \
"v_mfma_f32_16x16x32_bf16 v[28:31], a[252:255], v[124:127], v[28:31]\n" \
"s_waitcnt lgkmcnt(6)\n" \
"buffer_load_dwordx4 v51, s[36:39], s68 offen offset:3072 lds\n" \
"v_mfma_f32_16x16x32_bf16 v[16:19], a[192:195], v[128:131], v[16:19]\n" \
"ds_read_b64_tr_b16 a[224:225], v243 offset:0\n" \
"ds_read_b64_tr_b16 a[226:227], v243 offset:2048\n" \
"v_mfma_f32_16x16x32_bf16 v[16:19], a[196:199], v[136:139], v[16:19]\n" \
"s_mov_b32 s69, 229376\n" \
"v_mfma_f32_16x16x32_bf16 v[16:19], a[200:203], v[144:147], v[16:19]\n" \
"s_add_u32 m0, s50, 114688\n" \
"ds_read_b64_tr_b16 a[228:229], v243 offset:4096\n" \
"ds_read_b64_tr_b16 a[230:231], v243 offset:6144\n" \
"v_mfma_f32_16x16x32_bf16 v[16:19], a[204:207], v[152:155], v[16:19]\n" \
"buffer_load_dwordx4 v48, s[36:39], s69 offen offset:0 lds\n" \
"v_mfma_f32_16x16x32_bf16 v[16:19], a[208:211], v[160:163], v[16:19]\n" \
"s_waitcnt lgkmcnt(6)\n" \
"ds_read_b64_tr_b16 a[232:233], v243 offset:8192\n" \
"ds_read_b64_tr_b16 a[234:235], v243 offset:10240\n" \
"v_mfma_f32_16x16x32_bf16 v[16:19], a[212:215], v[168:171], v[16:19]\n" \
"v_mfma_f32_16x16x32_bf16 v[16:19], a[216:219], v[176:179], v[16:19]\n" \
"s_waitcnt lgkmcnt(6)\n" \
"ds_read_b64_tr_b16 a[236:237], v243 offset:12288\n" \
"ds_read_b64_tr_b16 a[238:239], v243 offset:14336\n" \
"v_mfma_f32_16x16x32_bf16 v[16:19], a[220:223], v[184:187], v[16:19]\n" \
"buffer_load_dwordx4 v49, s[36:39], s69 offen offset:1024 lds\n" \
"v_mfma_f32_16x16x32_bf16 v[20:23], a[192:195], v[132:135], v[20:23]\n" \
"ds_read_b64_tr_b16 a[240:241], v243 offset:16384\n" \
"ds_read_b64_tr_b16 a[242:243], v243 offset:18432\n" \
"v_mfma_f32_16x16x32_bf16 v[20:23], a[196:199], v[140:143], v[20:23]\n" \
"v_mfma_f32_16x16x32_bf16 v[20:23], a[200:203], v[148:151], v[20:23]\n" \
"ds_read_b64_tr_b16 a[244:245], v243 offset:20480\n" \
"ds_read_b64_tr_b16 a[246:247], v243 offset:22528\n" \
"v_mfma_f32_16x16x32_bf16 v[20:23], a[204:207], v[156:159], v[20:23]\n" \
"buffer_load_dwordx4 v50, s[36:39], s69 offen offset:2048 lds\n" \
"v_mfma_f32_16x16x32_bf16 v[20:23], a[208:211], v[164:167], v[20:23]\n" \
"ds_read_b64_tr_b16 a[248:249], v243 offset:24576\n" \
"ds_read_b64_tr_b16 a[250:251], v243 offset:26624\n" \
"v_mfma_f32_16x16x32_bf16 v[20:23], a[212:215], v[172:175], v[20:23]\n" \
"s_waitcnt lgkmcnt(6)\n" \
"v_mfma_f32_16x16x32_bf16 v[20:23], a[216:219], v[180:183], v[20:23]\n" \
"s_waitcnt vmcnt(15)\n" \
"ds_read_b64_tr_b16 a[252:253], v243 offset:28672\n" \
"ds_read_b64_tr_b16 a[254:255], v243 offset:30720\n" \
"v_mfma_f32_16x16x32_bf16 v[20:23], a[220:223], v[188:191], v[20:23]\n" \
"s_barrier\n" \
"buffer_load_dwordx4 v51, s[36:39], s69 offen offset:3072 lds\n" \
"v_mfma_f32_16x16x32_bf16 v[24:27], a[224:227], v[128:131], v[24:27]\n" \
"ds_read_b64_tr_b16 a[192:193], v242 offset:32768\n" \
"ds_read_b64_tr_b16 a[194:195], v242 offset:34816\n" \
"v_mfma_f32_16x16x32_bf16 v[24:27], a[228:231], v[136:139], v[24:27]\n" \
"s_mov_b32 s68, 458752\n" \
"v_mfma_f32_16x16x32_bf16 v[24:27], a[232:235], v[144:147], v[24:27]\n" \
"s_add_u32 m0, s50, 131072\n" \
"ds_read_b64_tr_b16 a[196:197], v242 offset:36864\n" \
"ds_read_b64_tr_b16 a[198:199], v242 offset:38912\n" \
"v_mfma_f32_16x16x32_bf16 v[24:27], a[236:239], v[152:155], v[24:27]\n" \
"s_waitcnt lgkmcnt(6)\n" \
"buffer_load_dwordx4 v48, s[36:39], s68 offen offset:0 lds\n" \
"v_mfma_f32_16x16x32_bf16 v[24:27], a[240:243], v[160:163], v[24:27]\n" \
"ds_read_b64_tr_b16 a[200:201], v242 offset:40960\n" \
"ds_read_b64_tr_b16 a[202:203], v242 offset:43008\n" \
"v_mfma_f32_16x16x32_bf16 v[24:27], a[244:247], v[168:171], v[24:27]\n" \
"v_mfma_f32_16x16x32_bf16 v[24:27], a[248:251], v[176:179], v[24:27]\n" \
"s_waitcnt lgkmcnt(6)\n" \
"ds_read_b64_tr_b16 a[204:205], v242 offset:45056\n" \
"ds_read_b64_tr_b16 a[206:207], v242 offset:47104\n" \
"v_mfma_f32_16x16x32_bf16 v[24:27], a[252:255], v[184:187], v[24:27]\n" \
"buffer_load_dwordx4 v49, s[36:39], s68 offen offset:1024 lds\n" \
"v_mfma_f32_16x16x32_bf16 v[28:31], a[224:227], v[132:135], v[28:31]\n" \
"ds_read_b64_tr_b16 a[208:209], v242 offset:49152\n" \
"ds_read_b64_tr_b16 a[210:211], v242 offset:51200\n" \
"v_mfma_f32_16x16x32_bf16 v[28:31], a[228:231], v[140:143], v[28:31]\n" \
"v_mfma_f32_16x16x32_bf16 v[28:31], a[232:235], v[148:151], v[28:31]\n" \
"ds_read_b64_tr_b16 a[212:213], v242 offset:53248\n" \
"ds_read_b64_tr_b16 a[214:215], v242 offset:55296\n" \
"v_mfma_f32_16x16x32_bf16 v[28:31], a[236:239], v[156:159], v[28:31]\n" \
"buffer_load_dwordx4 v50, s[36:39], s68 offen offset:2048 lds\n" \
"v_mfma_f32_16x16x32_bf16 v[28:31], a[240:243], v[164:167], v[28:31]\n" \
"ds_read_b64_tr_b16 a[216:217], v242 offset:57344\n" \
"ds_read_b64_tr_b16 a[218:219], v242 offset:59392\n" \
"v_mfma_f32_16x16x32_bf16 v[28:31], a[244:247], v[172:175], v[28:31]\n" \
"v_mfma_f32_16x16x32_bf16 v[28:31], a[248:251], v[180:183], v[28:31]\n" \
"ds_read_b64_tr_b16 a[220:221], v242 offset:61440\n" \
"ds_read_b64_tr_b16 a[222:223], v242 offset:63488\n" \
"v_mfma_f32_16x16x32_bf16 v[28:31], a[252:255], v[188:191], v[28:31]\n" \
"s_waitcnt lgkmcnt(6)\n" \
"buffer_load_dwordx4 v51, s[36:39], s68 offen offset:3072 lds\n" \
"v_mfma_f32_16x16x32_bf16 v[0:3], a[192:195], v[64:67], 0\n" \
"ds_read_b64_tr_b16 a[224:225], v243 offset:32768\n" \
"ds_read_b64_tr_b16 a[226:227], v243 offset:34816\n" \
"v_mfma_f32_16x16x32_bf16 v[0:3], a[196:199], v[72:75], v[0:3]\n" \
"s_mov_b32 s69, 688128\n" \
"v_mfma_f32_16x16x32_bf16 v[0:3], a[200:203], v[80:83], v[0:3]\n" \
"s_add_u32 m0, s50, 147456\n" \
"ds_read_b64_tr_b16 a[228:229], v243 offset:36864\n" \
"ds_read_b64_tr_b16 a[230:231], v243 offset:38912\n" \
"v_mfma_f32_16x16x32_bf16 v[0:3], a[204:207], v[88:91], v[0:3]\n" \
"buffer_load_dwordx4 v48, s[36:39], s69 offen offset:0 lds\n" \
"v_mfma_f32_16x16x32_bf16 v[0:3], a[208:211], v[96:99], v[0:3]\n" \
"s_waitcnt lgkmcnt(6)\n" \
"ds_read_b64_tr_b16 a[232:233], v243 offset:40960\n" \
"ds_read_b64_tr_b16 a[234:235], v243 offset:43008\n" \
"v_mfma_f32_16x16x32_bf16 v[0:3], a[212:215], v[104:107], v[0:3]\n" \
"v_cvt_pk_bf16_f32 v232, v16, v17\n" \
"v_cvt_pk_bf16_f32 v233, v18, v19\n" \
"v_mfma_f32_16x16x32_bf16 v[0:3], a[216:219], v[112:115], v[0:3]\n" \
"s_waitcnt lgkmcnt(6)\n" \
"ds_read_b64_tr_b16 a[236:237], v243 offset:45056\n" \
"ds_read_b64_tr_b16 a[238:239], v243 offset:47104\n" \
"v_mfma_f32_16x16x32_bf16 v[0:3], a[220:223], v[120:123], v[0:3]\n" \
"buffer_load_dwordx4 v49, s[36:39], s69 offen offset:1024 lds\n" \
"v_mfma_f32_16x16x32_bf16 v[4:7], a[192:195], v[68:71], 0\n" \
"ds_read_b64_tr_b16 a[240:241], v243 offset:49152\n" \
"ds_read_b64_tr_b16 a[242:243], v243 offset:51200\n" \
"v_mfma_f32_16x16x32_bf16 v[4:7], a[196:199], v[76:79], v[4:7]\n" \
"v_cvt_pk_bf16_f32 v234, v24, v25\n" \
"v_cvt_pk_bf16_f32 v235, v26, v27\n" \
"v_mfma_f32_16x16x32_bf16 v[4:7], a[200:203], v[84:87], v[4:7]\n" \
"ds_read_b64_tr_b16 a[244:245], v243 offset:53248\n" \
"ds_read_b64_tr_b16 a[246:247], v243 offset:55296\n" \
"v_mfma_f32_16x16x32_bf16 v[4:7], a[204:207], v[92:95], v[4:7]\n" \
"buffer_load_dwordx4 v50, s[36:39], s69 offen offset:2048 lds\n" \
"v_mfma_f32_16x16x32_bf16 v[4:7], a[208:211], v[100:103], v[4:7]\n" \
"ds_read_b64_tr_b16 a[248:249], v243 offset:57344\n" \
"ds_read_b64_tr_b16 a[250:251], v243 offset:59392\n" \
"v_mfma_f32_16x16x32_bf16 v[4:7], a[212:215], v[108:111], v[4:7]\n" \
"s_waitcnt lgkmcnt(6)\n" \
"v_cvt_pk_bf16_f32 v236, v20, v21\n" \
"v_cvt_pk_bf16_f32 v237, v22, v23\n" \
"v_mfma_f32_16x16x32_bf16 v[4:7], a[216:219], v[116:119], v[4:7]\n" \
"s_waitcnt vmcnt(15)\n" \
"ds_read_b64_tr_b16 a[252:253], v243 offset:61440\n" \
"ds_read_b64_tr_b16 a[254:255], v243 offset:63488\n" \
"v_mfma_f32_16x16x32_bf16 v[4:7], a[220:223], v[124:127], v[4:7]\n" \
"s_barrier\n" \
"buffer_load_dwordx4 v51, s[36:39], s69 offen offset:3072 lds\n" \
"v_mfma_f32_16x16x32_bf16 v[8:11], a[224:227], v[64:67], 0\n" \
"ds_read_b64_tr_b16 a[192:193], v244 offset:0\n" \
"ds_read_b64_tr_b16 a[194:195], v244 offset:2048\n" \
"v_mfma_f32_16x16x32_bf16 v[8:11], a[228:231], v[72:75], v[8:11]\n" \
"s_mov_b32 s68, 917504\n" \
"v_cvt_pk_bf16_f32 v238, v28, v29\n" \
"v_cvt_pk_bf16_f32 v239, v30, v31\n" \
"v_mfma_f32_16x16x32_bf16 v[8:11], a[232:235], v[80:83], v[8:11]\n" \
"s_add_u32 m0, s50, 0\n" \
"ds_read_b64_tr_b16 a[196:197], v244 offset:4096\n" \
"ds_read_b64_tr_b16 a[198:199], v244 offset:6144\n" \
"v_mfma_f32_16x16x32_bf16 v[8:11], a[236:239], v[88:91], v[8:11]\n" \
"s_waitcnt lgkmcnt(6)\n" \
"buffer_load_dwordx4 v48, s[36:39], s68 offen offset:0 lds\n" \
"v_mfma_f32_16x16x32_bf16 v[8:11], a[240:243], v[96:99], v[8:11]\n" \
"ds_read_b64_tr_b16 a[200:201], v244 offset:8192\n" \
"ds_read_b64_tr_b16 a[202:203], v244 offset:10240\n" \
"v_mfma_f32_16x16x32_bf16 v[8:11], a[244:247], v[104:107], v[8:11]\n" \
"v_mfma_f32_16x16x32_bf16 v[8:11], a[248:251], v[112:115], v[8:11]\n" \
"s_waitcnt lgkmcnt(6)\n" \
"ds_read_b64_tr_b16 a[204:205], v244 offset:12288\n" \
"ds_read_b64_tr_b16 a[206:207], v244 offset:14336\n" \
"v_mfma_f32_16x16x32_bf16 v[8:11], a[252:255], v[120:123], v[8:11]\n" \
"buffer_load_dwordx4 v49, s[36:39], s68 offen offset:1024 lds\n" \
"v_mfma_f32_16x16x32_bf16 v[12:15], a[224:227], v[68:71], 0\n" \
"ds_read_b64_tr_b16 a[208:209], v244 offset:16384\n" \
"ds_read_b64_tr_b16 a[210:211], v244 offset:18432\n" \
"v_mfma_f32_16x16x32_bf16 v[12:15], a[228:231], v[76:79], v[12:15]\n" \
"v_mfma_f32_16x16x32_bf16 v[12:15], a[232:235], v[84:87], v[12:15]\n" \
"ds_read_b64_tr_b16 a[212:213], v244 offset:20480\n" \
"ds_read_b64_tr_b16 a[214:215], v244 offset:22528\n" \
"v_mfma_f32_16x16x32_bf16 v[12:15], a[236:239], v[92:95], v[12:15]\n" \
"buffer_load_dwordx4 v50, s[36:39], s68 offen offset:2048 lds\n" \
"v_mfma_f32_16x16x32_bf16 v[12:15], a[240:243], v[100:103], v[12:15]\n" \
"ds_read_b64_tr_b16 a[216:217], v244 offset:24576\n" \
"ds_read_b64_tr_b16 a[218:219], v244 offset:26624\n" \
"v_mfma_f32_16x16x32_bf16 v[12:15], a[244:247], v[108:111], v[12:15]\n" \
"v_mfma_f32_16x16x32_bf16 v[12:15], a[248:251], v[116:119], v[12:15]\n" \
"ds_read_b64_tr_b16 a[220:221], v244 offset:28672\n" \
"ds_read_b64_tr_b16 a[222:223], v244 offset:30720\n" \
"v_mfma_f32_16x16x32_bf16 v[12:15], a[252:255], v[124:127], v[12:15]\n" \
"s_waitcnt lgkmcnt(6)\n" \
"buffer_load_dwordx4 v51, s[36:39], s68 offen offset:3072 lds\n" \
"v_mfma_f32_16x16x32_bf16 v[0:3], a[192:195], v[128:131], v[0:3]\n" \
"ds_read_b64_tr_b16 a[224:225], v245 offset:0\n" \
"ds_read_b64_tr_b16 a[226:227], v245 offset:2048\n" \
"v_mfma_f32_16x16x32_bf16 v[0:3], a[196:199], v[136:139], v[0:3]\n" \
"s_mov_b32 s69, 1146880\n" \
"v_mfma_f32_16x16x32_bf16 v[0:3], a[200:203], v[144:147], v[0:3]\n" \
"s_add_u32 m0, s50, 16384\n" \
"ds_read_b64_tr_b16 a[228:229], v245 offset:4096\n" \
"ds_read_b64_tr_b16 a[230:231], v245 offset:6144\n" \
"v_mfma_f32_16x16x32_bf16 v[0:3], a[204:207], v[152:155], v[0:3]\n" \
"buffer_load_dwordx4 v48, s[36:39], s69 offen offset:0 lds\n" \
"v_mfma_f32_16x16x32_bf16 v[0:3], a[208:211], v[160:163], v[0:3]\n" \
"s_waitcnt lgkmcnt(6)\n" \
"ds_read_b64_tr_b16 a[232:233], v245 offset:8192\n" \
"ds_read_b64_tr_b16 a[234:235], v245 offset:10240\n" \
"v_mfma_f32_16x16x32_bf16 v[0:3], a[212:215], v[168:171], v[0:3]\n" \
"v_mfma_f32_16x16x32_bf16 v[0:3], a[216:219], v[176:179], v[0:3]\n" \
"s_waitcnt lgkmcnt(6)\n" \
"ds_read_b64_tr_b16 a[236:237], v245 offset:12288\n" \
"ds_read_b64_tr_b16 a[238:239], v245 offset:14336\n" \
"v_mfma_f32_16x16x32_bf16 v[0:3], a[220:223], v[184:187], v[0:3]\n" \
"buffer_load_dwordx4 v49, s[36:39], s69 offen offset:1024 lds\n" \
"v_mfma_f32_16x16x32_bf16 v[4:7], a[192:195], v[132:135], v[4:7]\n" \
"ds_read_b64_tr_b16 a[240:241], v245 offset:16384\n" \
"ds_read_b64_tr_b16 a[242:243], v245 offset:18432\n" \
"v_mfma_f32_16x16x32_bf16 v[4:7], a[196:199], v[140:143], v[4:7]\n" \
"v_mfma_f32_16x16x32_bf16 v[4:7], a[200:203], v[148:151], v[4:7]\n" \
"ds_read_b64_tr_b16 a[244:245], v245 offset:20480\n" \
"ds_read_b64_tr_b16 a[246:247], v245 offset:22528\n" \
"v_mfma_f32_16x16x32_bf16 v[4:7], a[204:207], v[156:159], v[4:7]\n" \
"buffer_load_dwordx4 v50, s[36:39], s69 offen offset:2048 lds\n" \
"v_mfma_f32_16x16x32_bf16 v[4:7], a[208:211], v[164:167], v[4:7]\n" \
"ds_read_b64_tr_b16 a[248:249], v245 offset:24576\n" \
"ds_read_b64_tr_b16 a[250:251], v245 offset:26624\n" \
"v_mfma_f32_16x16x32_bf16 v[4:7], a[212:215], v[172:175], v[4:7]\n" \
"v_mfma_f32_16x16x32_bf16 v[4:7], a[216:219], v[180:183], v[4:7]\n" \
"ds_read_b64_tr_b16 a[252:253], v245 offset:28672\n" \
"ds_read_b64_tr_b16 a[254:255], v245 offset:30720\n" \
"v_mfma_f32_16x16x32_bf16 v[4:7], a[220:223], v[188:191], v[4:7]\n" \
"s_waitcnt lgkmcnt(6)\n" \
"buffer_load_dwordx4 v51, s[36:39], s69 offen offset:3072 lds\n" \
"v_mfma_f32_16x16x32_bf16 v[8:11], a[224:227], v[128:131], v[8:11]\n" \
"v_mfma_f32_16x16x32_bf16 v[8:11], a[228:231], v[136:139], v[8:11]\n" \
"v_mfma_f32_16x16x32_bf16 v[8:11], a[232:235], v[144:147], v[8:11]\n" \
"v_mfma_f32_16x16x32_bf16 v[8:11], a[236:239], v[152:155], v[8:11]\n" \
"v_mfma_f32_16x16x32_bf16 v[8:11], a[240:243], v[160:163], v[8:11]\n" \
"s_waitcnt lgkmcnt(2)\n" \
"v_mfma_f32_16x16x32_bf16 v[8:11], a[244:247], v[168:171], v[8:11]\n" \
"v_mfma_f32_16x16x32_bf16 v[8:11], a[248:251], v[176:179], v[8:11]\n" \
"s_waitcnt lgkmcnt(0)\n" \
"v_mfma_f32_16x16x32_bf16 v[8:11], a[252:255], v[184:187], v[8:11]\n" \
"v_mfma_f32_16x16x32_bf16 v[12:15], a[224:227], v[132:135], v[12:15]\n" \
"v_mfma_f32_16x16x32_bf16 v[12:15], a[228:231], v[140:143], v[12:15]\n" \
"v_mfma_f32_16x16x32_bf16 v[12:15], a[232:235], v[148:151], v[12:15]\n" \
"v_mfma_f32_16x16x32_bf16 v[12:15], a[236:239], v[156:159], v[12:15]\n" \
"v_mfma_f32_16x16x32_bf16 v[12:15], a[240:243], v[164:167], v[12:15]\n" \
"v_mfma_f32_16x16x32_bf16 v[12:15], a[244:247], v[172:175], v[12:15]\n" \
"v_mfma_f32_16x16x32_bf16 v[12:15], a[248:251], v[180:183], v[12:15]\n" \
"v_mfma_f32_16x16x32_bf16 v[12:15], a[252:255], v[188:191], v[12:15]\n" \
"buffer_store_dwordx4 v[216:219], v252, s[64:67], 0 offen offset:0 sc1\n" \
"buffer_store_dwordx4 v[220:223], v252, s[64:67], 0 offen offset:1024 sc1\n" \
"v_add_u32 v252, 0x2000, v252\n" \
"buffer_store_dwordx4 v[224:227], v252, s[64:67], 0 offen offset:0 sc1\n" \
"buffer_store_dwordx4 v[228:231], v252, s[64:67], 0 offen offset:1024 sc1\n" \
"v_add_u32 v252, 0x2000, v252\n" \
"buffer_store_dwordx4 v[232:235], v252, s[64:67], 0 offen offset:0 sc1\n" \
"buffer_store_dwordx4 v[236:239], v252, s[64:67], 0 offen offset:1024 sc1\n" \
"v_add_u32 v252, 0x2000, v252\n" \
"v_cvt_pk_bf16_f32 v32, v0, v1\n" \
"v_cvt_pk_bf16_f32 v33, v2, v3\n" \
"v_cvt_pk_bf16_f32 v34, v8, v9\n" \
"v_cvt_pk_bf16_f32 v35, v10, v11\n" \
"v_cvt_pk_bf16_f32 v36, v4, v5\n" \
"v_cvt_pk_bf16_f32 v37, v6, v7\n" \
"v_cvt_pk_bf16_f32 v38, v12, v13\n" \
"v_cvt_pk_bf16_f32 v39, v14, v15\n" \
"s_nop 0\n" \
"buffer_store_dwordx4 v[32:35], v252, s[64:67], 0 offen offset:0 sc1\n" \
"buffer_store_dwordx4 v[36:39], v252, s[64:67], 0 offen offset:1024 sc1\n" \
"s_waitcnt vmcnt(0)\n" \
"v_mov_b32 v56, s30\n" \
"s_waitcnt lgkmcnt(0)\n" \
"s_barrier\n" \
"buffer_store_dword v56, v55, s[76:79], 0 offen sc1\n" \
"buffer_load_dword v60, v61, s[76:79], 0 offen sc1\n"
#define K4_ASM \
"s_mov_b32 s69, 0x100000\n" \
"L_w1_%=:\n" \
"buffer_load_dword v16, v229, s[76:79], 0 offen sc1\n" \
"s_sub_u32 s69, s69, 1\n" \
"s_cbranch_scc1 L_g1_%=\n" \
"s_waitcnt vmcnt(0)\n" \
"v_cmp_ne_u32 vcc, s30, v16\n" \
"s_cbranch_vccnz L_w1_%=\n" \
"L_g1_%=:\n" \
"s_add_u32 m0, s50, 32768\n" \
"s_mov_b32 s68, 0\n" \
"s_nop 0\n" \
"buffer_load_dwordx4 v231, s[72:75], s68 offen offset:0 sc1 lds\n" \
"buffer_load_dwordx4 v231, s[72:75], s68 offen offset:1024 sc1 lds\n" \
"buffer_load_dwordx4 v231, s[72:75], s68 offen offset:2048 sc1 lds\n" \
"buffer_load_dwordx4 v231, s[72:75], s68 offen offset:3072 sc1 lds\n" \
"s_add_u32 m0, s50, 49152\n" \
"s_mov_b32 s69, 16384\n" \
"s_nop 0\n" \
"buffer_load_dwordx4 v231, s[72:75], s69 offen offset:0 sc1 lds\n" \
"buffer_load_dwordx4 v231, s[72:75], s69 offen offset:1024 sc1 lds\n" \
"buffer_load_dwordx4 v231, s[72:75], s69 offen offset:2048 sc1 lds\n" \
"buffer_load_dwordx4 v231, s[72:75], s69 offen offset:3072 sc1 lds\n" \
"s_add_u32 m0, s50, 65536\n" \
"s_mov_b32 s68, 32768\n" \
"s_nop 0\n" \
"buffer_load_dwordx4 v231, s[72:75], s68 offen offset:0 sc1 lds\n" \
"buffer_load_dwordx4 v231, s[72:75], s68 offen offset:1024 sc1 lds\n" \
"buffer_load_dwordx4 v231, s[72:75], s68 offen offset:2048 sc1 lds\n" \
"buffer_load_dwordx4 v231, s[72:75], s68 offen offset:3072 sc1 lds\n" \
"v_add_u32 v231, v210, v231\n" \
"s_add_u32 m0, s50, 81920\n" \
"s_mov_b32 s69, 49152\n" \
"s_nop 0\n" \
"buffer_load_dwordx4 v231, s[72:75], s69 offen offset:0 sc1 lds\n" \
"buffer_load_dwordx4 v231, s[72:75], s69 offen offset:1024 sc1 lds\n" \
"buffer_load_dwordx4 v231, s[72:75], s69 offen offset:2048 sc1 lds\n" \
"buffer_load_dwordx4 v231, s[72:75], s69 offen offset:3072 sc1 lds\n" \
"v_mov_b32 v208, 0xbdd2d3e8\n" \
"v_mov_b32 v209, 0xc0135761\n" \
"s_waitcnt vmcnt(12)\n" \
"s_barrier\n" \
"ds_read_b128 v[64:67], v228 offset:0\n" \
"ds_read_b128 v[72:75], v228 offset:4096\n" \
"ds_read_b128 v[80:83], v228 offset:8192\n" \
"ds_read_b128 v[88:91], v228 offset:12288\n" \
"ds_read_b128 v[68:71], v228 offset:1024\n" \
"ds_read_b128 v[76:79], v228 offset:5120\n" \
"ds_read_b128 v[84:87], v228 offset:9216\n" \
"ds_read_b128 v[92:95], v228 offset:13312\n" \
"ds_read_b64_tr_b16 a[192:193], v216 offset:32768\n" \
"ds_read_b64_tr_b16 a[194:195], v216 offset:34816\n" \
"ds_read_b64_tr_b16 a[196:197], v216 offset:36864\n" \
"ds_read_b64_tr_b16 a[198:199], v216 offset:38912\n" \
"ds_read_b64_tr_b16 a[200:201], v216 offset:49152\n" \
"ds_read_b64_tr_b16 a[202:203], v216 offset:51200\n" \
"ds_read_b64_tr_b16 a[204:205], v216 offset:53248\n" \
"ds_read_b64_tr_b16 a[206:207], v216 offset:55296\n" \
"ds_read_b64_tr_b16 a[208:209], v217 offset:32768\n" \
"ds_read_b64_tr_b16 a[210:211], v217 offset:34816\n" \
"ds_read_b64_tr_b16 a[212:213], v217 offset:36864\n" \
"ds_read_b64_tr_b16 a[214:215], v217 offset:38912\n" \
"ds_read_b64_tr_b16 a[216:217], v217 offset:49152\n" \
"ds_read_b64_tr_b16 a[218:219], v217 offset:51200\n" \
"ds_read_b64_tr_b16 a[220:221], v217 offset:53248\n" \
"ds_read_b64_tr_b16 a[222:223], v217 offset:55296\n" \
"s_waitcnt lgkmcnt(14)\n" \
"v_mfma_f32_16x16x32_bf16 v[0:3], a[192:195], v[64:67], v[176:179]\n" \
"s_waitcnt lgkmcnt(12)\n" \
"ds_read_b64_tr_b16 a[224:225], v218 offset:32768\n" \
"ds_read_b64_tr_b16 a[226:227], v218 offset:34816\n" \
"v_mfma_f32_16x16x32_bf16 v[0:3], a[196:199], v[72:75], v[0:3]\n" \
"s_waitcnt lgkmcnt(12)\n" \
"v_mfma_f32_16x16x32_bf16 v[0:3], a[200:203], v[80:83], v[0:3]\n" \
"s_waitcnt lgkmcnt(10)\n" \
"ds_read_b64_tr_b16 a[228:229], v218 offset:36864\n" \
"ds_read_b64_tr_b16 a[230:231], v218 offset:38912\n" \
"v_mfma_f32_16x16x32_bf16 v[0:3], a[204:207], v[88:91], v[0:3]\n" \
"v_mfma_f32_16x16x32_bf16 v[4:7], a[192:195], v[68:71], v[176:179]\n" \
"ds_read_b64_tr_b16 a[232:233], v218 offset:49152\n" \
"ds_read_b64_tr_b16 a[234:235], v218 offset:51200\n" \
"v_mfma_f32_16x16x32_bf16 v[4:7], a[196:199], v[76:79], v[4:7]\n" \
"v_mfma_f32_16x16x32_bf16 v[4:7], a[200:203], v[84:87], v[4:7]\n" \
"ds_read_b64_tr_b16 a[236:237], v218 offset:53248\n" \
"ds_read_b64_tr_b16 a[238:239], v218 offset:55296\n" \
"v_mfma_f32_16x16x32_bf16 v[4:7], a[204:207], v[92:95], v[4:7]\n" \
"s_waitcnt lgkmcnt(6)\n" \
"v_mfma_f32_16x16x32_bf16 v[8:11], a[208:211], v[64:67], v[180:183]\n" \
"ds_read_b64_tr_b16 a[240:241], v219 offset:32768\n" \
"ds_read_b64_tr_b16 a[242:243], v219 offset:34816\n" \
"v_mfma_f32_16x16x32_bf16 v[8:11], a[212:215], v[72:75], v[8:11]\n" \
"v_mfma_f32_16x16x32_bf16 v[8:11], a[216:219], v[80:83], v[8:11]\n" \
"ds_read_b64_tr_b16 a[244:245], v219 offset:36864\n" \
"ds_read_b64_tr_b16 a[246:247], v219 offset:38912\n" \
"v_mfma_f32_16x16x32_bf16 v[8:11], a[220:223], v[88:91], v[8:11]\n" \
"v_mfma_f32_16x16x32_bf16 v[12:15], a[208:211], v[68:71], v[180:183]\n" \
"ds_read_b64_tr_b16 a[248:249], v219 offset:49152\n" \
"ds_read_b64_tr_b16 a[250:251], v219 offset:51200\n" \
"v_mfma_f32_16x16x32_bf16 v[12:15], a[212:215], v[76:79], v[12:15]\n" \
"v_mfma_f32_16x16x32_bf16 v[12:15], a[216:219], v[84:87], v[12:15]\n" \
"s_waitcnt vmcnt(8)\n" \
"ds_read_b64_tr_b16 a[252:253], v219 offset:53248\n" \
"ds_read_b64_tr_b16 a[254:255], v219 offset:55296\n" \
"v_mfma_f32_16x16x32_bf16 v[12:15], a[220:223], v[92:95], v[12:15]\n" \
"s_barrier\n" \
"ds_read_b128 v[96:99], v228 offset:16384\n" \
"ds_read_b128 v[104:107], v228 offset:20480\n" \
"v_mfma_f32_16x16x32_bf16 v[16:19], a[224:227], v[64:67], v[184:187]\n" \
"s_waitcnt lgkmcnt(8)\n" \
"ds_read_b64_tr_b16 a[192:193], v220 offset:0\n" \
"ds_read_b64_tr_b16 a[194:195], v220 offset:2048\n" \
"ds_read_b128 v[112:115], v228 offset:24576\n" \
"v_mfma_f32_16x16x32_bf16 v[16:19], a[228:231], v[72:75], v[16:19]\n" \
"ds_read_b128 v[120:123], v228 offset:28672\n" \
"ds_read_b128 v[100:103], v228 offset:17408\n" \
"v_mfma_f32_16x16x32_bf16 v[16:19], a[232:235], v[80:83], v[16:19]\n" \
"ds_read_b64_tr_b16 a[196:197], v220 offset:4096\n" \
"ds_read_b64_tr_b16 a[198:199], v220 offset:6144\n" \
"ds_read_b128 v[108:111], v228 offset:21504\n" \
"v_mfma_f32_16x16x32_bf16 v[16:19], a[236:239], v[88:91], v[16:19]\n" \
"ds_read_b128 v[116:119], v228 offset:25600\n" \
"ds_read_b128 v[124:127], v228 offset:29696\n" \
"v_mfma_f32_16x16x32_bf16 v[20:23], a[224:227], v[68:71], v[184:187]\n" \
"ds_read_b64_tr_b16 a[200:201], v220 offset:16384\n" \
"ds_read_b64_tr_b16 a[202:203], v220 offset:18432\n" \
"v_mfma_f32_16x16x32_bf16 v[20:23], a[228:231], v[76:79], v[20:23]\n" \
"v_mfma_f32_16x16x32_bf16 v[20:23], a[232:235], v[84:87], v[20:23]\n" \
"ds_read_b64_tr_b16 a[204:205], v220 offset:20480\n" \
"ds_read_b64_tr_b16 a[206:207], v220 offset:22528\n" \
"v_mfma_f32_16x16x32_bf16 v[20:23], a[236:239], v[92:95], v[20:23]\n" \
"v_mfma_f32_16x16x32_bf16 v[24:27], a[240:243], v[64:67], v[188:191]\n" \
"s_waitcnt lgkmcnt(9)\n" \
"ds_read_b64_tr_b16 a[208:209], v221 offset:0\n" \
"ds_read_b64_tr_b16 a[210:211], v221 offset:2048\n" \
"v_mfma_f32_16x16x32_bf16 v[24:27], a[244:247], v[72:75], v[24:27]\n" \
"v_mfma_f32_16x16x32_bf16 v[24:27], a[248:251], v[80:83], v[24:27]\n" \
"ds_read_b64_tr_b16 a[212:213], v221 offset:4096\n" \
"ds_read_b64_tr_b16 a[214:215], v221 offset:6144\n" \
"v_mfma_f32_16x16x32_bf16 v[24:27], a[252:255], v[88:91], v[24:27]\n" \
"v_mfma_f32_16x16x32_bf16 v[28:31], a[240:243], v[68:71], v[188:191]\n" \
"ds_read_b64_tr_b16 a[216:217], v221 offset:16384\n" \
"ds_read_b64_tr_b16 a[218:219], v221 offset:18432\n" \
"v_mfma_f32_16x16x32_bf16 v[28:31], a[244:247], v[76:79], v[28:31]\n" \
"v_mfma_f32_16x16x32_bf16 v[28:31], a[248:251], v[84:87], v[28:31]\n" \
"ds_read_b64_tr_b16 a[220:221], v221 offset:20480\n" \
"ds_read_b64_tr_b16 a[222:223], v221 offset:22528\n" \
"v_mfma_f32_16x16x32_bf16 v[28:31], a[252:255], v[92:95], v[28:31]\n" \
"s_waitcnt lgkmcnt(6)\n" \
"v_mfma_f32_16x16x32_bf16 v[0:3], a[192:195], v[96:99], v[0:3]\n" \
"ds_read_b64_tr_b16 a[224:225], v222 offset:0\n" \
"ds_read_b64_tr_b16 a[226:227], v222 offset:2048\n" \
"v_mfma_f32_16x16x32_bf16 v[0:3], a[196:199], v[104:107], v[0:3]\n" \
"v_mfma_f32_16x16x32_bf16 v[0:3], a[200:203], v[112:115], v[0:3]\n" \
"ds_read_b64_tr_b16 a[228:229], v222 offset:4096\n" \
"ds_read_b64_tr_b16 a[230:231], v222 offset:6144\n" \
"v_mfma_f32_16x16x32_bf16 v[0:3], a[204:207], v[120:123], v[0:3]\n" \
"v_mfma_f32_16x16x32_bf16 v[4:7], a[192:195], v[100:103], v[4:7]\n" \
"ds_read_b64_tr_b16 a[232:233], v222 offset:16384\n" \
"ds_read_b64_tr_b16 a[234:235], v222 offset:18432\n" \
"v_mfma_f32_16x16x32_bf16 v[4:7], a[196:199], v[108:111], v[4:7]\n" \
"v_mfma_f32_16x16x32_bf16 v[4:7], a[200:203], v[116:119], v[4:7]\n" \
"ds_read_b64_tr_b16 a[236:237], v222 offset:20480\n" \
"ds_read_b64_tr_b16 a[238:239], v222 offset:22528\n" \
"v_mfma_f32_16x16x32_bf16 v[4:7], a[204:207], v[124:127], v[4:7]\n" \
"v_mfma_f32_16x16x32_bf16 v[8:11], a[208:211], v[96:99], v[8:11]\n" \
"s_waitcnt lgkmcnt(6)\n" \
"ds_read_b64_tr_b16 a[240:241], v223 offset:0\n" \
"ds_read_b64_tr_b16 a[242:243], v223 offset:2048\n" \
"v_mfma_f32_16x16x32_bf16 v[8:11], a[212:215], v[104:107], v[8:11]\n" \
"v_mfma_f32_16x16x32_bf16 v[8:11], a[216:219], v[112:115], v[8:11]\n" \
"ds_read_b64_tr_b16 a[244:245], v223 offset:4096\n" \
"ds_read_b64_tr_b16 a[246:247], v223 offset:6144\n" \
"v_mfma_f32_16x16x32_bf16 v[8:11], a[220:223], v[120:123], v[8:11]\n" \
"v_mfma_f32_16x16x32_bf16 v[12:15], a[208:211], v[100:103], v[12:15]\n" \
"ds_read_b64_tr_b16 a[248:249], v223 offset:16384\n" \
"ds_read_b64_tr_b16 a[250:251], v223 offset:18432\n" \
"v_mfma_f32_16x16x32_bf16 v[12:15], a[212:215], v[108:111], v[12:15]\n" \
"v_mfma_f32_16x16x32_bf16 v[12:15], a[216:219], v[116:119], v[12:15]\n" \
"s_waitcnt vmcnt(4)\n" \
"ds_read_b64_tr_b16 a[252:253], v223 offset:20480\n" \
"ds_read_b64_tr_b16 a[254:255], v223 offset:22528\n" \
"v_mfma_f32_16x16x32_bf16 v[12:15], a[220:223], v[124:127], v[12:15]\n" \
"s_barrier\n" \
"ds_read_b128 v[128:131], v228 offset:32768\n" \
"ds_read_b128 v[136:139], v228 offset:36864\n" \
"v_mfma_f32_16x16x32_bf16 v[16:19], a[224:227], v[96:99], v[16:19]\n" \
"s_waitcnt lgkmcnt(8)\n" \
"ds_read_b64_tr_b16 a[192:193], v212 offset:0\n" \
"ds_read_b64_tr_b16 a[194:195], v212 offset:2048\n" \
"ds_read_b128 v[144:147], v228 offset:40960\n" \
"v_mfma_f32_16x16x32_bf16 v[16:19], a[228:231], v[104:107], v[16:19]\n" \
"s_mov_b32 s68, 1376256\n" \
"ds_read_b128 v[152:155], v228 offset:45056\n" \
"ds_read_b128 v[132:135], v228 offset:33792\n" \
"v_mfma_f32_16x16x32_bf16 v[16:19], a[232:235], v[112:115], v[16:19]\n" \
"s_add_u32 m0, s50, 32768\n" \
"ds_read_b64_tr_b16 a[196:197], v212 offset:4096\n" \
"ds_read_b64_tr_b16 a[198:199], v212 offset:6144\n" \
"ds_read_b128 v[140:143], v228 offset:37888\n" \
"v_mfma_f32_16x16x32_bf16 v[16:19], a[236:239], v[120:123], v[16:19]\n" \
"buffer_load_dwordx4 v224, s[56:59], s68 offen offset:0 lds\n" \
"v_mfma_f32_16x16x32_bf16 v[20:23], a[224:227], v[100:103], v[20:23]\n" \
"ds_read_b64_tr_b16 a[200:201], v212 offset:16384\n" \
"ds_read_b64_tr_b16 a[202:203], v212 offset:18432\n" \
"ds_read_b128 v[148:151], v228 offset:41984\n" \
"v_mfma_f32_16x16x32_bf16 v[20:23], a[228:231], v[108:111], v[20:23]\n" \
"buffer_load_dwordx4 v225, s[56:59], s68 offen offset:1024 lds\n" \
"v_mfma_f32_16x16x32_bf16 v[20:23], a[232:235], v[116:119], v[20:23]\n" \
"ds_read_b64_tr_b16 a[204:205], v212 offset:20480\n" \
"ds_read_b64_tr_b16 a[206:207], v212 offset:22528\n" \
"ds_read_b128 v[156:159], v228 offset:46080\n" \
"v_mfma_f32_16x16x32_bf16 v[20:23], a[236:239], v[124:127], v[20:23]\n" \
"buffer_load_dwordx4 v226, s[56:59], s68 offen offset:2048 lds\n" \
"v_mfma_f32_16x16x32_bf16 v[24:27], a[240:243], v[96:99], v[24:27]\n" \
"s_waitcnt lgkmcnt(9)\n" \
"ds_read_b64_tr_b16 a[208:209], v213 offset:0\n" \
"ds_read_b64_tr_b16 a[210:211], v213 offset:2048\n" \
"v_mfma_f32_16x16x32_bf16 v[24:27], a[244:247], v[104:107], v[24:27]\n" \
"s_mov_b32 s69, 128\n" \
"buffer_load_dwordx4 v227, s[56:59], s68 offen offset:3072 lds\n" \
"v_mfma_f32_16x16x32_bf16 v[24:27], a[248:251], v[112:115], v[24:27]\n" \
"s_add_u32 m0, s50, 49152\n" \
"ds_read_b64_tr_b16 a[212:213], v213 offset:4096\n" \
"ds_read_b64_tr_b16 a[214:215], v213 offset:6144\n" \
"v_mfma_f32_16x16x32_bf16 v[24:27], a[252:255], v[120:123], v[24:27]\n" \
"buffer_load_dwordx4 v224, s[56:59], s69 offen offset:0 lds\n" \
"v_mfma_f32_16x16x32_bf16 v[28:31], a[240:243], v[100:103], v[28:31]\n" \
"ds_read_b64_tr_b16 a[216:217], v213 offset:16384\n" \
"ds_read_b64_tr_b16 a[218:219], v213 offset:18432\n" \
"v_mfma_f32_16x16x32_bf16 v[28:31], a[244:247], v[108:111], v[28:31]\n" \
"buffer_load_dwordx4 v225, s[56:59], s69 offen offset:1024 lds\n" \
"v_mfma_f32_16x16x32_bf16 v[28:31], a[248:251], v[116:119], v[28:31]\n" \
"ds_read_b64_tr_b16 a[220:221], v213 offset:20480\n" \
"ds_read_b64_tr_b16 a[222:223], v213 offset:22528\n" \
"v_mfma_f32_16x16x32_bf16 v[28:31], a[252:255], v[124:127], v[28:31]\n" \
"s_waitcnt lgkmcnt(6)\n" \
"buffer_load_dwordx4 v226, s[56:59], s69 offen offset:2048 lds\n" \
"v_mfma_f32_16x16x32_bf16 v[0:3], a[192:195], v[128:131], v[0:3]\n" \
"ds_read_b64_tr_b16 a[224:225], v214 offset:0\n" \
"ds_read_b64_tr_b16 a[226:227], v214 offset:2048\n" \
"v_mfma_f32_16x16x32_bf16 v[0:3], a[196:199], v[136:139], v[0:3]\n" \
"s_mov_b32 s68, 229504\n" \
"buffer_load_dwordx4 v227, s[56:59], s69 offen offset:3072 lds\n" \
"v_mfma_f32_16x16x32_bf16 v[0:3], a[200:203], v[144:147], v[0:3]\n" \
"s_add_u32 m0, s50, 98304\n" \
"ds_read_b64_tr_b16 a[228:229], v214 offset:4096\n" \
"ds_read_b64_tr_b16 a[230:231], v214 offset:6144\n" \
"v_mfma_f32_16x16x32_bf16 v[0:3], a[204:207], v[152:155], v[0:3]\n" \
"buffer_load_dwordx4 v224, s[56:59], s68 offen offset:0 lds\n" \
"v_mfma_f32_16x16x32_bf16 v[4:7], a[192:195], v[132:135], v[4:7]\n" \
"ds_read_b64_tr_b16 a[232:233], v214 offset:16384\n" \
"ds_read_b64_tr_b16 a[234:235], v214 offset:18432\n" \
"v_mfma_f32_16x16x32_bf16 v[4:7], a[196:199], v[140:143], v[4:7]\n" \
"buffer_load_dwordx4 v225, s[56:59], s68 offen offset:1024 lds\n" \
"v_mfma_f32_16x16x32_bf16 v[4:7], a[200:203], v[148:151], v[4:7]\n" \
"ds_read_b64_tr_b16 a[236:237], v214 offset:20480\n" \
"ds_read_b64_tr_b16 a[238:239], v214 offset:22528\n" \
"v_mfma_f32_16x16x32_bf16 v[4:7], a[204:207], v[156:159], v[4:7]\n" \
"buffer_load_dwordx4 v226, s[56:59], s68 offen offset:2048 lds\n" \
"v_mfma_f32_16x16x32_bf16 v[8:11], a[208:211], v[128:131], v[8:11]\n" \
"s_waitcnt lgkmcnt(6)\n" \
"ds_read_b64_tr_b16 a[240:241], v215 offset:0\n" \
"ds_read_b64_tr_b16 a[242:243], v215 offset:2048\n" \
"v_mfma_f32_16x16x32_bf16 v[8:11], a[212:215], v[136:139], v[8:11]\n" \
"s_mov_b32 s69, 458880\n" \
"buffer_load_dwordx4 v227, s[56:59], s68 offen offset:3072 lds\n" \
"v_mfma_f32_16x16x32_bf16 v[8:11], a[216:219], v[144:147], v[8:11]\n" \
"s_add_u32 m0, s50, 114688\n" \
"ds_read_b64_tr_b16 a[244:245], v215 offset:4096\n" \
"ds_read_b64_tr_b16 a[246:247], v215 offset:6144\n" \
"v_mfma_f32_16x16x32_bf16 v[8:11], a[220:223], v[152:155], v[8:11]\n" \
"buffer_load_dwordx4 v224, s[56:59], s69 offen offset:0 lds\n" \
"v_mfma_f32_16x16x32_bf16 v[12:15], a[208:211], v[132:135], v[12:15]\n" \
"ds_read_b64_tr_b16 a[248:249], v215 offset:16384\n" \
"ds_read_b64_tr_b16 a[250:251], v215 offset:18432\n" \
"v_mfma_f32_16x16x32_bf16 v[12:15], a[212:215], v[140:143], v[12:15]\n" \
"buffer_load_dwordx4 v225, s[56:59], s69 offen offset:1024 lds\n" \
"v_mfma_f32_16x16x32_bf16 v[12:15], a[216:219], v[148:151], v[12:15]\n" \
"s_waitcnt vmcnt(6)\n" \
"ds_read_b64_tr_b16 a[252:253], v215 offset:20480\n" \
"ds_read_b64_tr_b16 a[254:255], v215 offset:22528\n" \
"v_mfma_f32_16x16x32_bf16 v[12:15], a[220:223], v[156:159], v[12:15]\n" \
"s_barrier\n" \
"buffer_load_dwordx4 v226, s[56:59], s69 offen offset:2048 lds\n" \
"v_mfma_f32_16x16x32_bf16 v[16:19], a[224:227], v[128:131], v[16:19]\n" \
"s_waitcnt lgkmcnt(6)\n" \
"ds_read_b64_tr_b16 a[192:193], v212 offset:32768\n" \
"ds_read_b64_tr_b16 a[194:195], v212 offset:34816\n" \
"ds_read_b128 v[160:163], v228 offset:49152\n" \
"v_mfma_f32_16x16x32_bf16 v[16:19], a[228:231], v[136:139], v[16:19]\n" \
"s_mov_b32 s68, 688256\n" \
"buffer_load_dwordx4 v227, s[56:59], s69 offen offset:3072 lds\n" \
"v_mfma_f32_16x16x32_bf16 v[16:19], a[232:235], v[144:147], v[16:19]\n" \
"s_add_u32 m0, s50, 131072\n" \
"ds_read_b64_tr_b16 a[196:197], v212 offset:36864\n" \
"ds_read_b64_tr_b16 a[198:199], v212 offset:38912\n" \
"ds_read_b128 v[168:171], v228 offset:53248\n" \
"v_mfma_f32_16x16x32_bf16 v[16:19], a[236:239], v[152:155], v[16:19]\n" \
"buffer_load_dwordx4 v224, s[56:59], s68 offen offset:0 lds\n" \
"v_mfma_f32_16x16x32_bf16 v[20:23], a[224:227], v[132:135], v[20:23]\n" \
"ds_read_b64_tr_b16 a[200:201], v213 offset:32768\n" \
"ds_read_b64_tr_b16 a[202:203], v213 offset:34816\n" \
"ds_read_b128 v[164:167], v228 offset:50176\n" \
"v_mfma_f32_16x16x32_bf16 v[20:23], a[228:231], v[140:143], v[20:23]\n" \
"ds_read_b128 v[172:175], v228 offset:54272\n" \
"v_mfma_f32_16x16x32_bf16 v[20:23], a[232:235], v[148:151], v[20:23]\n" \
"ds_read_b64_tr_b16 a[204:205], v213 offset:36864\n" \
"ds_read_b64_tr_b16 a[206:207], v213 offset:38912\n" \
"v_mfma_f32_16x16x32_bf16 v[20:23], a[236:239], v[156:159], v[20:23]\n" \
"buffer_load_dwordx4 v225, s[56:59], s68 offen offset:1024 lds\n" \
"v_mfma_f32_16x16x32_bf16 v[24:27], a[240:243], v[128:131], v[24:27]\n" \
"s_waitcnt lgkmcnt(9)\n" \
"ds_read_b64_tr_b16 a[208:209], v214 offset:32768\n" \
"ds_read_b64_tr_b16 a[210:211], v214 offset:34816\n" \
"v_mfma_f32_16x16x32_bf16 v[24:27], a[244:247], v[136:139], v[24:27]\n" \
"v_mfma_f32_16x16x32_bf16 v[24:27], a[248:251], v[144:147], v[24:27]\n" \
"ds_read_b64_tr_b16 a[212:213], v214 offset:36864\n" \
"ds_read_b64_tr_b16 a[214:215], v214 offset:38912\n" \
"v_mfma_f32_16x16x32_bf16 v[24:27], a[252:255], v[152:155], v[24:27]\n" \
"buffer_load_dwordx4 v226, s[56:59], s68 offen offset:2048 lds\n" \
"v_mfma_f32_16x16x32_bf16 v[28:31], a[240:243], v[132:135], v[28:31]\n" \
"ds_read_b64_tr_b16 a[216:217], v215 offset:32768\n" \
"ds_read_b64_tr_b16 a[218:219], v215 offset:34816\n" \
"v_mfma_f32_16x16x32_bf16 v[28:31], a[244:247], v[140:143], v[28:31]\n" \
"v_mfma_f32_16x16x32_bf16 v[28:31], a[248:251], v[148:151], v[28:31]\n" \
"ds_read_b64_tr_b16 a[220:221], v215 offset:36864\n" \
"ds_read_b64_tr_b16 a[222:223], v215 offset:38912\n" \
"v_mfma_f32_16x16x32_bf16 v[28:31], a[252:255], v[156:159], v[28:31]\n" \
"s_waitcnt lgkmcnt(6)\n" \
"buffer_load_dwordx4 v227, s[56:59], s68 offen offset:3072 lds\n" \
"v_mfma_f32_16x16x32_bf16 v[0:3], a[192:195], v[160:163], v[0:3]\n" \
"ds_read_b64_tr_b16 a[224:225], v212 offset:49152\n" \
"ds_read_b64_tr_b16 a[226:227], v212 offset:51200\n" \
"v_mfma_f32_16x16x32_bf16 v[0:3], a[196:199], v[168:171], v[0:3]\n" \
"s_mov_b32 s69, 917632\n" \
"v_mfma_f32_16x16x32_bf16 v[4:7], a[192:195], v[164:167], v[4:7]\n" \
"s_add_u32 m0, s50, 147456\n" \
"ds_read_b64_tr_b16 a[228:229], v212 offset:53248\n" \
"ds_read_b64_tr_b16 a[230:231], v212 offset:55296\n" \
"v_mfma_f32_16x16x32_bf16 v[4:7], a[196:199], v[172:175], v[4:7]\n" \
"buffer_load_dwordx4 v224, s[56:59], s69 offen offset:0 lds\n" \
"v_mfma_f32_16x16x32_bf16 v[8:11], a[200:203], v[160:163], v[8:11]\n" \
"ds_read_b64_tr_b16 a[232:233], v213 offset:49152\n" \
"ds_read_b64_tr_b16 a[234:235], v213 offset:51200\n" \
"v_mfma_f32_16x16x32_bf16 v[8:11], a[204:207], v[168:171], v[8:11]\n" \
"v_mfma_f32_16x16x32_bf16 v[12:15], a[200:203], v[164:167], v[12:15]\n" \
"ds_read_b64_tr_b16 a[236:237], v213 offset:53248\n" \
"ds_read_b64_tr_b16 a[238:239], v213 offset:55296\n" \
"v_mfma_f32_16x16x32_bf16 v[12:15], a[204:207], v[172:175], v[12:15]\n" \
"buffer_load_dwordx4 v225, s[56:59], s69 offen offset:1024 lds\n" \
"v_mfma_f32_16x16x32_bf16 v[16:19], a[208:211], v[160:163], v[16:19]\n" \
"s_waitcnt lgkmcnt(6)\n" \
"ds_read_b64_tr_b16 a[240:241], v214 offset:49152\n" \
"ds_read_b64_tr_b16 a[242:243], v214 offset:51200\n" \
"v_mfma_f32_16x16x32_bf16 v[16:19], a[212:215], v[168:171], v[16:19]\n" \
"v_mfma_f32_16x16x32_bf16 v[20:23], a[208:211], v[164:167], v[20:23]\n" \
"ds_read_b64_tr_b16 a[244:245], v214 offset:53248\n" \
"ds_read_b64_tr_b16 a[246:247], v214 offset:55296\n" \
"v_mfma_f32_16x16x32_bf16 v[20:23], a[212:215], v[172:175], v[20:23]\n" \
"buffer_load_dwordx4 v226, s[56:59], s69 offen offset:2048 lds\n" \
"v_mfma_f32_16x16x32_bf16 v[24:27], a[216:219], v[160:163], v[24:27]\n" \
"ds_read_b64_tr_b16 a[248:249], v215 offset:49152\n" \
"ds_read_b64_tr_b16 a[250:251], v215 offset:51200\n" \
"v_mfma_f32_16x16x32_bf16 v[24:27], a[220:223], v[168:171], v[24:27]\n" \
"v_mul_f32 v232, v0, v0\n" \
"v_mul_f32 v233, v1, v1\n" \
"v_mfma_f32_16x16x32_bf16 v[28:31], a[216:219], v[164:167], v[28:31]\n" \
"s_waitcnt vmcnt(7)\n" \
"ds_read_b64_tr_b16 a[252:253], v215 offset:53248\n" \
"ds_read_b64_tr_b16 a[254:255], v215 offset:55296\n" \
"v_mfma_f32_16x16x32_bf16 v[28:31], a[220:223], v[172:175], v[28:31]\n" \
"s_barrier\n" \
"buffer_load_dwordx4 v227, s[56:59], s69 offen offset:3072 lds\n" \
"v_mfma_f32_16x16x32_bf16 v[32:35], a[224:227], v[64:67], v[192:195]\n" \
"s_waitcnt lgkmcnt(6)\n" \
"ds_read_b64_tr_b16 a[192:193], v216 offset:32768\n" \
"ds_read_b64_tr_b16 a[194:195], v216 offset:34816\n" \
"v_mfma_f32_16x16x32_bf16 v[32:35], a[228:231], v[72:75], v[32:35]\n" \
"s_mov_b32 s68, 1147008\n" \
"v_mul_f32 v234, v2, v2\n" \
"v_mul_f32 v235, v3, v3\n" \
"v_mfma_f32_16x16x32_bf16 v[36:39], a[224:227], v[68:71], v[192:195]\n" \
"s_add_u32 m0, s50, 0\n" \
"ds_read_b64_tr_b16 a[196:197], v216 offset:36864\n" \
"ds_read_b64_tr_b16 a[198:199], v216 offset:38912\n" \
"v_mfma_f32_16x16x32_bf16 v[36:39], a[228:231], v[76:79], v[36:39]\n" \
"buffer_load_dwordx4 v224, s[56:59], s68 offen offset:0 lds\n" \
"v_mfma_f32_16x16x32_bf16 v[40:43], a[232:235], v[64:67], v[196:199]\n" \
"ds_read_b64_tr_b16 a[200:201], v216 offset:49152\n" \
"ds_read_b64_tr_b16 a[202:203], v216 offset:51200\n" \
"v_mfma_f32_16x16x32_bf16 v[40:43], a[236:239], v[72:75], v[40:43]\n" \
"buffer_load_dwordx4 v225, s[56:59], s68 offen offset:1024 lds\n" \
"v_mfma_f32_16x16x32_bf16 v[44:47], a[232:235], v[68:71], v[196:199]\n" \
"ds_read_b64_tr_b16 a[204:205], v216 offset:53248\n" \
"ds_read_b64_tr_b16 a[206:207], v216 offset:55296\n" \
"v_mfma_f32_16x16x32_bf16 v[44:47], a[236:239], v[76:79], v[44:47]\n" \
"buffer_load_dwordx4 v226, s[56:59], s68 offen offset:2048 lds\n" \
"v_mfma_f32_16x16x32_bf16 v[48:51], a[240:243], v[64:67], v[200:203]\n" \
"s_waitcnt lgkmcnt(6)\n" \
"ds_read_b64_tr_b16 a[208:209], v217 offset:32768\n" \
"ds_read_b64_tr_b16 a[210:211], v217 offset:34816\n" \
"v_mfma_f32_16x16x32_bf16 v[48:51], a[244:247], v[72:75], v[48:51]\n" \
"s_mov_b32 s69, 1376384\n" \
"buffer_load_dwordx4 v227, s[56:59], s68 offen offset:3072 lds\n" \
"v_mfma_f32_16x16x32_bf16 v[52:55], a[240:243], v[68:71], v[200:203]\n" \
"s_add_u32 m0, s50, 16384\n" \
"ds_read_b64_tr_b16 a[212:213], v217 offset:36864\n" \
"ds_read_b64_tr_b16 a[214:215], v217 offset:38912\n" \
"v_mfma_f32_16x16x32_bf16 v[52:55], a[244:247], v[76:79], v[52:55]\n" \
"buffer_load_dwordx4 v224, s[56:59], s69 offen offset:0 lds\n" \
"v_mfma_f32_16x16x32_bf16 v[56:59], a[248:251], v[64:67], v[204:207]\n" \
"ds_read_b64_tr_b16 a[216:217], v217 offset:49152\n" \
"ds_read_b64_tr_b16 a[218:219], v217 offset:51200\n" \
"v_mfma_f32_16x16x32_bf16 v[56:59], a[252:255], v[72:75], v[56:59]\n" \
"buffer_load_dwordx4 v225, s[56:59], s69 offen offset:1024 lds\n" \
"v_mfma_f32_16x16x32_bf16 v[60:63], a[248:251], v[68:71], v[204:207]\n" \
"ds_read_b64_tr_b16 a[220:221], v217 offset:53248\n" \
"ds_read_b64_tr_b16 a[222:223], v217 offset:55296\n" \
"v_mfma_f32_16x16x32_bf16 v[60:63], a[252:255], v[76:79], v[60:63]\n" \
"buffer_load_dwordx4 v226, s[56:59], s69 offen offset:2048 lds\n" \
"v_mfma_f32_16x16x32_bf16 v[32:35], a[192:195], v[80:83], v[32:35]\n" \
"s_waitcnt lgkmcnt(6)\n" \
"ds_read_b64_tr_b16 a[224:225], v218 offset:32768\n" \
"ds_read_b64_tr_b16 a[226:227], v218 offset:34816\n" \
"v_mfma_f32_16x16x32_bf16 v[32:35], a[196:199], v[88:91], v[32:35]\n" \
"s_mov_b32 s68, 0\n" \
"buffer_load_dwordx4 v227, s[56:59], s69 offen offset:3072 lds\n" \
"v_mfma_f32_16x16x32_bf16 v[32:35], a[200:203], v[96:99], v[32:35]\n" \
"s_add_u32 m0, s50, 65536\n" \
"ds_read_b64_tr_b16 a[228:229], v218 offset:36864\n" \
"ds_read_b64_tr_b16 a[230:231], v218 offset:38912\n" \
"v_mfma_f32_16x16x32_bf16 v[32:35], a[204:207], v[104:107], v[32:35]\n" \
"buffer_load_dwordx4 v248, s[60:63], s68 offen offset:0 lds\n" \
"v_mfma_f32_16x16x32_bf16 v[36:39], a[192:195], v[84:87], v[36:39]\n" \
"ds_read_b64_tr_b16 a[232:233], v218 offset:49152\n" \
"ds_read_b64_tr_b16 a[234:235], v218 offset:51200\n" \
"v_mfma_f32_16x16x32_bf16 v[36:39], a[196:199], v[92:95], v[36:39]\n" \
"buffer_load_dwordx4 v249, s[60:63], s68 offen offset:1024 lds\n" \
"v_mfma_f32_16x16x32_bf16 v[36:39], a[200:203], v[100:103], v[36:39]\n" \
"ds_read_b64_tr_b16 a[236:237], v218 offset:53248\n" \
"ds_read_b64_tr_b16 a[238:239], v218 offset:55296\n" \
"v_mfma_f32_16x16x32_bf16 v[36:39], a[204:207], v[108:111], v[36:39]\n" \
"buffer_load_dwordx4 v250, s[60:63], s68 offen offset:2048 lds\n" \
"v_mfma_f32_16x16x32_bf16 v[40:43], a[208:211], v[80:83], v[40:43]\n" \
"s_waitcnt lgkmcnt(6)\n" \
"ds_read_b64_tr_b16 a[240:241], v219 offset:32768\n" \
"ds_read_b64_tr_b16 a[242:243], v219 offset:34816\n" \
"v_mfma_f32_16x16x32_bf16 v[40:43], a[212:215], v[88:91], v[40:43]\n" \
"s_mov_b32 s69, 114688\n" \
"buffer_load_dwordx4 v251, s[60:63], s68 offen offset:3072 lds\n" \
"v_mfma_f32_16x16x32_bf16 v[40:43], a[216:219], v[96:99], v[40:43]\n" \
"s_add_u32 m0, s50, 81920\n" \
"ds_read_b64_tr_b16 a[244:245], v219 offset:36864\n" \
"ds_read_b64_tr_b16 a[246:247], v219 offset:38912\n" \
"v_mfma_f32_16x16x32_bf16 v[40:43], a[220:223], v[104:107], v[40:43]\n" \
"buffer_load_dwordx4 v248, s[60:63], s69 offen offset:0 lds\n" \
"v_mfma_f32_16x16x32_bf16 v[44:47], a[208:211], v[84:87], v[44:47]\n" \
"ds_read_b64_tr_b16 a[248:249], v219 offset:49152\n" \
"ds_read_b64_tr_b16 a[250:251], v219 offset:51200\n" \
"v_mfma_f32_16x16x32_bf16 v[44:47], a[212:215], v[92:95], v[44:47]\n" \
"buffer_load_dwordx4 v249, s[60:63], s69 offen offset:1024 lds\n" \
"v_mfma_f32_16x16x32_bf16 v[44:47], a[216:219], v[100:103], v[44:47]\n" \
"s_waitcnt vmcnt(14)\n" \
"ds_read_b64_tr_b16 a[252:253], v219 offset:53248\n" \
"ds_read_b64_tr_b16 a[254:255], v219 offset:55296\n" \
"v_mfma_f32_16x16x32_bf16 v[44:47], a[220:223], v[108:111], v[44:47]\n" \
"s_barrier\n" \
"buffer_load_dwordx4 v250, s[60:63], s69 offen offset:2048 lds\n" \
"v_mfma_f32_16x16x32_bf16 v[48:51], a[224:227], v[80:83], v[48:51]\n" \
"s_waitcnt lgkmcnt(6)\n" \
"ds_read_b64_tr_b16 a[192:193], v220 offset:0\n" \
"ds_read_b64_tr_b16 a[194:195], v220 offset:2048\n" \
"v_mfma_f32_16x16x32_bf16 v[48:51], a[228:231], v[88:91], v[48:51]\n" \
"s_mov_b32 s68, 128\n" \
"buffer_load_dwordx4 v251, s[60:63], s69 offen offset:3072 lds\n" \
"v_mfma_f32_16x16x32_bf16 v[48:51], a[232:235], v[96:99], v[48:51]\n" \
"s_add_u32 m0, s50, 32768\n" \
"ds_read_b64_tr_b16 a[196:197], v220 offset:4096\n" \
"ds_read_b64_tr_b16 a[198:199], v220 offset:6144\n" \
"v_mfma_f32_16x16x32_bf16 v[48:51], a[236:239], v[104:107], v[48:51]\n" \
"buffer_load_dwordx4 v248, s[60:63], s68 offen offset:0 lds\n" \
"v_mfma_f32_16x16x32_bf16 v[52:55], a[224:227], v[84:87], v[52:55]\n" \
"ds_read_b64_tr_b16 a[200:201], v220 offset:16384\n" \
"ds_read_b64_tr_b16 a[202:203], v220 offset:18432\n" \
"v_mfma_f32_16x16x32_bf16 v[52:55], a[228:231], v[92:95], v[52:55]\n" \
"v_mul_f32 v236, v8, v8\n" \
"v_mul_f32 v237, v9, v9\n" \
"v_mfma_f32_16x16x32_bf16 v[52:55], a[232:235], v[100:103], v[52:55]\n" \
"ds_read_b64_tr_b16 a[204:205], v220 offset:20480\n" \
"ds_read_b64_tr_b16 a[206:207], v220 offset:22528\n" \
"v_mfma_f32_16x16x32_bf16 v[52:55], a[236:239], v[108:111], v[52:55]\n" \
"buffer_load_dwordx4 v249, s[60:63], s68 offen offset:1024 lds\n" \
"v_mfma_f32_16x16x32_bf16 v[56:59], a[240:243], v[80:83], v[56:59]\n" \
"s_waitcnt lgkmcnt(6)\n" \
"ds_read_b64_tr_b16 a[208:209], v221 offset:0\n" \
"ds_read_b64_tr_b16 a[210:211], v221 offset:2048\n" \
"v_mfma_f32_16x16x32_bf16 v[56:59], a[244:247], v[88:91], v[56:59]\n" \
"v_mul_f32 v238, v10, v10\n" \
"v_mul_f32 v239, v11, v11\n" \
"v_mfma_f32_16x16x32_bf16 v[56:59], a[248:251], v[96:99], v[56:59]\n" \
"ds_read_b64_tr_b16 a[212:213], v221 offset:4096\n" \
"ds_read_b64_tr_b16 a[214:215], v221 offset:6144\n" \
"v_mfma_f32_16x16x32_bf16 v[56:59], a[252:255], v[104:107], v[56:59]\n" \
"buffer_load_dwordx4 v250, s[60:63], s68 offen offset:2048 lds\n" \
"v_mfma_f32_16x16x32_bf16 v[60:63], a[240:243], v[84:87], v[60:63]\n" \
"ds_read_b64_tr_b16 a[216:217], v221 offset:16384\n" \
"ds_read_b64_tr_b16 a[218:219], v221 offset:18432\n" \
"v_mfma_f32_16x16x32_bf16 v[60:63], a[244:247], v[92:95], v[60:63]\n" \
"v_fma_f32 v232, v232, v208, v209\n" \
"v_fma_f32 v233, v233, v208, v209\n" \
"v_mfma_f32_16x16x32_bf16 v[60:63], a[248:251], v[100:103], v[60:63]\n" \
"ds_read_b64_tr_b16 a[220:221], v221 offset:20480\n" \
"ds_read_b64_tr_b16 a[222:223], v221 offset:22528\n" \
"v_mfma_f32_16x16x32_bf16 v[60:63], a[252:255], v[108:111], v[60:63]\n" \
"buffer_load_dwordx4 v251, s[60:63], s68 offen offset:3072 lds\n" \
"v_mfma_f32_16x16x32_bf16 v[32:35], a[192:195], v[112:115], v[32:35]\n" \
"s_waitcnt lgkmcnt(6)\n" \
"ds_read_b64_tr_b16 a[224:225], v222 offset:0\n" \
"ds_read_b64_tr_b16 a[226:227], v222 offset:2048\n" \
"v_mfma_f32_16x16x32_bf16 v[32:35], a[196:199], v[120:123], v[32:35]\n" \
"s_mov_b32 s69, 114816\n" \
"v_fma_f32 v234, v234, v208, v209\n" \
"v_fma_f32 v235, v235, v208, v209\n" \
"v_mfma_f32_16x16x32_bf16 v[32:35], a[200:203], v[128:131], v[32:35]\n" \
"s_add_u32 m0, s50, 49152\n" \
"ds_read_b64_tr_b16 a[228:229], v222 offset:4096\n" \
"ds_read_b64_tr_b16 a[230:231], v222 offset:6144\n" \
"v_mfma_f32_16x16x32_bf16 v[32:35], a[204:207], v[136:139], v[32:35]\n" \
"buffer_load_dwordx4 v248, s[60:63], s69 offen offset:0 lds\n" \
"v_mfma_f32_16x16x32_bf16 v[36:39], a[192:195], v[116:119], v[36:39]\n" \
"ds_read_b64_tr_b16 a[232:233], v222 offset:16384\n" \
"ds_read_b64_tr_b16 a[234:235], v222 offset:18432\n" \
"v_mfma_f32_16x16x32_bf16 v[36:39], a[196:199], v[124:127], v[36:39]\n" \
"v_fma_f32 v236, v236, v208, v209\n" \
"v_fma_f32 v237, v237, v208, v209\n" \
"v_mfma_f32_16x16x32_bf16 v[36:39], a[200:203], v[132:135], v[36:39]\n" \
"ds_read_b64_tr_b16 a[236:237], v222 offset:20480\n" \
"ds_read_b64_tr_b16 a[238:239], v222 offset:22528\n" \
"v_mfma_f32_16x16x32_bf16 v[36:39], a[204:207], v[140:143], v[36:39]\n" \
"buffer_load_dwordx4 v249, s[60:63], s69 offen offset:1024 lds\n" \
"v_mfma_f32_16x16x32_bf16 v[40:43], a[208:211], v[112:115], v[40:43]\n" \
"s_waitcnt lgkmcnt(6)\n" \
"ds_read_b64_tr_b16 a[240:241], v223 offset:0\n" \
"ds_read_b64_tr_b16 a[242:243], v223 offset:2048\n" \
"v_mfma_f32_16x16x32_bf16 v[40:43], a[212:215], v[120:123], v[40:43]\n" \
"v_fma_f32 v238, v238, v208, v209\n" \
"v_fma_f32 v239, v239, v208, v209\n" \
"v_mfma_f32_16x16x32_bf16 v[40:43], a[216:219], v[128:131], v[40:43]\n" \
"ds_read_b64_tr_b16 a[244:245], v223 offset:4096\n" \
"ds_read_b64_tr_b16 a[246:247], v223 offset:6144\n" \
"v_mfma_f32_16x16x32_bf16 v[40:43], a[220:223], v[136:139], v[40:43]\n" \
"buffer_load_dwordx4 v250, s[60:63], s69 offen offset:2048 lds\n" \
"v_mfma_f32_16x16x32_bf16 v[44:47], a[208:211], v[116:119], v[44:47]\n" \
"ds_read_b64_tr_b16 a[248:249], v223 offset:16384\n" \
"ds_read_b64_tr_b16 a[250:251], v223 offset:18432\n" \
"v_mfma_f32_16x16x32_bf16 v[44:47], a[212:215], v[124:127], v[44:47]\n" \
"v_mul_f32 v232, v232, v0\n" \
"v_mul_f32 v233, v233, v1\n" \
"v_mfma_f32_16x16x32_bf16 v[44:47], a[216:219], v[132:135], v[44:47]\n" \
"s_waitcnt vmcnt(15)\n" \
"ds_read_b64_tr_b16 a[252:253], v223 offset:20480\n" \
"ds_read_b64_tr_b16 a[254:255], v223 offset:22528\n" \
"v_mfma_f32_16x16x32_bf16 v[44:47], a[220:223], v[140:143], v[44:47]\n" \
"s_barrier\n" \
"buffer_load_dwordx4 v251, s[60:63], s69 offen offset:3072 lds\n" \
"v_mfma_f32_16x16x32_bf16 v[48:51], a[224:227], v[112:115], v[48:51]\n" \
"s_waitcnt lgkmcnt(6)\n" \
"ds_read_b64_tr_b16 a[192:193], v212 offset:0\n" \
"ds_read_b64_tr_b16 a[194:195], v212 offset:2048\n" \
"v_mfma_f32_16x16x32_bf16 v[48:51], a[228:231], v[120:123], v[48:51]\n" \
"s_mov_b32 s68, 256\n" \
"v_mul_f32 v234, v234, v2\n" \
"v_mul_f32 v235, v235, v3\n" \
"v_mfma_f32_16x16x32_bf16 v[48:51], a[232:235], v[128:131], v[48:51]\n" \
"s_add_u32 m0, s50, 98304\n" \
"ds_read_b64_tr_b16 a[196:197], v212 offset:4096\n" \
"ds_read_b64_tr_b16 a[198:199], v212 offset:6144\n" \
"v_mfma_f32_16x16x32_bf16 v[48:51], a[236:239], v[136:139], v[48:51]\n" \
"buffer_load_dwordx4 v248, s[60:63], s68 offen offset:0 lds\n" \
"v_mfma_f32_16x16x32_bf16 v[52:55], a[224:227], v[116:119], v[52:55]\n" \
"ds_read_b64_tr_b16 a[200:201], v212 offset:16384\n" \
"ds_read_b64_tr_b16 a[202:203], v212 offset:18432\n" \
"v_mfma_f32_16x16x32_bf16 v[52:55], a[228:231], v[124:127], v[52:55]\n" \
"v_mul_f32 v236, v236, v8\n" \
"v_mul_f32 v237, v237, v9\n" \
"v_mfma_f32_16x16x32_bf16 v[52:55], a[232:235], v[132:135], v[52:55]\n" \
"ds_read_b64_tr_b16 a[204:205], v212 offset:20480\n" \
"ds_read_b64_tr_b16 a[206:207], v212 offset:22528\n" \
"v_mfma_f32_16x16x32_bf16 v[52:55], a[236:239], v[140:143], v[52:55]\n" \
"buffer_load_dwordx4 v249, s[60:63], s68 offen offset:1024 lds\n" \
"v_mfma_f32_16x16x32_bf16 v[56:59], a[240:243], v[112:115], v[56:59]\n" \
"s_waitcnt lgkmcnt(6)\n" \
"ds_read_b64_tr_b16 a[208:209], v213 offset:0\n" \
"ds_read_b64_tr_b16 a[210:211], v213 offset:2048\n" \
"v_mfma_f32_16x16x32_bf16 v[56:59], a[244:247], v[120:123], v[56:59]\n" \
"v_mul_f32 v238, v238, v10\n" \
"v_mul_f32 v239, v239, v11\n" \
"v_mfma_f32_16x16x32_bf16 v[56:59], a[248:251], v[128:131], v[56:59]\n" \
"ds_read_b64_tr_b16 a[212:213], v213 offset:4096\n" \
"ds_read_b64_tr_b16 a[214:215], v213 offset:6144\n" \
"v_mfma_f32_16x16x32_bf16 v[56:59], a[252:255], v[136:139], v[56:59]\n" \
"buffer_load_dwordx4 v250, s[60:63], s68 offen offset:2048 lds\n" \
"v_mfma_f32_16x16x32_bf16 v[60:63], a[240:243], v[116:119], v[60:63]\n" \
"ds_read_b64_tr_b16 a[216:217], v213 offset:16384\n" \
"ds_read_b64_tr_b16 a[218:219], v213 offset:18432\n" \
"v_mfma_f32_16x16x32_bf16 v[60:63], a[244:247], v[124:127], v[60:63]\n" \
"v_exp_f32 v232, v232\n" \
"v_mfma_f32_16x16x32_bf16 v[60:63], a[248:251], v[132:135], v[60:63]\n" \
"ds_read_b64_tr_b16 a[220:221], v213 offset:20480\n" \
"ds_read_b64_tr_b16 a[222:223], v213 offset:22528\n" \
"v_mfma_f32_16x16x32_bf16 v[60:63], a[252:255], v[140:143], v[60:63]\n" \
"buffer_load_dwordx4 v251, s[60:63], s68 offen offset:3072 lds\n" \
"v_mfma_f32_16x16x32_bf16 v[32:35], a[192:195], v[144:147], v[32:35]\n" \
"s_waitcnt lgkmcnt(6)\n" \
"ds_read_b64_tr_b16 a[224:225], v214 offset:0\n" \
"ds_read_b64_tr_b16 a[226:227], v214 offset:2048\n" \
"v_mfma_f32_16x16x32_bf16 v[32:35], a[196:199], v[152:155], v[32:35]\n" \
"s_mov_b32 s69, 114944\n" \
"v_exp_f32 v233, v233\n" \
"v_mfma_f32_16x16x32_bf16 v[32:35], a[200:203], v[160:163], v[32:35]\n" \
"s_add_u32 m0, s50, 114688\n" \
"ds_read_b64_tr_b16 a[228:229], v214 offset:4096\n" \
"ds_read_b64_tr_b16 a[230:231], v214 offset:6144\n" \
"v_mfma_f32_16x16x32_bf16 v[32:35], a[204:207], v[168:171], v[32:35]\n" \
"buffer_load_dwordx4 v248, s[60:63], s69 offen offset:0 lds\n" \
"v_mfma_f32_16x16x32_bf16 v[36:39], a[192:195], v[148:151], v[36:39]\n" \
"ds_read_b64_tr_b16 a[232:233], v214 offset:16384\n" \
"ds_read_b64_tr_b16 a[234:235], v214 offset:18432\n" \
"v_mfma_f32_16x16x32_bf16 v[36:39], a[196:199], v[156:159], v[36:39]\n" \
"v_exp_f32 v234, v234\n" \
"v_mfma_f32_16x16x32_bf16 v[36:39], a[200:203], v[164:167], v[36:39]\n" \
"ds_read_b64_tr_b16 a[236:237], v214 offset:20480\n" \
"ds_read_b64_tr_b16 a[238:239], v214 offset:22528\n" \
"v_mfma_f32_16x16x32_bf16 v[36:39], a[204:207], v[172:175], v[36:39]\n" \
"buffer_load_dwordx4 v249, s[60:63], s69 offen offset:1024 lds\n" \
"v_mfma_f32_16x16x32_bf16 v[40:43], a[208:211], v[144:147], v[40:43]\n" \
"s_waitcnt lgkmcnt(6)\n" \
"ds_read_b64_tr_b16 a[240:241], v215 offset:0\n" \
"ds_read_b64_tr_b16 a[242:243], v215 offset:2048\n" \
"v_mfma_f32_16x16x32_bf16 v[40:43], a[212:215], v[152:155], v[40:43]\n" \
"v_exp_f32 v235, v235\n" \
"v_mfma_f32_16x16x32_bf16 v[40:43], a[216:219], v[160:163], v[40:43]\n" \
"ds_read_b64_tr_b16 a[244:245], v215 offset:4096\n" \
"ds_read_b64_tr_b16 a[246:247], v215 offset:6144\n" \
"v_mfma_f32_16x16x32_bf16 v[40:43], a[220:223], v[168:171], v[40:43]\n" \
"buffer_load_dwordx4 v250, s[60:63], s69 offen offset:2048 lds\n" \
"v_mfma_f32_16x16x32_bf16 v[44:47], a[208:211], v[148:151], v[44:47]\n" \
"ds_read_b64_tr_b16 a[248:249], v215 offset:16384\n" \
"ds_read_b64_tr_b16 a[250:251], v215 offset:18432\n" \
"v_mfma_f32_16x16x32_bf16 v[44:47], a[212:215], v[156:159], v[44:47]\n" \
"v_exp_f32 v236, v236\n" \
"v_mfma_f32_16x16x32_bf16 v[44:47], a[216:219], v[164:167], v[44:47]\n" \
"ds_read_b64_tr_b16 a[252:253], v215 offset:20480\n" \
"ds_read_b64_tr_b16 a[254:255], v215 offset:22528\n" \
"v_mfma_f32_16x16x32_bf16 v[44:47], a[220:223], v[172:175], v[44:47]\n" \
"buffer_load_dwordx4 v251, s[60:63], s69 offen offset:3072 lds\n" \
"v_mfma_f32_16x16x32_bf16 v[48:51], a[224:227], v[144:147], v[48:51]\n" \
"s_waitcnt lgkmcnt(6)\n" \
"v_exp_f32 v237, v237\n" \
"v_mfma_f32_16x16x32_bf16 v[48:51], a[228:231], v[152:155], v[48:51]\n" \
"v_exp_f32 v238, v238\n" \
"v_mfma_f32_16x16x32_bf16 v[48:51], a[232:235], v[160:163], v[48:51]\n" \
"v_exp_f32 v239, v239\n" \
"v_mfma_f32_16x16x32_bf16 v[48:51], a[236:239], v[168:171], v[48:51]\n" \
"v_add_f32 v232, 1.0, v232\n" \
"v_add_f32 v233, 1.0, v233\n" \
"v_mfma_f32_16x16x32_bf16 v[52:55], a[224:227], v[148:151], v[52:55]\n" \
"v_add_f32 v234, 1.0, v234\n" \
"v_add_f32 v235, 1.0, v235\n" \
"v_mfma_f32_16x16x32_bf16 v[52:55], a[228:231], v[156:159], v[52:55]\n" \
"v_add_f32 v236, 1.0, v236\n" \
"v_add_f32 v237, 1.0, v237\n" \
"v_mfma_f32_16x16x32_bf16 v[52:55], a[232:235], v[164:167], v[52:55]\n" \
"v_add_f32 v238, 1.0, v238\n" \
"v_add_f32 v239, 1.0, v239\n" \
"v_mfma_f32_16x16x32_bf16 v[52:55], a[236:239], v[172:175], v[52:55]\n" \
"v_rcp_f32 v232, v232\n" \
"v_mfma_f32_16x16x32_bf16 v[56:59], a[240:243], v[144:147], v[56:59]\n" \
"s_waitcnt lgkmcnt(0)\n" \
"v_rcp_f32 v233, v233\n" \
"v_mfma_f32_16x16x32_bf16 v[56:59], a[244:247], v[152:155], v[56:59]\n" \
"v_rcp_f32 v234, v234\n" \
"v_mfma_f32_16x16x32_bf16 v[56:59], a[248:251], v[160:163], v[56:59]\n" \
"v_rcp_f32 v235, v235\n" \
"v_mfma_f32_16x16x32_bf16 v[56:59], a[252:255], v[168:171], v[56:59]\n" \
"v_rcp_f32 v236, v236\n" \
"v_mfma_f32_16x16x32_bf16 v[60:63], a[240:243], v[148:151], v[60:63]\n" \
"v_rcp_f32 v237, v237\n" \
"v_mfma_f32_16x16x32_bf16 v[60:63], a[244:247], v[156:159], v[60:63]\n" \
"v_rcp_f32 v238, v238\n" \
"v_mfma_f32_16x16x32_bf16 v[60:63], a[248:251], v[164:167], v[60:63]\n" \
"v_rcp_f32 v239, v239\n" \
"v_mfma_f32_16x16x32_bf16 v[60:63], a[252:255], v[172:175], v[60:63]\n" \
"v_mul_f32 v232, v232, v0\n" \
"v_mul_f32 v233, v233, v1\n" \
"s_barrier\n" \
"s_add_u32 m0, s50, 0\n" \
"s_mov_b32 s68, 384\n" \
"s_nop 0\n" \
"buffer_load_dwordx4 v248, s[60:63], s68 offen offset:0 lds\n" \
"buffer_load_dwordx4 v249, s[60:63], s68 offen offset:1024 lds\n" \
"buffer_load_dwordx4 v250, s[60:63], s68 offen offset:2048 lds\n" \
"buffer_load_dwordx4 v251, s[60:63], s68 offen offset:3072 lds\n" \
"s_add_u32 m0, s50, 16384\n" \
"s_mov_b32 s69, 115072\n" \
"s_nop 0\n" \
"buffer_load_dwordx4 v248, s[60:63], s69 offen offset:0 lds\n" \
"buffer_load_dwordx4 v249, s[60:63], s69 offen offset:1024 lds\n" \
"buffer_load_dwordx4 v250, s[60:63], s69 offen offset:2048 lds\n" \
"buffer_load_dwordx4 v251, s[60:63], s69 offen offset:3072 lds\n" \
"v_mul_f32 v234, v234, v2\n" \
"v_mul_f32 v235, v235, v3\n" \
"v_mul_f32 v236, v236, v8\n" \
"v_mul_f32 v237, v237, v9\n" \
"v_mul_f32 v238, v238, v10\n" \
"v_mul_f32 v239, v239, v11\n" \
"v_cvt_pk_bf16_f32 v0, v232, v233\n" \
"v_cvt_pk_bf16_f32 v1, v234, v235\n" \
"v_cvt_pk_bf16_f32 v2, v236, v237\n" \
"v_cvt_pk_bf16_f32 v3, v238, v239\n" \
"v_mul_f32 v232, v4, v4\n" \
"v_mul_f32 v233, v5, v5\n" \
"v_mul_f32 v234, v6, v6\n" \
"v_mul_f32 v235, v7, v7\n" \
"v_mul_f32 v236, v12, v12\n" \
"v_mul_f32 v237, v13, v13\n" \
"v_mul_f32 v238, v14, v14\n" \
"v_mul_f32 v239, v15, v15\n" \
"v_fma_f32 v232, v232, v208, v209\n" \
"v_fma_f32 v233, v233, v208, v209\n" \
"v_fma_f32 v234, v234, v208, v209\n" \
"v_fma_f32 v235, v235, v208, v209\n" \
"v_fma_f32 v236, v236, v208, v209\n" \
"v_fma_f32 v237, v237, v208, v209\n" \
"v_fma_f32 v238, v238, v208, v209\n" \
"v_fma_f32 v239, v239, v208, v209\n" \
"v_mul_f32 v232, v232, v4\n" \
"v_mul_f32 v233, v233, v5\n" \
"v_mul_f32 v234, v234, v6\n" \
"v_mul_f32 v235, v235, v7\n" \
"v_mul_f32 v236, v236, v12\n" \
"v_mul_f32 v237, v237, v13\n" \
"v_mul_f32 v238, v238, v14\n" \
"v_mul_f32 v239, v239, v15\n" \
"v_exp_f32 v232, v232\n" \
"v_exp_f32 v233, v233\n" \
"v_exp_f32 v234, v234\n" \
"v_exp_f32 v235, v235\n" \
"v_exp_f32 v236, v236\n" \
"v_exp_f32 v237, v237\n" \
"v_exp_f32 v238, v238\n" \
"v_exp_f32 v239, v239\n" \
"v_add_f32 v232, 1.0, v232\n" \
"v_add_f32 v233, 1.0, v233\n" \
"v_add_f32 v234, 1.0, v234\n" \
"v_add_f32 v235, 1.0, v235\n" \
"v_add_f32 v236, 1.0, v236\n" \
"v_add_f32 v237, 1.0, v237\n" \
"v_add_f32 v238, 1.0, v238\n" \
"v_add_f32 v239, 1.0, v239\n" \
"v_rcp_f32 v232, v232\n" \
"v_rcp_f32 v233, v233\n" \
"v_rcp_f32 v234, v234\n" \
"v_rcp_f32 v235, v235\n" \
"v_rcp_f32 v236, v236\n" \
"v_rcp_f32 v237, v237\n" \
"v_rcp_f32 v238, v238\n" \
"v_rcp_f32 v239, v239\n" \
"v_mul_f32 v232, v232, v4\n" \
"v_mul_f32 v233, v233, v5\n" \
"v_mul_f32 v234, v234, v6\n" \
"v_mul_f32 v235, v235, v7\n" \
"v_mul_f32 v236, v236, v12\n" \
"v_mul_f32 v237, v237, v13\n" \
"v_mul_f32 v238, v238, v14\n" \
"v_mul_f32 v239, v239, v15\n" \
"v_cvt_pk_bf16_f32 v4, v232, v233\n" \
"v_cvt_pk_bf16_f32 v5, v234, v235\n" \
"v_cvt_pk_bf16_f32 v6, v236, v237\n" \
"v_cvt_pk_bf16_f32 v7, v238, v239\n" \
"v_mul_f32 v232, v16, v16\n" \
"v_mul_f32 v233, v17, v17\n" \
"v_mul_f32 v234, v18, v18\n" \
"v_mul_f32 v235, v19, v19\n" \
"v_mul_f32 v236, v24, v24\n" \
"v_mul_f32 v237, v25, v25\n" \
"v_mul_f32 v238, v26, v26\n" \
"v_mul_f32 v239, v27, v27\n" \
"v_fma_f32 v232, v232, v208, v209\n" \
"v_fma_f32 v233, v233, v208, v209\n" \
"v_fma_f32 v234, v234, v208, v209\n" \
"v_fma_f32 v235, v235, v208, v209\n" \
"v_fma_f32 v236, v236, v208, v209\n" \
"v_fma_f32 v237, v237, v208, v209\n" \
"v_fma_f32 v238, v238, v208, v209\n" \
"v_fma_f32 v239, v239, v208, v209\n" \
"v_mul_f32 v232, v232, v16\n" \
"v_mul_f32 v233, v233, v17\n" \
"v_mul_f32 v234, v234, v18\n" \
"v_mul_f32 v235, v235, v19\n" \
"v_mul_f32 v236, v236, v24\n" \
"v_mul_f32 v237, v237, v25\n" \
"v_mul_f32 v238, v238, v26\n" \
"v_mul_f32 v239, v239, v27\n" \
"v_exp_f32 v232, v232\n" \
"v_exp_f32 v233, v233\n" \
"v_exp_f32 v234, v234\n" \
"v_exp_f32 v235, v235\n" \
"v_exp_f32 v236, v236\n" \
"v_exp_f32 v237, v237\n" \
"v_exp_f32 v238, v238\n" \
"v_exp_f32 v239, v239\n" \
"v_add_f32 v232, 1.0, v232\n" \
"v_add_f32 v233, 1.0, v233\n" \
"v_add_f32 v234, 1.0, v234\n" \
"v_add_f32 v235, 1.0, v235\n" \
"v_add_f32 v236, 1.0, v236\n" \
"v_add_f32 v237, 1.0, v237\n" \
"v_add_f32 v238, 1.0, v238\n" \
"v_add_f32 v239, 1.0, v239\n" \
"v_rcp_f32 v232, v232\n" \
"v_rcp_f32 v233, v233\n" \
"v_rcp_f32 v234, v234\n" \
"v_rcp_f32 v235, v235\n" \
"v_rcp_f32 v236, v236\n" \
"v_rcp_f32 v237, v237\n" \
"v_rcp_f32 v238, v238\n" \
"v_rcp_f32 v239, v239\n" \
"v_mul_f32 v232, v232, v16\n" \
"v_mul_f32 v233, v233, v17\n" \
"v_mul_f32 v234, v234, v18\n" \
"v_mul_f32 v235, v235, v19\n" \
"v_mul_f32 v236, v236, v24\n" \
"v_mul_f32 v237, v237, v25\n" \
"v_mul_f32 v238, v238, v26\n" \
"v_mul_f32 v239, v239, v27\n" \
"v_cvt_pk_bf16_f32 v16, v232, v233\n" \
"v_cvt_pk_bf16_f32 v17, v234, v235\n" \
"v_cvt_pk_bf16_f32 v18, v236, v237\n" \
"v_cvt_pk_bf16_f32 v19, v238, v239\n" \
"v_mul_f32 v232, v20, v20\n" \
"v_mul_f32 v233, v21, v21\n" \
"v_mul_f32 v234, v22, v22\n" \
"v_mul_f32 v235, v23, v23\n" \
"v_mul_f32 v236, v28, v28\n" \
"v_mul_f32 v237, v29, v29\n" \
"v_mul_f32 v238, v30, v30\n" \
"v_mul_f32 v239, v31, v31\n" \
"v_fma_f32 v232, v232, v208, v209\n" \
"v_fma_f32 v233, v233, v208, v209\n" \
"v_fma_f32 v234, v234, v208, v209\n" \
"v_fma_f32 v235, v235, v208, v209\n" \
"v_fma_f32 v236, v236, v208, v209\n" \
"v_fma_f32 v237, v237, v208, v209\n" \
"v_fma_f32 v238, v238, v208, v209\n" \
"v_fma_f32 v239, v239, v208, v209\n" \
"v_mul_f32 v232, v232, v20\n" \
"v_mul_f32 v233, v233, v21\n" \
"v_mul_f32 v234, v234, v22\n" \
"v_mul_f32 v235, v235, v23\n" \
"v_mul_f32 v236, v236, v28\n" \
"v_mul_f32 v237, v237, v29\n" \
"v_mul_f32 v238, v238, v30\n" \
"v_mul_f32 v239, v239, v31\n" \
"v_exp_f32 v232, v232\n" \
"v_exp_f32 v233, v233\n" \
"v_exp_f32 v234, v234\n" \
"v_exp_f32 v235, v235\n" \
"v_exp_f32 v236, v236\n" \
"v_exp_f32 v237, v237\n" \
"v_exp_f32 v238, v238\n" \
"v_exp_f32 v239, v239\n" \
"v_add_f32 v232, 1.0, v232\n" \
"v_add_f32 v233, 1.0, v233\n" \
"v_add_f32 v234, 1.0, v234\n" \
"v_add_f32 v235, 1.0, v235\n" \
"v_add_f32 v236, 1.0, v236\n" \
"v_add_f32 v237, 1.0, v237\n" \
"v_add_f32 v238, 1.0, v238\n" \
"v_add_f32 v239, 1.0, v239\n" \
"v_rcp_f32 v232, v232\n" \
"v_rcp_f32 v233, v233\n" \
"v_rcp_f32 v234, v234\n" \
"v_rcp_f32 v235, v235\n" \
"v_rcp_f32 v236, v236\n" \
"v_rcp_f32 v237, v237\n" \
"v_rcp_f32 v238, v238\n" \
"v_rcp_f32 v239, v239\n" \
"v_mul_f32 v232, v232, v20\n" \
"v_mul_f32 v233, v233, v21\n" \
"v_mul_f32 v234, v234, v22\n" \
"v_mul_f32 v235, v235, v23\n" \
"v_mul_f32 v236, v236, v28\n" \
"v_mul_f32 v237, v237, v29\n" \
"v_mul_f32 v238, v238, v30\n" \
"v_mul_f32 v239, v239, v31\n" \
"v_cvt_pk_bf16_f32 v20, v232, v233\n" \
"v_cvt_pk_bf16_f32 v21, v234, v235\n" \
"v_cvt_pk_bf16_f32 v22, v236, v237\n" \
"v_cvt_pk_bf16_f32 v23, v238, v239\n" \
"v_mul_f32 v232, v32, v32\n" \
"v_mul_f32 v233, v33, v33\n" \
"v_mul_f32 v234, v34, v34\n" \
"v_mul_f32 v235, v35, v35\n" \
"v_mul_f32 v236, v40, v40\n" \
"v_mul_f32 v237, v41, v41\n" \
"v_mul_f32 v238, v42, v42\n" \
"v_mul_f32 v239, v43, v43\n" \
"v_fma_f32 v232, v232, v208, v209\n" \
"v_fma_f32 v233, v233, v208, v209\n" \
"v_fma_f32 v234, v234, v208, v209\n" \
"v_fma_f32 v235, v235, v208, v209\n" \
"v_fma_f32 v236, v236, v208, v209\n" \
"v_fma_f32 v237, v237, v208, v209\n" \
"v_fma_f32 v238, v238, v208, v209\n" \
"v_fma_f32 v239, v239, v208, v209\n" \
"v_mul_f32 v232, v232, v32\n" \
"v_mul_f32 v233, v233, v33\n" \
"v_mul_f32 v234, v234, v34\n" \
"v_mul_f32 v235, v235, v35\n" \
"v_mul_f32 v236, v236, v40\n" \
"v_mul_f32 v237, v237, v41\n" \
"v_mul_f32 v238, v238, v42\n" \
"v_mul_f32 v239, v239, v43\n" \
"v_exp_f32 v232, v232\n" \
"v_exp_f32 v233, v233\n" \
"v_exp_f32 v234, v234\n" \
"v_exp_f32 v235, v235\n" \
"v_exp_f32 v236, v236\n" \
"v_exp_f32 v237, v237\n" \
"v_exp_f32 v238, v238\n" \
"v_exp_f32 v239, v239\n" \
"v_add_f32 v232, 1.0, v232\n" \
"v_add_f32 v233, 1.0, v233\n" \
"v_add_f32 v234, 1.0, v234\n" \
"v_add_f32 v235, 1.0, v235\n" \
"v_add_f32 v236, 1.0, v236\n" \
"v_add_f32 v237, 1.0, v237\n" \
"v_add_f32 v238, 1.0, v238\n" \
"v_add_f32 v239, 1.0, v239\n" \
"v_rcp_f32 v232, v232\n" \
"v_rcp_f32 v233, v233\n" \
"v_rcp_f32 v234, v234\n" \
"v_rcp_f32 v235, v235\n" \
"v_rcp_f32 v236, v236\n" \
"v_rcp_f32 v237, v237\n" \
"v_rcp_f32 v238, v238\n" \
"v_rcp_f32 v239, v239\n" \
"v_mul_f32 v232, v232, v32\n" \
"v_mul_f32 v233, v233, v33\n" \
"v_mul_f32 v234, v234, v34\n" \
"v_mul_f32 v235, v235, v35\n" \
"v_mul_f32 v236, v236, v40\n" \
"v_mul_f32 v237, v237, v41\n" \
"v_mul_f32 v238, v238, v42\n" \
"v_mul_f32 v239, v239, v43\n" \
"v_cvt_pk_bf16_f32 v32, v232, v233\n" \
"v_cvt_pk_bf16_f32 v33, v234, v235\n" \
"v_cvt_pk_bf16_f32 v34, v236, v237\n" \
"v_cvt_pk_bf16_f32 v35, v238, v239\n" \
"v_mul_f32 v232, v36, v36\n" \
"v_mul_f32 v233, v37, v37\n" \
"v_mul_f32 v234, v38, v38\n" \
"v_mul_f32 v235, v39, v39\n" \
"v_mul_f32 v236, v44, v44\n" \
"v_mul_f32 v237, v45, v45\n" \
"v_mul_f32 v238, v46, v46\n" \
"v_mul_f32 v239, v47, v47\n" \
"v_fma_f32 v232, v232, v208, v209\n" \
"v_fma_f32 v233, v233, v208, v209\n" \
"v_fma_f32 v234, v234, v208, v209\n" \
"v_fma_f32 v235, v235, v208, v209\n" \
"v_fma_f32 v236, v236, v208, v209\n" \
"v_fma_f32 v237, v237, v208, v209\n" \
"v_fma_f32 v238, v238, v208, v209\n" \
"v_fma_f32 v239, v239, v208, v209\n" \
"v_mul_f32 v232, v232, v36\n" \
"v_mul_f32 v233, v233, v37\n" \
"v_mul_f32 v234, v234, v38\n" \
"v_mul_f32 v235, v235, v39\n" \
"v_mul_f32 v236, v236, v44\n" \
"v_mul_f32 v237, v237, v45\n" \
"v_mul_f32 v238, v238, v46\n" \
"v_mul_f32 v239, v239, v47\n" \
"v_exp_f32 v232, v232\n" \
"v_exp_f32 v233, v233\n" \
"v_exp_f32 v234, v234\n" \
"v_exp_f32 v235, v235\n" \
"v_exp_f32 v236, v236\n" \
"v_exp_f32 v237, v237\n" \
"v_exp_f32 v238, v238\n" \
"v_exp_f32 v239, v239\n" \
"v_add_f32 v232, 1.0, v232\n" \
"v_add_f32 v233, 1.0, v233\n" \
"v_add_f32 v234, 1.0, v234\n" \
"v_add_f32 v235, 1.0, v235\n" \
"v_add_f32 v236, 1.0, v236\n" \
"v_add_f32 v237, 1.0, v237\n" \
"v_add_f32 v238, 1.0, v238\n" \
"v_add_f32 v239, 1.0, v239\n" \
"v_rcp_f32 v232, v232\n" \
"v_rcp_f32 v233, v233\n" \
"v_rcp_f32 v234, v234\n" \
"v_rcp_f32 v235, v235\n" \
"v_rcp_f32 v236, v236\n" \
"v_rcp_f32 v237, v237\n" \
"v_rcp_f32 v238, v238\n" \
"v_rcp_f32 v239, v239\n" \
"v_mul_f32 v232, v232, v36\n" \
"v_mul_f32 v233, v233, v37\n" \
"v_mul_f32 v234, v234, v38\n" \
"v_mul_f32 v235, v235, v39\n" \
"v_mul_f32 v236, v236, v44\n" \
"v_mul_f32 v237, v237, v45\n" \
"v_mul_f32 v238, v238, v46\n" \
"v_mul_f32 v239, v239, v47\n" \
"v_cvt_pk_bf16_f32 v36, v232, v233\n" \
"v_cvt_pk_bf16_f32 v37, v234, v235\n" \
"v_cvt_pk_bf16_f32 v38, v236, v237\n" \
"v_cvt_pk_bf16_f32 v39, v238, v239\n" \
"v_mul_f32 v232, v48, v48\n" \
"v_mul_f32 v233, v49, v49\n" \
"v_mul_f32 v234, v50, v50\n" \
"v_mul_f32 v235, v51, v51\n" \
"v_mul_f32 v236, v56, v56\n" \
"v_mul_f32 v237, v57, v57\n" \
"v_mul_f32 v238, v58, v58\n" \
"v_mul_f32 v239, v59, v59\n" \
"v_fma_f32 v232, v232, v208, v209\n" \
"v_fma_f32 v233, v233, v208, v209\n" \
"v_fma_f32 v234, v234, v208, v209\n" \
"v_fma_f32 v235, v235, v208, v209\n" \
"v_fma_f32 v236, v236, v208, v209\n" \
"v_fma_f32 v237, v237, v208, v209\n" \
"v_fma_f32 v238, v238, v208, v209\n" \
"v_fma_f32 v239, v239, v208, v209\n" \
"v_mul_f32 v232, v232, v48\n" \
"v_mul_f32 v233, v233, v49\n" \
"v_mul_f32 v234, v234, v50\n" \
"v_mul_f32 v235, v235, v51\n" \
"v_mul_f32 v236, v236, v56\n" \
"v_mul_f32 v237, v237, v57\n" \
"v_mul_f32 v238, v238, v58\n" \
"v_mul_f32 v239, v239, v59\n" \
"v_exp_f32 v232, v232\n" \
"v_exp_f32 v233, v233\n" \
"v_exp_f32 v234, v234\n" \
"v_exp_f32 v235, v235\n" \
"v_exp_f32 v236, v236\n" \
"v_exp_f32 v237, v237\n" \
"v_exp_f32 v238, v238\n" \
"v_exp_f32 v239, v239\n" \
"v_add_f32 v232, 1.0, v232\n" \
"v_add_f32 v233, 1.0, v233\n" \
"v_add_f32 v234, 1.0, v234\n" \
"v_add_f32 v235, 1.0, v235\n" \
"v_add_f32 v236, 1.0, v236\n" \
"v_add_f32 v237, 1.0, v237\n" \
"v_add_f32 v238, 1.0, v238\n" \
"v_add_f32 v239, 1.0, v239\n" \
"v_rcp_f32 v232, v232\n" \
"v_rcp_f32 v233, v233\n" \
"v_rcp_f32 v234, v234\n" \
"v_rcp_f32 v235, v235\n" \
"v_rcp_f32 v236, v236\n" \
"v_rcp_f32 v237, v237\n" \
"v_rcp_f32 v238, v238\n" \
"v_rcp_f32 v239, v239\n" \
"v_mul_f32 v232, v232, v48\n" \
"v_mul_f32 v233, v233, v49\n" \
"v_mul_f32 v234, v234, v50\n" \
"v_mul_f32 v235, v235, v51\n" \
"v_mul_f32 v236, v236, v56\n" \
"v_mul_f32 v237, v237, v57\n" \
"v_mul_f32 v238, v238, v58\n" \
"v_mul_f32 v239, v239, v59\n" \
"v_cvt_pk_bf16_f32 v48, v232, v233\n" \
"v_cvt_pk_bf16_f32 v49, v234, v235\n" \
"v_cvt_pk_bf16_f32 v50, v236, v237\n" \
"v_cvt_pk_bf16_f32 v51, v238, v239\n" \
"v_mul_f32 v232, v52, v52\n" \
"v_mul_f32 v233, v53, v53\n" \
"v_mul_f32 v234, v54, v54\n" \
"v_mul_f32 v235, v55, v55\n" \
"v_mul_f32 v236, v60, v60\n" \
"v_mul_f32 v237, v61, v61\n" \
"v_mul_f32 v238, v62, v62\n" \
"v_mul_f32 v239, v63, v63\n" \
"v_fma_f32 v232, v232, v208, v209\n" \
"v_fma_f32 v233, v233, v208, v209\n" \
"v_fma_f32 v234, v234, v208, v209\n" \
"v_fma_f32 v235, v235, v208, v209\n" \
"v_fma_f32 v236, v236, v208, v209\n" \
"v_fma_f32 v237, v237, v208, v209\n" \
"v_fma_f32 v238, v238, v208, v209\n" \
"v_fma_f32 v239, v239, v208, v209\n" \
"v_mul_f32 v232, v232, v52\n" \
"v_mul_f32 v233, v233, v53\n" \
"v_mul_f32 v234, v234, v54\n" \
"v_mul_f32 v235, v235, v55\n" \
"v_mul_f32 v236, v236, v60\n" \
"v_mul_f32 v237, v237, v61\n" \
"v_mul_f32 v238, v238, v62\n" \
"v_mul_f32 v239, v239, v63\n" \
"v_exp_f32 v232, v232\n" \
"v_exp_f32 v233, v233\n" \
"v_exp_f32 v234, v234\n" \
"v_exp_f32 v235, v235\n" \
"v_exp_f32 v236, v236\n" \
"v_exp_f32 v237, v237\n" \
"v_exp_f32 v238, v238\n" \
"v_exp_f32 v239, v239\n" \
"v_add_f32 v232, 1.0, v232\n" \
"v_add_f32 v233, 1.0, v233\n" \
"v_add_f32 v234, 1.0, v234\n" \
"v_add_f32 v235, 1.0, v235\n" \
"v_add_f32 v236, 1.0, v236\n" \
"v_add_f32 v237, 1.0, v237\n" \
"v_add_f32 v238, 1.0, v238\n" \
"v_add_f32 v239, 1.0, v239\n" \
"v_rcp_f32 v232, v232\n" \
"v_rcp_f32 v233, v233\n" \
"v_rcp_f32 v234, v234\n" \
"v_rcp_f32 v235, v235\n" \
"v_rcp_f32 v236, v236\n" \
"v_rcp_f32 v237, v237\n" \
"v_rcp_f32 v238, v238\n" \
"v_rcp_f32 v239, v239\n" \
"v_mul_f32 v232, v232, v52\n" \
"v_mul_f32 v233, v233, v53\n" \
"v_mul_f32 v234, v234, v54\n" \
"v_mul_f32 v235, v235, v55\n" \
"v_mul_f32 v236, v236, v60\n" \
"v_mul_f32 v237, v237, v61\n" \
"v_mul_f32 v238, v238, v62\n" \
"v_mul_f32 v239, v239, v63\n" \
"v_cvt_pk_bf16_f32 v52, v232, v233\n" \
"v_cvt_pk_bf16_f32 v53, v234, v235\n" \
"v_cvt_pk_bf16_f32 v54, v236, v237\n" \
"v_cvt_pk_bf16_f32 v55, v238, v239\n" \
"ds_write_b128 v253, v[0:3] offset:0\n" \
"ds_write_b128 v253, v[4:7] offset:1024\n" \
"ds_write_b128 v253, v[16:19] offset:4096\n" \
"ds_write_b128 v253, v[20:23] offset:5120\n" \
"ds_write_b128 v253, v[32:35] offset:8192\n" \
"ds_write_b128 v253, v[36:39] offset:9216\n" \
"ds_write_b128 v253, v[48:51] offset:12288\n" \
"ds_write_b128 v253, v[52:55] offset:13312\n" \
"s_waitcnt vmcnt(24) lgkmcnt(0)\n" \
"s_barrier\n" \
"ds_read_b128 v[64:67], v254 offset:0\n" \
"ds_read_b128 v[72:75], v254 offset:4096\n" \
"ds_read_b128 v[80:83], v254 offset:8192\n" \
"ds_read_b128 v[88:91], v254 offset:12288\n" \
"ds_read_b128 v[96:99], v254 offset:16384\n" \
"ds_read_b128 v[104:107], v254 offset:20480\n" \
"ds_read_b128 v[112:115], v254 offset:24576\n" \
"ds_read_b128 v[120:123], v254 offset:28672\n" \
"ds_read_b128 v[68:71], v254 offset:1024\n" \
"ds_read_b128 v[76:79], v254 offset:5120\n" \
"ds_read_b128 v[84:87], v254 offset:9216\n" \
"ds_read_b128 v[92:95], v254 offset:13312\n" \
"ds_read_b128 v[100:103], v254 offset:17408\n" \
"ds_read_b128 v[108:111], v254 offset:21504\n" \
"ds_read_b128 v[116:119], v254 offset:25600\n" \
"ds_read_b128 v[124:127], v254 offset:29696\n" \
"ds_read_b64_tr_b16 a[192:193], v244 offset:0\n" \
"ds_read_b64_tr_b16 a[194:195], v244 offset:2048\n" \
"ds_read_b64_tr_b16 a[196:197], v244 offset:4096\n" \
"ds_read_b64_tr_b16 a[198:199], v244 offset:6144\n" \
"ds_read_b64_tr_b16 a[200:201], v244 offset:8192\n" \
"ds_read_b64_tr_b16 a[202:203], v244 offset:10240\n" \
"ds_read_b64_tr_b16 a[204:205], v244 offset:12288\n" \
"ds_read_b64_tr_b16 a[206:207], v244 offset:14336\n" \
"ds_read_b64_tr_b16 a[208:209], v244 offset:16384\n" \
"ds_read_b64_tr_b16 a[210:211], v244 offset:18432\n" \
"ds_read_b64_tr_b16 a[212:213], v244 offset:20480\n" \
"ds_read_b64_tr_b16 a[214:215], v244 offset:22528\n" \
"ds_read_b64_tr_b16 a[216:217], v244 offset:24576\n" \
"ds_read_b64_tr_b16 a[218:219], v244 offset:26624\n" \
"ds_read_b64_tr_b16 a[220:221], v244 offset:28672\n" \
"ds_read_b64_tr_b16 a[222:223], v244 offset:30720\n" \
"s_waitcnt lgkmcnt(14)\n" \
"v_mfma_f32_16x16x32_bf16 v[0:3], a[192:195], v[64:67], 0\n" \
"s_waitcnt lgkmcnt(12)\n" \
"ds_read_b64_tr_b16 a[224:225], v245 offset:0\n" \
"ds_read_b64_tr_b16 a[226:227], v245 offset:2048\n" \
"v_mfma_f32_16x16x32_bf16 v[0:3], a[196:199], v[72:75], v[0:3]\n" \
"s_waitcnt lgkmcnt(12)\n" \
"v_mfma_f32_16x16x32_bf16 v[0:3], a[200:203], v[80:83], v[0:3]\n" \
"s_waitcnt lgkmcnt(10)\n" \
"ds_read_b64_tr_b16 a[228:229], v245 offset:4096\n" \
"ds_read_b64_tr_b16 a[230:231], v245 offset:6144\n" \
"v_mfma_f32_16x16x32_bf16 v[0:3], a[204:207], v[88:91], v[0:3]\n" \
"s_waitcnt lgkmcnt(10)\n" \
"v_mfma_f32_16x16x32_bf16 v[0:3], a[208:211], v[96:99], v[0:3]\n" \
"s_waitcnt lgkmcnt(8)\n" \
"ds_read_b64_tr_b16 a[232:233], v245 offset:8192\n" \
"ds_read_b64_tr_b16 a[234:235], v245 offset:10240\n" \
"v_mfma_f32_16x16x32_bf16 v[0:3], a[212:215], v[104:107], v[0:3]\n" \
"s_waitcnt lgkmcnt(8)\n" \
"v_mfma_f32_16x16x32_bf16 v[0:3], a[216:219], v[112:115], v[0:3]\n" \
"s_waitcnt lgkmcnt(6)\n" \
"ds_read_b64_tr_b16 a[236:237], v245 offset:12288\n" \
"ds_read_b64_tr_b16 a[238:239], v245 offset:14336\n" \
"v_mfma_f32_16x16x32_bf16 v[0:3], a[220:223], v[120:123], v[0:3]\n" \
"v_mfma_f32_16x16x32_bf16 v[4:7], a[192:195], v[68:71], 0\n" \
"ds_read_b64_tr_b16 a[240:241], v245 offset:16384\n" \
"ds_read_b64_tr_b16 a[242:243], v245 offset:18432\n" \
"v_mfma_f32_16x16x32_bf16 v[4:7], a[196:199], v[76:79], v[4:7]\n" \
"v_mfma_f32_16x16x32_bf16 v[4:7], a[200:203], v[84:87], v[4:7]\n" \
"ds_read_b64_tr_b16 a[244:245], v245 offset:20480\n" \
"ds_read_b64_tr_b16 a[246:247], v245 offset:22528\n" \
"v_mfma_f32_16x16x32_bf16 v[4:7], a[204:207], v[92:95], v[4:7]\n" \
"v_mfma_f32_16x16x32_bf16 v[4:7], a[208:211], v[100:103], v[4:7]\n" \
"ds_read_b64_tr_b16 a[248:249], v245 offset:24576\n" \
"ds_read_b64_tr_b16 a[250:251], v245 offset:26624\n" \
"v_mfma_f32_16x16x32_bf16 v[4:7], a[212:215], v[108:111], v[4:7]\n" \
"s_waitcnt lgkmcnt(6)\n" \
"v_mfma_f32_16x16x32_bf16 v[4:7], a[216:219], v[116:119], v[4:7]\n" \
"s_waitcnt vmcnt(16)\n" \
"ds_read_b64_tr_b16 a[252:253], v245 offset:28672\n" \
"ds_read_b64_tr_b16 a[254:255], v245 offset:30720\n" \
"v_mfma_f32_16x16x32_bf16 v[4:7], a[220:223], v[124:127], v[4:7]\n" \
"s_barrier\n" \
"v_mfma_f32_16x16x32_bf16 v[8:11], a[224:227], v[64:67], 0\n" \
"ds_read_b64_tr_b16 a[192:193], v242 offset:32768\n" \
"ds_read_b64_tr_b16 a[194:195], v242 offset:34816\n" \
"v_mfma_f32_16x16x32_bf16 v[8:11], a[228:231], v[72:75], v[8:11]\n" \
"s_mov_b32 s68, 512\n" \
"v_mfma_f32_16x16x32_bf16 v[8:11], a[232:235], v[80:83], v[8:11]\n" \
"s_add_u32 m0, s50, 131072\n" \
"ds_read_b64_tr_b16 a[196:197], v242 offset:36864\n" \
"ds_read_b64_tr_b16 a[198:199], v242 offset:38912\n" \
"v_mfma_f32_16x16x32_bf16 v[8:11], a[236:239], v[88:91], v[8:11]\n" \
"s_waitcnt lgkmcnt(6)\n" \
"buffer_load_dwordx4 v248, s[60:63], s68 offen offset:0 lds\n" \
"v_mfma_f32_16x16x32_bf16 v[8:11], a[240:243], v[96:99], v[8:11]\n" \
"ds_read_b64_tr_b16 a[200:201], v242 offset:40960\n" \
"ds_read_b64_tr_b16 a[202:203], v242 offset:43008\n" \
"v_mfma_f32_16x16x32_bf16 v[8:11], a[244:247], v[104:107], v[8:11]\n" \
"v_mfma_f32_16x16x32_bf16 v[8:11], a[248:251], v[112:115], v[8:11]\n" \
"s_waitcnt lgkmcnt(6)\n" \
"ds_read_b64_tr_b16 a[204:205], v242 offset:45056\n" \
"ds_read_b64_tr_b16 a[206:207], v242 offset:47104\n" \
"v_mfma_f32_16x16x32_bf16 v[8:11], a[252:255], v[120:123], v[8:11]\n" \
"buffer_load_dwordx4 v249, s[60:63], s68 offen offset:1024 lds\n" \
"v_mfma_f32_16x16x32_bf16 v[12:15], a[224:227], v[68:71], 0\n" \
"ds_read_b64_tr_b16 a[208:209], v242 offset:49152\n" \
"ds_read_b64_tr_b16 a[210:211], v242 offset:51200\n" \
"v_mfma_f32_16x16x32_bf16 v[12:15], a[228:231], v[76:79], v[12:15]\n" \
"v_mfma_f32_16x16x32_bf16 v[12:15], a[232:235], v[84:87], v[12:15]\n" \
"ds_read_b64_tr_b16 a[212:213], v242 offset:53248\n" \
"ds_read_b64_tr_b16 a[214:215], v242 offset:55296\n" \
"v_mfma_f32_16x16x32_bf16 v[12:15], a[236:239], v[92:95], v[12:15]\n" \
"buffer_load_dwordx4 v250, s[60:63], s68 offen offset:2048 lds\n" \
"v_mfma_f32_16x16x32_bf16 v[12:15], a[240:243], v[100:103], v[12:15]\n" \
"ds_read_b64_tr_b16 a[216:217], v242 offset:57344\n" \
"ds_read_b64_tr_b16 a[218:219], v242 offset:59392\n" \
"v_mfma_f32_16x16x32_bf16 v[12:15], a[244:247], v[108:111], v[12:15]\n" \
"v_mfma_f32_16x16x32_bf16 v[12:15], a[248:251], v[116:119], v[12:15]\n" \
"ds_read_b64_tr_b16 a[220:221], v242 offset:61440\n" \
"ds_read_b64_tr_b16 a[222:223], v242 offset:63488\n" \
"v_mfma_f32_16x16x32_bf16 v[12:15], a[252:255], v[124:127], v[12:15]\n" \
"s_waitcnt lgkmcnt(6)\n" \
"buffer_load_dwordx4 v251, s[60:63], s68 offen offset:3072 lds\n" \
"v_mfma_f32_16x16x32_bf16 v[16:19], a[192:195], v[64:67], 0\n" \
"ds_read_b64_tr_b16 a[224:225], v243 offset:32768\n" \
"ds_read_b64_tr_b16 a[226:227], v243 offset:34816\n" \
"v_mfma_f32_16x16x32_bf16 v[16:19], a[196:199], v[72:75], v[16:19]\n" \
"s_mov_b32 s69, 115200\n" \
"v_mfma_f32_16x16x32_bf16 v[16:19], a[200:203], v[80:83], v[16:19]\n" \
"s_add_u32 m0, s50, 147456\n" \
"ds_read_b64_tr_b16 a[228:229], v243 offset:36864\n" \
"ds_read_b64_tr_b16 a[230:231], v243 offset:38912\n" \
"v_mfma_f32_16x16x32_bf16 v[16:19], a[204:207], v[88:91], v[16:19]\n" \
"buffer_load_dwordx4 v248, s[60:63], s69 offen offset:0 lds\n" \
"v_mfma_f32_16x16x32_bf16 v[16:19], a[208:211], v[96:99], v[16:19]\n" \
"s_waitcnt lgkmcnt(6)\n" \
"ds_read_b64_tr_b16 a[232:233], v243 offset:40960\n" \
"ds_read_b64_tr_b16 a[234:235], v243 offset:43008\n" \
"v_mfma_f32_16x16x32_bf16 v[16:19], a[212:215], v[104:107], v[16:19]\n" \
"v_cvt_pk_bf16_f32 v128, v0, v1\n" \
"v_cvt_pk_bf16_f32 v129, v2, v3\n" \
"v_mfma_f32_16x16x32_bf16 v[16:19], a[216:219], v[112:115], v[16:19]\n" \
"s_waitcnt lgkmcnt(6)\n" \
"ds_read_b64_tr_b16 a[236:237], v243 offset:45056\n" \
"ds_read_b64_tr_b16 a[238:239], v243 offset:47104\n" \
"v_mfma_f32_16x16x32_bf16 v[16:19], a[220:223], v[120:123], v[16:19]\n" \
"buffer_load_dwordx4 v249, s[60:63], s69 offen offset:1024 lds\n" \
"v_mfma_f32_16x16x32_bf16 v[20:23], a[192:195], v[68:71], 0\n" \
"ds_read_b64_tr_b16 a[240:241], v243 offset:49152\n" \
"ds_read_b64_tr_b16 a[242:243], v243 offset:51200\n" \
"v_mfma_f32_16x16x32_bf16 v[20:23], a[196:199], v[76:79], v[20:23]\n" \
"v_cvt_pk_bf16_f32 v130, v8, v9\n" \
"v_cvt_pk_bf16_f32 v131, v10, v11\n" \
"v_mfma_f32_16x16x32_bf16 v[20:23], a[200:203], v[84:87], v[20:23]\n" \
"ds_read_b64_tr_b16 a[244:245], v243 offset:53248\n" \
"ds_read_b64_tr_b16 a[246:247], v243 offset:55296\n" \
"v_mfma_f32_16x16x32_bf16 v[20:23], a[204:207], v[92:95], v[20:23]\n" \
"buffer_load_dwordx4 v250, s[60:63], s69 offen offset:2048 lds\n" \
"v_mfma_f32_16x16x32_bf16 v[20:23], a[208:211], v[100:103], v[20:23]\n" \
"ds_read_b64_tr_b16 a[248:249], v243 offset:57344\n" \
"ds_read_b64_tr_b16 a[250:251], v243 offset:59392\n" \
"v_mfma_f32_16x16x32_bf16 v[20:23], a[212:215], v[108:111], v[20:23]\n" \
"s_waitcnt lgkmcnt(6)\n" \
"v_cvt_pk_bf16_f32 v132, v4, v5\n" \
"v_cvt_pk_bf16_f32 v133, v6, v7\n" \
"v_mfma_f32_16x16x32_bf16 v[20:23], a[216:219], v[116:119], v[20:23]\n" \
"s_waitcnt vmcnt(15)\n" \
"ds_read_b64_tr_b16 a[252:253], v243 offset:61440\n" \
"ds_read_b64_tr_b16 a[254:255], v243 offset:63488\n" \
"v_mfma_f32_16x16x32_bf16 v[20:23], a[220:223], v[124:127], v[20:23]\n" \
"s_barrier\n" \
"buffer_load_dwordx4 v251, s[60:63], s69 offen offset:3072 lds\n" \
"v_mfma_f32_16x16x32_bf16 v[24:27], a[224:227], v[64:67], 0\n" \
"ds_read_b64_tr_b16 a[192:193], v244 offset:32768\n" \
"ds_read_b64_tr_b16 a[194:195], v244 offset:34816\n" \
"v_mfma_f32_16x16x32_bf16 v[24:27], a[228:231], v[72:75], v[24:27]\n" \
"s_mov_b32 s68, 640\n" \
"v_cvt_pk_bf16_f32 v134, v12, v13\n" \
"v_cvt_pk_bf16_f32 v135, v14, v15\n" \
"v_mfma_f32_16x16x32_bf16 v[24:27], a[232:235], v[80:83], v[24:27]\n" \
"s_add_u32 m0, s50, 65536\n" \
"ds_read_b64_tr_b16 a[196:197], v244 offset:36864\n" \
"ds_read_b64_tr_b16 a[198:199], v244 offset:38912\n" \
"v_mfma_f32_16x16x32_bf16 v[24:27], a[236:239], v[88:91], v[24:27]\n" \
"s_waitcnt lgkmcnt(6)\n" \
"buffer_load_dwordx4 v248, s[60:63], s68 offen offset:0 lds\n" \
"v_mfma_f32_16x16x32_bf16 v[24:27], a[240:243], v[96:99], v[24:27]\n" \
"ds_read_b64_tr_b16 a[200:201], v244 offset:40960\n" \
"ds_read_b64_tr_b16 a[202:203], v244 offset:43008\n" \
"v_mfma_f32_16x16x32_bf16 v[24:27], a[244:247], v[104:107], v[24:27]\n" \
"buffer_store_dwordx4 v[128:131], v252, s[64:67], 0 offen offset:0 sc1\n" \
"v_mfma_f32_16x16x32_bf16 v[24:27], a[248:251], v[112:115], v[24:27]\n" \
"s_waitcnt lgkmcnt(6)\n" \
"ds_read_b64_tr_b16 a[204:205], v244 offset:45056\n" \
"ds_read_b64_tr_b16 a[206:207], v244 offset:47104\n" \
"v_mfma_f32_16x16x32_bf16 v[24:27], a[252:255], v[120:123], v[24:27]\n" \
"buffer_load_dwordx4 v249, s[60:63], s68 offen offset:1024 lds\n" \
"v_mfma_f32_16x16x32_bf16 v[28:31], a[224:227], v[68:71], 0\n" \
"ds_read_b64_tr_b16 a[208:209], v244 offset:49152\n" \
"ds_read_b64_tr_b16 a[210:211], v244 offset:51200\n" \
"v_mfma_f32_16x16x32_bf16 v[28:31], a[228:231], v[76:79], v[28:31]\n" \
"buffer_store_dwordx4 v[132:135], v252, s[64:67], 0 offen offset:1024 sc1\n" \
"v_mfma_f32_16x16x32_bf16 v[28:31], a[232:235], v[84:87], v[28:31]\n" \
"ds_read_b64_tr_b16 a[212:213], v244 offset:53248\n" \
"ds_read_b64_tr_b16 a[214:215], v244 offset:55296\n" \
"v_mfma_f32_16x16x32_bf16 v[28:31], a[236:239], v[92:95], v[28:31]\n" \
"buffer_load_dwordx4 v250, s[60:63], s68 offen offset:2048 lds\n" \
"v_mfma_f32_16x16x32_bf16 v[28:31], a[240:243], v[100:103], v[28:31]\n" \
"ds_read_b64_tr_b16 a[216:217], v244 offset:57344\n" \
"ds_read_b64_tr_b16 a[218:219], v244 offset:59392\n" \
"v_mfma_f32_16x16x32_bf16 v[28:31], a[244:247], v[108:111], v[28:31]\n" \
"v_add_u32 v252, 0x2000, v252\n" \
"v_mfma_f32_16x16x32_bf16 v[28:31], a[248:251], v[116:119], v[28:31]\n" \
"ds_read_b64_tr_b16 a[220:221], v244 offset:61440\n" \
"ds_read_b64_tr_b16 a[222:223], v244 offset:63488\n" \
"v_mfma_f32_16x16x32_bf16 v[28:31], a[252:255], v[124:127], v[28:31]\n" \
"s_waitcnt lgkmcnt(6)\n" \
"buffer_load_dwordx4 v251, s[60:63], s68 offen offset:3072 lds\n" \
"v_mfma_f32_16x16x32_bf16 v[0:3], a[192:195], v[64:67], 0\n" \
"ds_read_b64_tr_b16 a[224:225], v245 offset:32768\n" \
"ds_read_b64_tr_b16 a[226:227], v245 offset:34816\n" \
"v_mfma_f32_16x16x32_bf16 v[0:3], a[196:199], v[72:75], v[0:3]\n" \
"s_mov_b32 s69, 115328\n" \
"v_mfma_f32_16x16x32_bf16 v[0:3], a[200:203], v[80:83], v[0:3]\n" \
"s_add_u32 m0, s50, 81920\n" \
"ds_read_b64_tr_b16 a[228:229], v245 offset:36864\n" \
"ds_read_b64_tr_b16 a[230:231], v245 offset:38912\n" \
"v_mfma_f32_16x16x32_bf16 v[0:3], a[204:207], v[88:91], v[0:3]\n" \
"buffer_load_dwordx4 v248, s[60:63], s69 offen offset:0 lds\n" \
"v_mfma_f32_16x16x32_bf16 v[0:3], a[208:211], v[96:99], v[0:3]\n" \
"s_waitcnt lgkmcnt(6)\n" \
"ds_read_b64_tr_b16 a[232:233], v245 offset:40960\n" \
"ds_read_b64_tr_b16 a[234:235], v245 offset:43008\n" \
"v_mfma_f32_16x16x32_bf16 v[0:3], a[212:215], v[104:107], v[0:3]\n" \
"v_cvt_pk_bf16_f32 v136, v16, v17\n" \
"v_cvt_pk_bf16_f32 v137, v18, v19\n" \
"v_mfma_f32_16x16x32_bf16 v[0:3], a[216:219], v[112:115], v[0:3]\n" \
"s_waitcnt lgkmcnt(6)\n" \
"ds_read_b64_tr_b16 a[236:237], v245 offset:45056\n" \
"ds_read_b64_tr_b16 a[238:239], v245 offset:47104\n" \
"v_mfma_f32_16x16x32_bf16 v[0:3], a[220:223], v[120:123], v[0:3]\n" \
"buffer_load_dwordx4 v249, s[60:63], s69 offen offset:1024 lds\n" \
"v_mfma_f32_16x16x32_bf16 v[4:7], a[192:195], v[68:71], 0\n" \
"ds_read_b64_tr_b16 a[240:241], v245 offset:49152\n" \
"ds_read_b64_tr_b16 a[242:243], v245 offset:51200\n" \
"v_mfma_f32_16x16x32_bf16 v[4:7], a[196:199], v[76:79], v[4:7]\n" \
"v_cvt_pk_bf16_f32 v138, v24, v25\n" \
"v_cvt_pk_bf16_f32 v139, v26, v27\n" \
"v_mfma_f32_16x16x32_bf16 v[4:7], a[200:203], v[84:87], v[4:7]\n" \
"ds_read_b64_tr_b16 a[244:245], v245 offset:53248\n" \
"ds_read_b64_tr_b16 a[246:247], v245 offset:55296\n" \
"v_mfma_f32_16x16x32_bf16 v[4:7], a[204:207], v[92:95], v[4:7]\n" \
"buffer_load_dwordx4 v250, s[60:63], s69 offen offset:2048 lds\n" \
"v_mfma_f32_16x16x32_bf16 v[4:7], a[208:211], v[100:103], v[4:7]\n" \
"ds_read_b64_tr_b16 a[248:249], v245 offset:57344\n" \
"ds_read_b64_tr_b16 a[250:251], v245 offset:59392\n" \
"v_mfma_f32_16x16x32_bf16 v[4:7], a[212:215], v[108:111], v[4:7]\n" \
"s_waitcnt lgkmcnt(6)\n" \
"v_cvt_pk_bf16_f32 v140, v20, v21\n" \
"v_cvt_pk_bf16_f32 v141, v22, v23\n" \
"v_mfma_f32_16x16x32_bf16 v[4:7], a[216:219], v[116:119], v[4:7]\n" \
"s_waitcnt vmcnt(17)\n" \
"ds_read_b64_tr_b16 a[252:253], v245 offset:61440\n" \
"ds_read_b64_tr_b16 a[254:255], v245 offset:63488\n" \
"v_mfma_f32_16x16x32_bf16 v[4:7], a[220:223], v[124:127], v[4:7]\n" \
"s_barrier\n" \
"buffer_load_dwordx4 v251, s[60:63], s69 offen offset:3072 lds\n" \
"v_mfma_f32_16x16x32_bf16 v[8:11], a[224:227], v[64:67], 0\n" \
"ds_read_b64_tr_b16 a[192:193], v242 offset:0\n" \
"ds_read_b64_tr_b16 a[194:195], v242 offset:2048\n" \
"v_mfma_f32_16x16x32_bf16 v[8:11], a[228:231], v[72:75], v[8:11]\n" \
"s_mov_b32 s68, 768\n" \
"v_cvt_pk_bf16_f32 v142, v28, v29\n" \
"v_cvt_pk_bf16_f32 v143, v30, v31\n" \
"v_mfma_f32_16x16x32_bf16 v[8:11], a[232:235], v[80:83], v[8:11]\n" \
"s_add_u32 m0, s50, 32768\n" \
"ds_read_b64_tr_b16 a[196:197], v242 offset:4096\n" \
"ds_read_b64_tr_b16 a[198:199], v242 offset:6144\n" \
"v_mfma_f32_16x16x32_bf16 v[8:11], a[236:239], v[88:91], v[8:11]\n" \
"s_waitcnt lgkmcnt(6)\n" \
"buffer_load_dwordx4 v248, s[60:63], s68 offen offset:0 lds\n" \
"v_mfma_f32_16x16x32_bf16 v[8:11], a[240:243], v[96:99], v[8:11]\n" \
"ds_read_b64_tr_b16 a[200:201], v242 offset:8192\n" \
"ds_read_b64_tr_b16 a[202:203], v242 offset:10240\n" \
"v_mfma_f32_16x16x32_bf16 v[8:11], a[244:247], v[104:107], v[8:11]\n" \
"buffer_store_dwordx4 v[136:139], v252, s[64:67], 0 offen offset:0 sc1\n" \
"v_mfma_f32_16x16x32_bf16 v[8:11], a[248:251], v[112:115], v[8:11]\n" \
"s_waitcnt lgkmcnt(6)\n" \
"ds_read_b64_tr_b16 a[204:205], v242 offset:12288\n" \
"ds_read_b64_tr_b16 a[206:207], v242 offset:14336\n" \
"v_mfma_f32_16x16x32_bf16 v[8:11], a[252:255], v[120:123], v[8:11]\n" \
"buffer_load_dwordx4 v249, s[60:63], s68 offen offset:1024 lds\n" \
"v_mfma_f32_16x16x32_bf16 v[12:15], a[224:227], v[68:71], 0\n" \
"ds_read_b64_tr_b16 a[208:209], v242 offset:16384\n" \
"ds_read_b64_tr_b16 a[210:211], v242 offset:18432\n" \
"v_mfma_f32_16x16x32_bf16 v[12:15], a[228:231], v[76:79], v[12:15]\n" \
"buffer_store_dwordx4 v[140:143], v252, s[64:67], 0 offen offset:1024 sc1\n" \
"v_mfma_f32_16x16x32_bf16 v[12:15], a[232:235], v[84:87], v[12:15]\n" \
"ds_read_b64_tr_b16 a[212:213], v242 offset:20480\n" \
"ds_read_b64_tr_b16 a[214:215], v242 offset:22528\n" \
"v_mfma_f32_16x16x32_bf16 v[12:15], a[236:239], v[92:95], v[12:15]\n" \
"buffer_load_dwordx4 v250, s[60:63], s68 offen offset:2048 lds\n" \
"v_mfma_f32_16x16x32_bf16 v[12:15], a[240:243], v[100:103], v[12:15]\n" \
"ds_read_b64_tr_b16 a[216:217], v242 offset:24576\n" \
"ds_read_b64_tr_b16 a[218:219], v242 offset:26624\n" \
"v_mfma_f32_16x16x32_bf16 v[12:15], a[244:247], v[108:111], v[12:15]\n" \
"v_add_u32 v252, 0x2000, v252\n" \
"v_mfma_f32_16x16x32_bf16 v[12:15], a[248:251], v[116:119], v[12:15]\n" \
"ds_read_b64_tr_b16 a[220:221], v242 offset:28672\n" \
"ds_read_b64_tr_b16 a[222:223], v242 offset:30720\n" \
"v_mfma_f32_16x16x32_bf16 v[12:15], a[252:255], v[124:127], v[12:15]\n" \
"s_waitcnt lgkmcnt(6)\n" \
"buffer_load_dwordx4 v251, s[60:63], s68 offen offset:3072 lds\n" \
"v_mfma_f32_16x16x32_bf16 v[16:19], a[192:195], v[64:67], 0\n" \
"ds_read_b64_tr_b16 a[224:225], v243 offset:0\n" \
"ds_read_b64_tr_b16 a[226:227], v243 offset:2048\n" \
"v_mfma_f32_16x16x32_bf16 v[16:19], a[196:199], v[72:75], v[16:19]\n" \
"s_mov_b32 s69, 115456\n" \
"v_mfma_f32_16x16x32_bf16 v[16:19], a[200:203], v[80:83], v[16:19]\n" \
"s_add_u32 m0, s50, 49152\n" \
"ds_read_b64_tr_b16 a[228:229], v243 offset:4096\n" \
"ds_read_b64_tr_b16 a[230:231], v243 offset:6144\n" \
"v_mfma_f32_16x16x32_bf16 v[16:19], a[204:207], v[88:91], v[16:19]\n" \
"buffer_load_dwordx4 v248, s[60:63], s69 offen offset:0 lds\n" \
"v_mfma_f32_16x16x32_bf16 v[16:19], a[208:211], v[96:99], v[16:19]\n" \
"s_waitcnt lgkmcnt(6)\n" \
"ds_read_b64_tr_b16 a[232:233], v243 offset:8192\n" \
"ds_read_b64_tr_b16 a[234:235], v243 offset:10240\n" \
"v_mfma_f32_16x16x32_bf16 v[16:19], a[212:215], v[104:107], v[16:19]\n" \
"v_cvt_pk_bf16_f32 v144, v0, v1\n" \
"v_cvt_pk_bf16_f32 v145, v2, v3\n" \
"v_mfma_f32_16x16x32_bf16 v[16:19], a[216:219], v[112:115], v[16:19]\n" \
"s_waitcnt lgkmcnt(6)\n" \
"ds_read_b64_tr_b16 a[236:237], v243 offset:12288\n" \
"ds_read_b64_tr_b16 a[238:239], v243 offset:14336\n" \
"v_mfma_f32_16x16x32_bf16 v[16:19], a[220:223], v[120:123], v[16:19]\n" \
"buffer_load_dwordx4 v249, s[60:63], s69 offen offset:1024 lds\n" \
"v_mfma_f32_16x16x32_bf16 v[20:23], a[192:195], v[68:71], 0\n" \
"ds_read_b64_tr_b16 a[240:241], v243 offset:16384\n" \
"ds_read_b64_tr_b16 a[242:243], v243 offset:18432\n" \
"v_mfma_f32_16x16x32_bf16 v[20:23], a[196:199], v[76:79], v[20:23]\n" \
"v_cvt_pk_bf16_f32 v146, v8, v9\n" \
"v_cvt_pk_bf16_f32 v147, v10, v11\n" \
"v_mfma_f32_16x16x32_bf16 v[20:23], a[200:203], v[84:87], v[20:23]\n" \
"ds_read_b64_tr_b16 a[244:245], v243 offset:20480\n" \
"ds_read_b64_tr_b16 a[246:247], v243 offset:22528\n" \
"v_mfma_f32_16x16x32_bf16 v[20:23], a[204:207], v[92:95], v[20:23]\n" \
"buffer_load_dwordx4 v250, s[60:63], s69 offen offset:2048 lds\n" \
"v_mfma_f32_16x16x32_bf16 v[20:23], a[208:211], v[100:103], v[20:23]\n" \
"ds_read_b64_tr_b16 a[248:249], v243 offset:24576\n" \
"ds_read_b64_tr_b16 a[250:251], v243 offset:26624\n" \
"v_mfma_f32_16x16x32_bf16 v[20:23], a[212:215], v[108:111], v[20:23]\n" \
"s_waitcnt lgkmcnt(6)\n" \
"v_cvt_pk_bf16_f32 v148, v4, v5\n" \
"v_cvt_pk_bf16_f32 v149, v6, v7\n" \
"v_mfma_f32_16x16x32_bf16 v[20:23], a[216:219], v[116:119], v[20:23]\n" \
"s_waitcnt vmcnt(19)\n" \
"ds_read_b64_tr_b16 a[252:253], v243 offset:28672\n" \
"ds_read_b64_tr_b16 a[254:255], v243 offset:30720\n" \
"v_mfma_f32_16x16x32_bf16 v[20:23], a[220:223], v[124:127], v[20:23]\n" \
"s_barrier\n" \
"buffer_load_dwordx4 v251, s[60:63], s69 offen offset:3072 lds\n" \
"v_mfma_f32_16x16x32_bf16 v[24:27], a[224:227], v[64:67], 0\n" \
"ds_read_b64_tr_b16 a[192:193], v246 offset:0\n" \
"ds_read_b64_tr_b16 a[194:195], v246 offset:2048\n" \
"v_mfma_f32_16x16x32_bf16 v[24:27], a[228:231], v[72:75], v[24:27]\n" \
"v_cvt_pk_bf16_f32 v150, v12, v13\n" \
"v_cvt_pk_bf16_f32 v151, v14, v15\n" \
"v_mfma_f32_16x16x32_bf16 v[24:27], a[232:235], v[80:83], v[24:27]\n" \
"ds_read_b64_tr_b16 a[196:197], v246 offset:4096\n" \
"ds_read_b64_tr_b16 a[198:199], v246 offset:6144\n" \
"v_mfma_f32_16x16x32_bf16 v[24:27], a[236:239], v[88:91], v[24:27]\n" \
"s_waitcnt lgkmcnt(6)\n" \
"buffer_store_dwordx4 v[144:147], v252, s[64:67], 0 offen offset:0 sc1\n" \
"v_mfma_f32_16x16x32_bf16 v[24:27], a[240:243], v[96:99], v[24:27]\n" \
"ds_read_b64_tr_b16 a[200:201], v246 offset:8192\n" \
"ds_read_b64_tr_b16 a[202:203], v246 offset:10240\n" \
"v_mfma_f32_16x16x32_bf16 v[24:27], a[244:247], v[104:107], v[24:27]\n" \
"buffer_store_dwordx4 v[148:151], v252, s[64:67], 0 offen offset:1024 sc1\n" \
"v_mfma_f32_16x16x32_bf16 v[24:27], a[248:251], v[112:115], v[24:27]\n" \
"s_waitcnt lgkmcnt(6)\n" \
"ds_read_b64_tr_b16 a[204:205], v246 offset:12288\n" \
"ds_read_b64_tr_b16 a[206:207], v246 offset:14336\n" \
"v_mfma_f32_16x16x32_bf16 v[24:27], a[252:255], v[120:123], v[24:27]\n" \
"v_add_u32 v252, 0x2000, v252\n" \
"v_mfma_f32_16x16x32_bf16 v[28:31], a[224:227], v[68:71], 0\n" \
"ds_read_b64_tr_b16 a[208:209], v246 offset:16384\n" \
"ds_read_b64_tr_b16 a[210:211], v246 offset:18432\n" \
"v_mfma_f32_16x16x32_bf16 v[28:31], a[228:231], v[76:79], v[28:31]\n" \
"v_mfma_f32_16x16x32_bf16 v[28:31], a[232:235], v[84:87], v[28:31]\n" \
"ds_read_b64_tr_b16 a[212:213], v246 offset:20480\n" \
"ds_read_b64_tr_b16 a[214:215], v246 offset:22528\n" \
"v_mfma_f32_16x16x32_bf16 v[28:31], a[236:239], v[92:95], v[28:31]\n" \
"v_mfma_f32_16x16x32_bf16 v[28:31], a[240:243], v[100:103], v[28:31]\n" \
"ds_read_b64_tr_b16 a[216:217], v246 offset:24576\n" \
"ds_read_b64_tr_b16 a[218:219], v246 offset:26624\n" \
"v_mfma_f32_16x16x32_bf16 v[28:31], a[244:247], v[108:111], v[28:31]\n" \
"v_mfma_f32_16x16x32_bf16 v[28:31], a[248:251], v[116:119], v[28:31]\n" \
"ds_read_b64_tr_b16 a[220:221], v246 offset:28672\n" \
"ds_read_b64_tr_b16 a[222:223], v246 offset:30720\n" \
"v_mfma_f32_16x16x32_bf16 v[28:31], a[252:255], v[124:127], v[28:31]\n" \
"s_waitcnt lgkmcnt(6)\n" \
"v_mfma_f32_16x16x32_bf16 v[0:3], a[192:195], v[64:67], 0\n" \
"ds_read_b64_tr_b16 a[224:225], v247 offset:0\n" \
"ds_read_b64_tr_b16 a[226:227], v247 offset:2048\n" \
"v_mfma_f32_16x16x32_bf16 v[0:3], a[196:199], v[72:75], v[0:3]\n" \
"v_mfma_f32_16x16x32_bf16 v[0:3], a[200:203], v[80:83], v[0:3]\n" \
"ds_read_b64_tr_b16 a[228:229], v247 offset:4096\n" \
"ds_read_b64_tr_b16 a[230:231], v247 offset:6144\n" \
"v_mfma_f32_16x16x32_bf16 v[0:3], a[204:207], v[88:91], v[0:3]\n" \
"v_cvt_pk_bf16_f32 v152, v16, v17\n" \
"v_cvt_pk_bf16_f32 v153, v18, v19\n" \
"v_mfma_f32_16x16x32_bf16 v[0:3], a[208:211], v[96:99], v[0:3]\n" \
"s_waitcnt lgkmcnt(6)\n" \
"ds_read_b64_tr_b16 a[232:233], v247 offset:8192\n" \
"ds_read_b64_tr_b16 a[234:235], v247 offset:10240\n" \
"v_mfma_f32_16x16x32_bf16 v[0:3], a[212:215], v[104:107], v[0:3]\n" \
"v_cvt_pk_bf16_f32 v154, v24, v25\n" \
"v_cvt_pk_bf16_f32 v155, v26, v27\n" \
"v_mfma_f32_16x16x32_bf16 v[0:3], a[216:219], v[112:115], v[0:3]\n" \
"s_waitcnt lgkmcnt(6)\n" \
"ds_read_b64_tr_b16 a[236:237], v247 offset:12288\n" \
"ds_read_b64_tr_b16 a[238:239], v247 offset:14336\n" \
"v_mfma_f32_16x16x32_bf16 v[0:3], a[220:223], v[120:123], v[0:3]\n" \
"v_cvt_pk_bf16_f32 v156, v20, v21\n" \
"v_cvt_pk_bf16_f32 v157, v22, v23\n" \
"v_mfma_f32_16x16x32_bf16 v[4:7], a[192:195], v[68:71], 0\n" \
"ds_read_b64_tr_b16 a[240:241], v247 offset:16384\n" \
"ds_read_b64_tr_b16 a[242:243], v247 offset:18432\n" \
"v_mfma_f32_16x16x32_bf16 v[4:7], a[196:199], v[76:79], v[4:7]\n" \
"v_cvt_pk_bf16_f32 v158, v28, v29\n" \
"v_cvt_pk_bf16_f32 v159, v30, v31\n" \
"v_mfma_f32_16x16x32_bf16 v[4:7], a[200:203], v[84:87], v[4:7]\n" \
"ds_read_b64_tr_b16 a[244:245], v247 offset:20480\n" \
"ds_read_b64_tr_b16 a[246:247], v247 offset:22528\n" \
"v_mfma_f32_16x16x32_bf16 v[4:7], a[204:207], v[92:95], v[4:7]\n" \
"buffer_store_dwordx4 v[152:155], v252, s[64:67], 0 offen offset:0 sc1\n" \
"v_mfma_f32_16x16x32_bf16 v[4:7], a[208:211], v[100:103], v[4:7]\n" \
"ds_read_b64_tr_b16 a[248:249], v247 offset:24576\n" \
"ds_read_b64_tr_b16 a[250:251], v247 offset:26624\n" \
"v_mfma_f32_16x16x32_bf16 v[4:7], a[212:215], v[108:111], v[4:7]\n" \
"s_waitcnt lgkmcnt(6)\n" \
"buffer_store_dwordx4 v[156:159], v252, s[64:67], 0 offen offset:1024 sc1\n" \
"v_mfma_f32_16x16x32_bf16 v[4:7], a[216:219], v[116:119], v[4:7]\n" \
"s_waitcnt vmcnt(14)\n" \
"ds_read_b64_tr_b16 a[252:253], v247 offset:28672\n" \
"ds_read_b64_tr_b16 a[254:255], v247 offset:30720\n" \
"v_mfma_f32_16x16x32_bf16 v[4:7], a[220:223], v[124:127], v[4:7]\n" \
"s_barrier\n" \
"v_add_u32 v252, 0x2000, v252\n" \
"v_mfma_f32_16x16x32_bf16 v[8:11], a[224:227], v[64:67], 0\n" \
"ds_read_b64_tr_b16 a[192:193], v244 offset:0\n" \
"ds_read_b64_tr_b16 a[194:195], v244 offset:2048\n" \
"v_mfma_f32_16x16x32_bf16 v[8:11], a[228:231], v[72:75], v[8:11]\n" \
"v_mfma_f32_16x16x32_bf16 v[8:11], a[232:235], v[80:83], v[8:11]\n" \
"ds_read_b64_tr_b16 a[196:197], v244 offset:4096\n" \
"ds_read_b64_tr_b16 a[198:199], v244 offset:6144\n" \
"v_mfma_f32_16x16x32_bf16 v[8:11], a[236:239], v[88:91], v[8:11]\n" \
"s_waitcnt lgkmcnt(6)\n" \
"v_mfma_f32_16x16x32_bf16 v[8:11], a[240:243], v[96:99], v[8:11]\n" \
"ds_read_b64_tr_b16 a[200:201], v244 offset:8192\n" \
"ds_read_b64_tr_b16 a[202:203], v244 offset:10240\n" \
"v_mfma_f32_16x16x32_bf16 v[8:11], a[244:247], v[104:107], v[8:11]\n" \
"v_mfma_f32_16x16x32_bf16 v[8:11], a[248:251], v[112:115], v[8:11]\n" \
"s_waitcnt lgkmcnt(6)\n" \
"ds_read_b64_tr_b16 a[204:205], v244 offset:12288\n" \
"ds_read_b64_tr_b16 a[206:207], v244 offset:14336\n" \
"v_mfma_f32_16x16x32_bf16 v[8:11], a[252:255], v[120:123], v[8:11]\n" \
"v_mfma_f32_16x16x32_bf16 v[12:15], a[224:227], v[68:71], 0\n" \
"ds_read_b64_tr_b16 a[208:209], v244 offset:16384\n" \
"ds_read_b64_tr_b16 a[210:211], v244 offset:18432\n" \
"v_mfma_f32_16x16x32_bf16 v[12:15], a[228:231], v[76:79], v[12:15]\n" \
"v_mfma_f32_16x16x32_bf16 v[12:15], a[232:235], v[84:87], v[12:15]\n" \
"ds_read_b64_tr_b16 a[212:213], v244 offset:20480\n" \
"ds_read_b64_tr_b16 a[214:215], v244 offset:22528\n" \
"v_mfma_f32_16x16x32_bf16 v[12:15], a[236:239], v[92:95], v[12:15]\n" \
"v_mfma_f32_16x16x32_bf16 v[12:15], a[240:243], v[100:103], v[12:15]\n" \
"ds_read_b64_tr_b16 a[216:217], v244 offset:24576\n" \
"ds_read_b64_tr_b16 a[218:219], v244 offset:26624\n" \
"v_mfma_f32_16x16x32_bf16 v[12:15], a[244:247], v[108:111], v[12:15]\n" \
"v_mfma_f32_16x16x32_bf16 v[12:15], a[248:251], v[116:119], v[12:15]\n" \
"ds_read_b64_tr_b16 a[220:221], v244 offset:28672\n" \
"ds_read_b64_tr_b16 a[222:223], v244 offset:30720\n" \
"v_mfma_f32_16x16x32_bf16 v[12:15], a[252:255], v[124:127], v[12:15]\n" \
"s_waitcnt lgkmcnt(6)\n" \
"v_mfma_f32_16x16x32_bf16 v[16:19], a[192:195], v[64:67], 0\n" \
"ds_read_b64_tr_b16 a[224:225], v245 offset:0\n" \
"ds_read_b64_tr_b16 a[226:227], v245 offset:2048\n" \
"v_mfma_f32_16x16x32_bf16 v[16:19], a[196:199], v[72:75], v[16:19]\n" \
"v_mfma_f32_16x16x32_bf16 v[16:19], a[200:203], v[80:83], v[16:19]\n" \
"ds_read_b64_tr_b16 a[228:229], v245 offset:4096\n" \
"ds_read_b64_tr_b16 a[230:231], v245 offset:6144\n" \
"v_mfma_f32_16x16x32_bf16 v[16:19], a[204:207], v[88:91], v[16:19]\n" \
"v_cvt_pk_bf16_f32 v160, v0, v1\n" \
"v_cvt_pk_bf16_f32 v161, v2, v3\n" \
"v_mfma_f32_16x16x32_bf16 v[16:19], a[208:211], v[96:99], v[16:19]\n" \
"s_waitcnt lgkmcnt(6)\n" \
"ds_read_b64_tr_b16 a[232:233], v245 offset:8192\n" \
"ds_read_b64_tr_b16 a[234:235], v245 offset:10240\n" \
"v_mfma_f32_16x16x32_bf16 v[16:19], a[212:215], v[104:107], v[16:19]\n" \
"v_cvt_pk_bf16_f32 v162, v8, v9\n" \
"v_cvt_pk_bf16_f32 v163, v10, v11\n" \
"v_mfma_f32_16x16x32_bf16 v[16:19], a[216:219], v[112:115], v[16:19]\n" \
"s_waitcnt lgkmcnt(6)\n" \
"ds_read_b64_tr_b16 a[236:237], v245 offset:12288\n" \
"ds_read_b64_tr_b16 a[238:239], v245 offset:14336\n" \
"v_mfma_f32_16x16x32_bf16 v[16:19], a[220:223], v[120:123], v[16:19]\n" \
"v_cvt_pk_bf16_f32 v164, v4, v5\n" \
"v_cvt_pk_bf16_f32 v165, v6, v7\n" \
"v_mfma_f32_16x16x32_bf16 v[20:23], a[192:195], v[68:71], 0\n" \
"ds_read_b64_tr_b16 a[240:241], v245 offset:16384\n" \
"ds_read_b64_tr_b16 a[242:243], v245 offset:18432\n" \
"v_mfma_f32_16x16x32_bf16 v[20:23], a[196:199], v[76:79], v[20:23]\n" \
"v_cvt_pk_bf16_f32 v166, v12, v13\n" \
"v_cvt_pk_bf16_f32 v167, v14, v15\n" \
"v_mfma_f32_16x16x32_bf16 v[20:23], a[200:203], v[84:87], v[20:23]\n" \
"ds_read_b64_tr_b16 a[244:245], v245 offset:20480\n" \
"ds_read_b64_tr_b16 a[246:247], v245 offset:22528\n" \
"v_mfma_f32_16x16x32_bf16 v[20:23], a[204:207], v[92:95], v[20:23]\n" \
"buffer_store_dwordx4 v[160:163], v252, s[64:67], 0 offen offset:0 sc1\n" \
"v_mfma_f32_16x16x32_bf16 v[20:23], a[208:211], v[100:103], v[20:23]\n" \
"ds_read_b64_tr_b16 a[248:249], v245 offset:24576\n" \
"ds_read_b64_tr_b16 a[250:251], v245 offset:26624\n" \
"v_mfma_f32_16x16x32_bf16 v[20:23], a[212:215], v[108:111], v[20:23]\n" \
"s_waitcnt lgkmcnt(6)\n" \
"buffer_store_dwordx4 v[164:167], v252, s[64:67], 0 offen offset:1024 sc1\n" \
"v_mfma_f32_16x16x32_bf16 v[20:23], a[216:219], v[116:119], v[20:23]\n" \
"s_waitcnt vmcnt(6)\n" \
"ds_read_b64_tr_b16 a[252:253], v245 offset:28672\n" \
"ds_read_b64_tr_b16 a[254:255], v245 offset:30720\n" \
"v_mfma_f32_16x16x32_bf16 v[20:23], a[220:223], v[124:127], v[20:23]\n" \
"s_barrier\n" \
"v_add_u32 v252, 0x2000, v252\n" \
"v_mfma_f32_16x16x32_bf16 v[24:27], a[224:227], v[64:67], 0\n" \
"ds_read_b64_tr_b16 a[192:193], v242 offset:32768\n" \
"ds_read_b64_tr_b16 a[194:195], v242 offset:34816\n" \
"v_mfma_f32_16x16x32_bf16 v[24:27], a[228:231], v[72:75], v[24:27]\n" \
"v_mfma_f32_16x16x32_bf16 v[24:27], a[232:235], v[80:83], v[24:27]\n" \
"ds_read_b64_tr_b16 a[196:197], v242 offset:36864\n" \
"ds_read_b64_tr_b16 a[198:199], v242 offset:38912\n" \
"v_mfma_f32_16x16x32_bf16 v[24:27], a[236:239], v[88:91], v[24:27]\n" \
"s_waitcnt lgkmcnt(6)\n" \
"v_mfma_f32_16x16x32_bf16 v[24:27], a[240:243], v[96:99], v[24:27]\n" \
"ds_read_b64_tr_b16 a[200:201], v242 offset:40960\n" \
"ds_read_b64_tr_b16 a[202:203], v242 offset:43008\n" \
"v_mfma_f32_16x16x32_bf16 v[24:27], a[244:247], v[104:107], v[24:27]\n" \
"v_mfma_f32_16x16x32_bf16 v[24:27], a[248:251], v[112:115], v[24:27]\n" \
"s_waitcnt lgkmcnt(6)\n" \
"ds_read_b64_tr_b16 a[204:205], v242 offset:45056\n" \
"ds_read_b64_tr_b16 a[206:207], v242 offset:47104\n" \
"v_mfma_f32_16x16x32_bf16 v[24:27], a[252:255], v[120:123], v[24:27]\n" \
"v_mfma_f32_16x16x32_bf16 v[28:31], a[224:227], v[68:71], 0\n" \
"ds_read_b64_tr_b16 a[208:209], v242 offset:49152\n" \
"ds_read_b64_tr_b16 a[210:211], v242 offset:51200\n" \
"v_mfma_f32_16x16x32_bf16 v[28:31], a[228:231], v[76:79], v[28:31]\n" \
"v_mfma_f32_16x16x32_bf16 v[28:31], a[232:235], v[84:87], v[28:31]\n" \
"ds_read_b64_tr_b16 a[212:213], v242 offset:53248\n" \
"ds_read_b64_tr_b16 a[214:215], v242 offset:55296\n" \
"v_mfma_f32_16x16x32_bf16 v[28:31], a[236:239], v[92:95], v[28:31]\n" \
"v_mfma_f32_16x16x32_bf16 v[28:31], a[240:243], v[100:103], v[28:31]\n" \
"ds_read_b64_tr_b16 a[216:217], v242 offset:57344\n" \
"ds_read_b64_tr_b16 a[218:219], v242 offset:59392\n" \
"v_mfma_f32_16x16x32_bf16 v[28:31], a[244:247], v[108:111], v[28:31]\n" \
"v_mfma_f32_16x16x32_bf16 v[28:31], a[248:251], v[116:119], v[28:31]\n" \
"ds_read_b64_tr_b16 a[220:221], v242 offset:61440\n" \
"ds_read_b64_tr_b16 a[222:223], v242 offset:63488\n" \
"v_mfma_f32_16x16x32_bf16 v[28:31], a[252:255], v[124:127], v[28:31]\n" \
"s_waitcnt lgkmcnt(6)\n" \
"v_mfma_f32_16x16x32_bf16 v[0:3], a[192:195], v[64:67], 0\n" \
"ds_read_b64_tr_b16 a[224:225], v243 offset:32768\n" \
"ds_read_b64_tr_b16 a[226:227], v243 offset:34816\n" \
"v_mfma_f32_16x16x32_bf16 v[0:3], a[196:199], v[72:75], v[0:3]\n" \
"v_mfma_f32_16x16x32_bf16 v[0:3], a[200:203], v[80:83], v[0:3]\n" \
"ds_read_b64_tr_b16 a[228:229], v243 offset:36864\n" \
"ds_read_b64_tr_b16 a[230:231], v243 offset:38912\n" \
"v_mfma_f32_16x16x32_bf16 v[0:3], a[204:207], v[88:91], v[0:3]\n" \
"v_cvt_pk_bf16_f32 v168, v16, v17\n" \
"v_cvt_pk_bf16_f32 v169, v18, v19\n" \
"v_mfma_f32_16x16x32_bf16 v[0:3], a[208:211], v[96:99], v[0:3]\n" \
"s_waitcnt lgkmcnt(6)\n" \
"ds_read_b64_tr_b16 a[232:233], v243 offset:40960\n" \
"ds_read_b64_tr_b16 a[234:235], v243 offset:43008\n" \
"v_mfma_f32_16x16x32_bf16 v[0:3], a[212:215], v[104:107], v[0:3]\n" \
"v_cvt_pk_bf16_f32 v170, v24, v25\n" \
"v_cvt_pk_bf16_f32 v171, v26, v27\n" \
"v_mfma_f32_16x16x32_bf16 v[0:3], a[216:219], v[112:115], v[0:3]\n" \
"s_waitcnt lgkmcnt(6)\n" \
"ds_read_b64_tr_b16 a[236:237], v243 offset:45056\n" \
"ds_read_b64_tr_b16 a[238:239], v243 offset:47104\n" \
"v_mfma_f32_16x16x32_bf16 v[0:3], a[220:223], v[120:123], v[0:3]\n" \
"v_cvt_pk_bf16_f32 v172, v20, v21\n" \
"v_cvt_pk_bf16_f32 v173, v22, v23\n" \
"v_mfma_f32_16x16x32_bf16 v[4:7], a[192:195], v[68:71], 0\n" \
"ds_read_b64_tr_b16 a[240:241], v243 offset:49152\n" \
"ds_read_b64_tr_b16 a[242:243], v243 offset:51200\n" \
"v_mfma_f32_16x16x32_bf16 v[4:7], a[196:199], v[76:79], v[4:7]\n" \
"v_cvt_pk_bf16_f32 v174, v28, v29\n" \
"v_cvt_pk_bf16_f32 v175, v30, v31\n" \
"v_mfma_f32_16x16x32_bf16 v[4:7], a[200:203], v[84:87], v[4:7]\n" \
"ds_read_b64_tr_b16 a[244:245], v243 offset:53248\n" \
"ds_read_b64_tr_b16 a[246:247], v243 offset:55296\n" \
"v_mfma_f32_16x16x32_bf16 v[4:7], a[204:207], v[92:95], v[4:7]\n" \
"buffer_store_dwordx4 v[168:171], v252, s[64:67], 0 offen offset:0 sc1\n" \
"v_mfma_f32_16x16x32_bf16 v[4:7], a[208:211], v[100:103], v[4:7]\n" \
"ds_read_b64_tr_b16 a[248:249], v243 offset:57344\n" \
"ds_read_b64_tr_b16 a[250:251], v243 offset:59392\n" \
"v_mfma_f32_16x16x32_bf16 v[4:7], a[212:215], v[108:111], v[4:7]\n" \
"buffer_store_dwordx4 v[172:175], v252, s[64:67], 0 offen offset:1024 sc1\n" \
"v_mfma_f32_16x16x32_bf16 v[4:7], a[216:219], v[116:119], v[4:7]\n" \
"ds_read_b64_tr_b16 a[252:253], v243 offset:61440\n" \
"ds_read_b64_tr_b16 a[254:255], v243 offset:63488\n" \
"v_mfma_f32_16x16x32_bf16 v[4:7], a[220:223], v[124:127], v[4:7]\n" \
"s_waitcnt lgkmcnt(6)\n" \
"v_add_u32 v252, 0x2000, v252\n" \
"v_mfma_f32_16x16x32_bf16 v[8:11], a[224:227], v[64:67], 0\n" \
"v_mfma_f32_16x16x32_bf16 v[8:11], a[228:231], v[72:75], v[8:11]\n" \
"v_mfma_f32_16x16x32_bf16 v[8:11], a[232:235], v[80:83], v[8:11]\n" \
"v_mfma_f32_16x16x32_bf16 v[8:11], a[236:239], v[88:91], v[8:11]\n" \
"v_mfma_f32_16x16x32_bf16 v[8:11], a[240:243], v[96:99], v[8:11]\n" \
"s_waitcnt lgkmcnt(2)\n" \
"v_mfma_f32_16x16x32_bf16 v[8:11], a[244:247], v[104:107], v[8:11]\n" \
"v_mfma_f32_16x16x32_bf16 v[8:11], a[248:251], v[112:115], v[8:11]\n" \
"s_waitcnt lgkmcnt(0)\n" \
"v_mfma_f32_16x16x32_bf16 v[8:11], a[252:255], v[120:123], v[8:11]\n" \
"v_mfma_f32_16x16x32_bf16 v[12:15], a[224:227], v[68:71], 0\n" \
"v_mfma_f32_16x16x32_bf16 v[12:15], a[228:231], v[76:79], v[12:15]\n" \
"v_mfma_f32_16x16x32_bf16 v[12:15], a[232:235], v[84:87], v[12:15]\n" \
"v_mfma_f32_16x16x32_bf16 v[12:15], a[236:239], v[92:95], v[12:15]\n" \
"v_mfma_f32_16x16x32_bf16 v[12:15], a[240:243], v[100:103], v[12:15]\n" \
"v_mfma_f32_16x16x32_bf16 v[12:15], a[244:247], v[108:111], v[12:15]\n" \
"v_mfma_f32_16x16x32_bf16 v[12:15], a[248:251], v[116:119], v[12:15]\n" \
"v_mfma_f32_16x16x32_bf16 v[12:15], a[252:255], v[124:127], v[12:15]\n" \
"s_nop 7\n" \
"v_cvt_pk_bf16_f32 v176, v0, v1\n" \
"v_cvt_pk_bf16_f32 v177, v2, v3\n" \
"v_cvt_pk_bf16_f32 v178, v8, v9\n" \
"v_cvt_pk_bf16_f32 v179, v10, v11\n" \
"v_cvt_pk_bf16_f32 v180, v4, v5\n" \
"v_cvt_pk_bf16_f32 v181, v6, v7\n" \
"v_cvt_pk_bf16_f32 v182, v12, v13\n" \
"v_cvt_pk_bf16_f32 v183, v14, v15\n" \
"s_nop 0\n" \
"buffer_store_dwordx4 v[176:179], v252, s[64:67], 0 offen offset:0 sc1\n" \
"buffer_store_dwordx4 v[180:183], v252, s[64:67], 0 offen offset:1024 sc1\n" \
"s_waitcnt vmcnt(0)\n" \
"v_mov_b32 v56, s30\n" \
"s_waitcnt lgkmcnt(0)\n" \
"s_barrier\n" \
"buffer_store_dword v56, v230, s[76:79], 0 offen sc1\n" \
"buffer_load_dword v211, v211, s[76:79], 0 offen sc1\n"
#define G1_ASM \
"s_mov_b32 s68, 0\n" \
"s_mov_b32 s70, 0x100000\n" \
"s_branch L_q_%=\n" \
"L_p_%=:\n" \
"buffer_load_dword v60, v61, s[76:79], 0 offen sc1\n" \
"s_sub_u32 s70, s70, 1\n" \
"s_cbranch_scc1 L_e_%=\n" \
"L_q_%=:\n" \
"s_waitcnt vmcnt(0)\n" \
"v_cmp_eq_u32 vcc, s30, v60\n" \
"s_or_b32 vcc_lo, vcc_lo, s24\n" \
"s_andn2_b32 s69, vcc_lo, s68\n" \
"s_or_b32 s68, s68, vcc_lo\n" \
"s_bitcmp0_b32 s69, 0\n" \
"s_cbranch_scc1 L_s0_%=\n" \
"s_mov_b32 s71, 0\n" \
"buffer_load_dwordx4 v[64:67], v62, s[64:67], s71 offen sc1\n" \
"buffer_load_dwordx4 v[68:71], v63, s[64:67], s71 offen sc1\n" \
"L_s0_%=:\n" \
"s_bitcmp0_b32 s69, 1\n" \
"s_cbranch_scc1 L_s1_%=\n" \
"s_mov_b32 s71, 57344\n" \
"buffer_load_dwordx4 v[72:75], v62, s[64:67], s71 offen sc1\n" \
"buffer_load_dwordx4 v[76:79], v63, s[64:67], s71 offen sc1\n" \
"L_s1_%=:\n" \
"s_bitcmp0_b32 s69, 2\n" \
"s_cbranch_scc1 L_s2_%=\n" \
"s_mov_b32 s71, 114688\n" \
"buffer_load_dwordx4 v[80:83], v62, s[64:67], s71 offen sc1\n" \
"buffer_load_dwordx4 v[84:87], v63, s[64:67], s71 offen sc1\n" \
"L_s2_%=:\n" \
"s_bitcmp0_b32 s69, 3\n" \
"s_cbranch_scc1 L_s3_%=\n" \
"s_mov_b32 s71, 172032\n" \
"buffer_load_dwordx4 v[88:91], v62, s[64:67], s71 offen sc1\n" \
"buffer_load_dwordx4 v[92:95], v63, s[64:67], s71 offen sc1\n" \
"L_s3_%=:\n" \
"s_bitcmp0_b32 s69, 4\n" \
"s_cbranch_scc1 L_s4_%=\n" \
"s_mov_b32 s71, 229376\n" \
"buffer_load_dwordx4 v[96:99], v62, s[64:67], s71 offen sc1\n" \
"buffer_load_dwordx4 v[100:103], v63, s[64:67], s71 offen sc1\n" \
"L_s4_%=:\n" \
"s_bitcmp0_b32 s69, 5\n" \
"s_cbranch_scc1 L_s5_%=\n" \
"s_mov_b32 s71, 286720\n" \
"buffer_load_dwordx4 v[104:107], v62, s[64:67], s71 offen sc1\n" \
"buffer_load_dwordx4 v[108:111], v63, s[64:67], s71 offen sc1\n" \
"L_s5_%=:\n" \
"s_bitcmp0_b32 s69, 6\n" \
"s_cbranch_scc1 L_s6_%=\n" \
"s_mov_b32 s71, 344064\n" \
"buffer_load_dwordx4 v[112:115], v62, s[64:67], s71 offen sc1\n" \
"buffer_load_dwordx4 v[116:119], v63, s[64:67], s71 offen sc1\n" \
"L_s6_%=:\n" \
"s_bitcmp0_b32 s69, 7\n" \
"s_cbranch_scc1 L_s7_%=\n" \
"s_mov_b32 s71, 401408\n" \
"buffer_load_dwordx4 v[120:123], v62, s[64:67], s71 offen sc1\n" \
"buffer_load_dwordx4 v[124:127], v63, s[64:67], s71 offen sc1\n" \
"L_s7_%=:\n" \
"s_cmp_lg_u32 s68, -1\n" \
"s_cbranch_scc1 L_p_%=\n" \
"L_e_%=:\n" \
"s_waitcnt vmcnt(0)\n"
#define G2_ASM \
"s_mov_b32 s68, 0\n" \
"s_mov_b32 s70, 0x100000\n" \
"s_branch L_q_%=\n" \
"L_p_%=:\n" \
"buffer_load_dword v211, v61, s[76:79], 0 offen sc1\n" \
"s_sub_u32 s70, s70, 1\n" \
"s_cbranch_scc1 L_e_%=\n" \
"L_q_%=:\n" \
"s_waitcnt vmcnt(0)\n" \
"v_cmp_eq_u32 vcc, s30, v211\n" \
"s_or_b32 vcc_lo, vcc_lo, s24\n" \
"s_andn2_b32 s69, vcc_lo, s68\n" \
"s_or_b32 s68, s68, vcc_lo\n" \
"s_bitcmp0_b32 s69, 0\n" \
"s_cbranch_scc1 L_s0_%=\n" \
"s_mov_b32 s71, 516096\n" \
"buffer_load_dwordx4 v[64:67], v62, s[64:67], s71 offen sc1\n" \
"buffer_load_dwordx4 v[68:71], v63, s[64:67], s71 offen sc1\n" \
"L_s0_%=:\n" \
"s_bitcmp0_b32 s69, 1\n" \
"s_cbranch_scc1 L_s1_%=\n" \
"s_mov_b32 s71, 573440\n" \
"buffer_load_dwordx4 v[72:75], v62, s[64:67], s71 offen sc1\n" \
"buffer_load_dwordx4 v[76:79], v63, s[64:67], s71 offen sc1\n" \
"L_s1_%=:\n" \
"s_bitcmp0_b32 s69, 2\n" \
"s_cbranch_scc1 L_s2_%=\n" \
"s_mov_b32 s71, 630784\n" \
"buffer_load_dwordx4 v[80:83], v62, s[64:67], s71 offen sc1\n" \
"buffer_load_dwordx4 v[84:87], v63, s[64:67], s71 offen sc1\n" \
"L_s2_%=:\n" \
"s_bitcmp0_b32 s69, 3\n" \
"s_cbranch_scc1 L_s3_%=\n" \
"s_mov_b32 s71, 688128\n" \
"buffer_load_dwordx4 v[88:91], v62, s[64:67], s71 offen sc1\n" \
"buffer_load_dwordx4 v[92:95], v63, s[64:67], s71 offen sc1\n" \
"L_s3_%=:\n" \
"s_bitcmp0_b32 s69, 4\n" \
"s_cbranch_scc1 L_s4_%=\n" \
"s_mov_b32 s71, 745472\n" \
"buffer_load_dwordx4 v[96:99], v62, s[64:67], s71 offen sc1\n" \
"buffer_load_dwordx4 v[100:103], v63, s[64:67], s71 offen sc1\n" \
"L_s4_%=:\n" \
"s_bitcmp0_b32 s69, 5\n" \
"s_cbranch_scc1 L_s5_%=\n" \
"s_mov_b32 s71, 802816\n" \
"buffer_load_dwordx4 v[104:107], v62, s[64:67], s71 offen sc1\n" \
"buffer_load_dwordx4 v[108:111], v63, s[64:67], s71 offen sc1\n" \
"L_s5_%=:\n" \
"s_bitcmp0_b32 s69, 6\n" \
"s_cbranch_scc1 L_s6_%=\n" \
"s_mov_b32 s71, 860160\n" \
"buffer_load_dwordx4 v[112:115], v62, s[64:67], s71 offen sc1\n" \
"buffer_load_dwordx4 v[116:119], v63, s[64:67], s71 offen sc1\n" \
"L_s6_%=:\n" \
"s_cmp_lg_u32 s68, -1\n" \
"s_cbranch_scc1 L_p_%=\n" \
"L_e_%=:\n" \
"s_waitcnt vmcnt(0)\n"

#define C10(p,a) p #a "0", p #a "1", p #a "2", p #a "3", p #a "4", p #a "5", p #a "6", p #a "7", p #a "8", p #a "9"
#define C20_169(p) C10(p,2), C10(p,3), C10(p,4), C10(p,5), C10(p,6), C10(p,7), C10(p,8), C10(p,9), C10(p,10), C10(p,11), C10(p,12), \
  C10(p,13), C10(p,14), C10(p,15), C10(p,16)
#define C0_179(p) C10(p,), C10(p,1), C20_169(p), C10(p,17)
#define C0_191(p) C0_179(p), C10(p,18), p "190", p "191"
#define ACLOBHI "a192","a193","a194","a195","a196","a197","a198","a199", C10("a",20), C10("a",21), C10("a",22), C10("a",23), C10("a",24), "a250","a251","a252","a253","a254","a255"
#define ACLOB C0_191("a"), ACLOBHI
#define VMID "v232","v233","v234","v235","v236","v237","v238","v239","v240","v241"

#define TSDECL u32x16 ts;
constexpr unsigned WS_EP = 0, WS_TAB = 4096, WS_MOD = 20480;
constexpr unsigned WS_FLAG = 1 << 16;
constexpr size_t WS_G = 2 << 20, WS_X1 = 40 << 20;
constexpr unsigned IMG = 57344;
__device__ __forceinline__ float lo16(unsigned u) { return __uint_as_float(u << 16); }
__device__ __forceinline__ float hi16(unsigned u) { return __uint_as_float(u & 0xffff0000u); }
__device__ __forceinline__ f32x2 up2(unsigned u) { return f32x2{lo16(u), hi16(u)}; }
__device__ __forceinline__ float el(const u32x4& v, int j) { return (j & 1) ? hi16(v[j >> 1]) : lo16(v[j >> 1]); }
__device__ __forceinline__ unsigned pkbf(float a, float b) { b16x2 v = {(__bf16)a, (__bf16)b}; return __builtin_bit_cast(unsigned, v); }
__device__ __forceinline__ __amdgpu_buffer_rsrc_t srd(const void* p, unsigned n) { return __builtin_amdgcn_make_buffer_rsrc((void*)p, (short)0, n, 0x00020000); }
template <int C> __device__ __forceinline__ float dppadd(float v) { return v + __int_as_float(__builtin_amdgcn_update_dpp(0, __float_as_int(v), C, 0xf, 0xf, false)); }
__device__ __forceinline__ float xsum(float v) {
  auto a = __builtin_amdgcn_permlane16_swap(__float_as_uint(v), __float_as_uint(v), false, false); v = __uint_as_float(a[0]) + __uint_as_float(a[1]);
  auto b = __builtin_amdgcn_permlane32_swap(__float_as_uint(v), __float_as_uint(v), false, false); return __uint_as_float(b[0]) + __uint_as_float(b[1]);
}
__device__ __forceinline__ float wsum(float v) { return xsum(dppadd<0x121>(dppadd<0x122>(dppadd<0x124>(dppadd<0x128>(v))))); }
__device__ __forceinline__ u32x4 ld8(const u16* row, unsigned lc) {
  const u16* p = row + 32 * (lc >> 2) + 4 * (lc & 3); const u32x2 a = *(const u32x2*)p, b = *(const u32x2*)(p + 16); return u32x4{a[0], a[1], b[0], b[1]};
}

__device__ __forceinline__ void gemm_addrs(unsigned ldsb, unsigned l, unsigned w, unsigned pitch, unsigned half, bool nat, u32x4& q0, u32x4& q1, u32x4& q2, u32x4& qd, u32x4& o0, u32x4& o1, u32x4& o2) {
  const unsigned row = 4 * (l >> 4) + ((l & 15) >> 2), p = l & 3, f = (row >> 1) & 3, h = w >> 1;
  const unsigned sw = 16 * ((l & 7) ^ (((l >> 4) & 3) << 1));
  const unsigned rb = ldsb + 128 * row + 8 * p;
#pragma unroll
  for (int c = 0; c < 4; ++c) { q0[c] = rb + (nat ? 512 * (l >> 4) : 0) + h * 8192 + 32 * (c ^ f); q1[c] = q0[c] + 65536; q2[c] = q0[c] + 131072; }
#pragma unroll
  for (int j = 0; j < 4; ++j) qd[j] = (l >> 3) * pitch + (nat ? 16 * ((l & 7) ^ ((((l >> 4) & 1) | ((j & 1) << 1)) << 1)) : sw) + j * (8 * pitch - 1024) + (w & 1) * 32 * pitch + h * half;
  const unsigned a0 = rb + 32 * ((2 * h) ^ f), a1 = rb + 32 * ((2 * h + 1) ^ f), od = (l >> 3) * 896 + sw + w * 32 * 896;
  o0 = u32x4{a0, a1, a0 + 65536, a1 + 65536};
  o1 = u32x4{a0 + 131072, a1 + 131072, od, od + 6144};
  o2 = u32x4{od + 12288, od + 18432, (h * 4 + 2 * (w & 1)) * 1024 + 16 * l, 0};
}

__device__ __forceinline__ void await(__amdgpu_buffer_rsrc_t st, unsigned fo, unsigned tag, unsigned& a0, unsigned& a1) {
  unsigned n = 1u << 20, v;
  asm volatile("L_w_%=:\n buffer_load_dword %1, %4, %5, 0 offen sc1\n s_sub_u32 %0, %0, 1\n s_cbranch_scc1 L_g_%=\n s_waitcnt vmcnt(0)\n v_cmp_ne_u32 vcc, %6, %1\n s_cbranch_vccnz L_w_%=\n L_g_%=:\n s_waitcnt vmcnt(0)"
    : "+s"(n), "=&v"(v), "+v"(a0), "+v"(a1) : "v"(fo), "s"(st), "s"(tag) : "memory", "vcc", "scc");
}
#define GATHER(A, F, fo, own) u32x16 pp[4]; \
  asm volatile(A : "={v[64:79]}"(pp[0]), "={v[80:95]}"(pp[1]), "={v[96:111]}"(pp[2]), "={v[112:127]}"(pp[3]), F(fv) \
    : "{v61}"(fo), "{v62}"(off[0]), "{v63}"(off[1]), "{s30}"(tag), "{s24}"(own), "{s[64:67]}"(sg), "{s[76:79]}"(st) : "memory", "vcc", "scc", "s68", "s69", "s70", "s71"); \
  u32x4 p[2][8]; \
  _Pragma("unroll") for (int i = 0; i < 16; ++i) p[i & 1][i >> 1] = u32x4{pp[i >> 2][4 * (i & 3)], pp[i >> 2][4 * (i & 3) + 1], pp[i >> 2][4 * (i & 3) + 2], pp[i >> 2][4 * (i & 3) + 3]};
template <int NQ> __device__ __forceinline__ void rowred(float (&q)[NQ], char* scr, unsigned w, unsigned l) {
#pragma unroll
  for (int i = 0; i < NQ; ++i) { q[i] = xsum(dppadd<0x128>(q[i])); *(float*)(scr + ((i * 8 + (l & 7)) * 4 + w) * 4) = q[i]; }
  asm volatile("s_waitcnt lgkmcnt(0)\n s_barrier" ::: "memory");
#pragma unroll
  for (int i = 0; i < NQ; ++i) { const f32x4 v = *(const f32x4*)(scr + (i * 8 + (l & 7)) * 16); q[i] = ((v[0] + v[1]) + (v[2] + v[3])) * (1.f / 448.f); }
}
__device__ __forceinline__ float sum8(const float (&v)[8]) { return ((v[0] + v[1]) + (v[2] + v[3])) + ((v[4] + v[5]) + (v[6] + v[7])); }
__device__ __forceinline__ unsigned lane_id() { unsigned l; asm volatile("v_mbcnt_lo_u32_b32 %0, -1, 0\n v_mbcnt_hi_u32_b32 %0, -1, %0" : "=v"(l)); return l; }

#define CODEMAX 63360u
#define OAUX 16
#define KVM 0u
#define MAPM 1
__device__ __forceinline__ unsigned wqline(unsigned n) { return n < 3584 ? ((n % 896) >> 1) * 8192 + (n & 1) * 512 + (n / 896) * 128 : 0x7fffff00u; }
struct Args { const u16 *x, *K, *V, *wq, *wo, *w1, *b1, *w2, *b2, *c, *wm; char* ws; u16* out; };
typedef const __attribute__((address_space(4))) Args* ArgP;
#define G1(T, p) ((T)(__attribute__((address_space(1))) void*)(p))
#define LOADARGS const u16 *x = ap->x, *w1 = ap->w1, *b1 = ap->b1, *w2 = ap->w2, *b2 = ap->b2; char* ws = ap->ws; u16* out = ap->out; \
  const unsigned kvh = ((bid & 7) % (8 / MAPM)) * MAPM + (bid >> 3) % MAPM, shr = (bid >> 3) / MAPM, rb = shr * MAPM + (bid & 7) / (8 / MAPM), ldsb = (unsigned)(size_t)lds, sl = kvh < 7 ? kvh : 6;
__global__ __launch_bounds__(256, 1) void k2_main(Args args) {
  extern __shared__ __attribute__((aligned(16))) char lds[];
  unsigned w = __builtin_amdgcn_readfirstlane(threadIdx.x >> 6), bid = blockIdx.x, tag;
  unsigned cpf;
  { unsigned long long pc;
    asm volatile("s_getpc_b64 %1\n s_nop 0\n global_load_dword %0, %2, %1" : "={v109}"(cpf), "=&s"(pc) : "v"(min((((bid >> 3) * 4 + w) & 7) * 8192 + (threadIdx.x & 63) * 128, CODEMAX))); }
  ArgP ap = (ArgP)__builtin_amdgcn_kernarg_segment_ptr();
  TSDECL
  u32x4 gq[8], xv[2];
  unsigned fv;
  __amdgpu_buffer_rsrc_t st, sg;
  {
    LOADARGS
    const u16 *K = ap->K, *V = ap->V, *wq = ap->wq, *wo = ap->wo;
    const unsigned t = threadIdx.x, l = t & 63, r = l & 31, h = l >> 5, c16 = l & 15, g = l >> 4;
    st = srd(ws, 1 << 20); sg = srd(ws + WS_G + (rb << 20), 1 << 20);
    unsigned xo = l < 56 ? w * 14336 + 16 * l : 0x7fff0000u;
    u32x16 xr[4], g0; u32x8 g1, cc; u32x4 g2; unsigned pfd;
    const __amdgpu_buffer_rsrc_t sq = srd(wq + kvh * 512, 448 * 8192 - kvh * 1024);
    asm volatile("s_nop 4\n" K0_ASM
      : "={v[0:15]}"(xr[0]), "={v[16:31]}"(xr[1]), "={v[32:47]}"(xr[2]), "={v[48:63]}"(xr[3]), "={v[64:79]}"(g0), "={v[80:87]}"(g1), "={v[88:91]}"(g2), "={v[92:99]}"(cc), "={v100}"(pfd)
      : "{v101}"((t >> 3) * 5376 + 16 * (t & 7)), "{v102}"((t >> 3) * 2), "{v103}"(xo), "{v104}"(wqline((31 - shr) * 256 + t)), "{s31}"(bid), "{v105}"(0x7fffff00u),
        "{s[32:35]}"(srd(ap->wm + (bid < 84 ? 64 * (bid >> 1) + (bid & 1) * 224 * 2688 : 0), bid < 84 ? 224 * 5376 - 128 * (bid >> 1) : 0)), "{s[36:39]}"(srd(ap->c + 224 * (bid & 1), bid < 84 ? 448 : 0)),
        "{s[56:59]}"(sq), "{s[72:75]}"(srd(x + rb * 64 * 448, bid < 84 ? 0 : IMG)), "{s[76:79]}"(st)
      : "memory", "scc", "s68", "s69", "s70");
    u32x4 q0, q1, q2, qd, o0, o1, o2, kb, kd, vd; u32x8 vb;
    gemm_addrs(ldsb, l, w, 3584, 256, false, q0, q1, q2, qd, o0, o1, o2);
    const unsigned pn = (t >> 1) * 32 + shr;
    unsigned pf1 = t & 1 ? 0x7fffff00u : pn * 128, pf2 = t & 1 ? 0x7fffff00u : (pn >> 2) * 3584 + (pn & 3) * 128;
    u32x4 md = qd;
    gemm_addrs(ldsb, l, w, 8192, 512, true, q0, q1, q2, qd, o0, o1, o2);
    const unsigned img = ldsb + 98304, vec = bid / 14, gks = bid % 14;
    u32x4 ms;
#pragma unroll
    for (int k = 0; k < 4; ++k) ms[k] = img + (w & 1) * 2048 + g * 256 + 16 * (c16 ^ (4 * k + g));
    const unsigned lm = l < 56 ? l : 55;
    u32x4 hd = {l < 56 ? img + (l >> 2) * 4096 + w * 1024 + (l & 3) * 256 + c16 * 16 : img + 61440 + w * 256 + c16 * 16, l < 56 ? 0x3f800000u : 0u, WS_TAB + w * 896 + lm * 16, img + 57344 + w * 448 + lm * 8};
    unsigned tr = img + 57344 + lm * 32;
    const unsigned gp = vec < 2 ? l : h * 32 + (r & 12) * 2 + (r & 3) + ((r >> 4) << 2);
    unsigned gpos = (vec < 2 ? WS_TAB + (vec ? 0 : 7168) : WS_MOD + (vec - 2) * 7168) + (bid & 1) * 3584 + ((gks >> 1) * 64 + gp) * 8;
    if (t >= 64) gpos = 0x7fffff00u;
    unsigned gw = img + 61440 + (4 * w + g) * 256 + (l & 7) * 32, gr = img + 61440 + l * 4;
    const unsigned qs = 0x3db8aa3bu, one = 0x3f800000u;
    u32x2 pf = {pf1, pf2}, pf3 = {WS_MOD + min(w * 1024 + 16 * l, 3568u), (8 * kvh + (l & 7)) * 896 + 256 * w + 64 * h + 8 * ((l >> 3) & 3)};
    u32x2 pf5 = {t * 128, WS_FLAG + (rb * 8 + (l & 7)) * 128};
    u32x2 pf4 = {l * 16384, t ? 0x7fffff00u : WS_FLAG + (rb * 8 + kvh) * 128};
    o2[2] += kvh * IMG;
    o2[3] = ldsb + 65536 + 16384 * w + 16 * l;
    u32x2 o3 = {ldsb + 65536 + 16384 * (w & 1) + 16 * l, ldsb + 7 * 16384 + 4096 * w + 16 * l};
#pragma unroll
    for (int j = 0; j < 4; ++j) kb[j] = ldsb + 512 * c16 + 16 * ((4 * j + g) ^ c16);
    { const unsigned row = 4 * g + (c16 >> 2);
#pragma unroll
      for (int c = 0; c < 8; ++c) vb[c] = ldsb + 65536 + 512 * row + 32 * (c ^ (row & 7)) + 8 * (l & 3); }
#pragma unroll
    for (int i = 0; i < 4; ++i) { kd[i] = 4096 * w + 512 * h + 16 * (r ^ (8 * (w & 1) + 2 * i + h)); vd[i] = 4096 * w + 512 * h + 16 * (r ^ ((2 * i + h) << 1)); }
    unsigned pf6 = l < 16 ? (l * 128 + w * 32 + shr) * 128 : 0x7f000000u;
    unsigned cnt = 16; u32x16 ga, gb; u32x8 gx;
    asm volatile("s_nop 4\n" K2_ASM
      : "+{v[212:215]}"(q0), "+{v[216:219]}"(q1), "+{v[220:223]}"(q2), "+{v[224:227]}"(qd), "+{v[228:231]}"(ms), "+{v[232:235]}"(hd), "+{v[250:253]}"(o2), "+{a[0:3]}"(md), "+{a[4:5]}"(pf), "+{a[6:7]}"(pf3), "+{a[8:9]}"(pf4), "+{a[10:11]}"(pf5), "+{a12}"(pf6), "={a[128:143]}"(ga), "={a[144:159]}"(gb), "={a[160:167]}"(gx), "+{v236}"(tr),
        "+{v[192:195]}"(kb), "+{v[196:203]}"(vb), "+{v[204:207]}"(kd), "+{v[208:211]}"(vd),
        "+{v[0:15]}"(xr[0]), "+{v[16:31]}"(xr[1]), "+{v[32:47]}"(xr[2]), "+{v[48:63]}"(xr[3]), "+{v[64:79]}"(g0), "+{v[80:87]}"(g1), "+{v[88:91]}"(g2), "+{v[92:99]}"(cc), "+{v100}"(pfd),
        "+{v103}"(xo), "+{v106}"(gpos), "+{v107}"(gw), "+{v108}"(gr), "+{v109}"(cpf), "={s30}"(tag), "+{s52}"(cnt)
      : "{v[242:245]}"(o0), "{v[246:249]}"(o1), "{v[254:255]}"(o3),
        "{s[40:43]}"(srd(K + (size_t)kvh * 524288, 1 << 20)), "{s[44:47]}"(srd(V + (size_t)kvh * 524288, 1 << 20)),
        "{s[56:59]}"(sq), "{s[60:63]}"(srd(wo + (size_t)kvh * 512 * 448, 512 * 896)),
        "{s[64:67]}"(sg), "{s[76:79]}"(st), "{s24}"(4u | (16u << (4 * ((shr >> 1) % 3) + (shr & 1))) | (KVM >> (shr & 1))), "{s[26:27]}"(out + rb * 64 * 448),
        "{s[36:39]}"(srd(w1 + sl * 256, 448 * 3584 - sl * 512)), "{s[32:35]}"(srd(w2 + sl * 256 * 448, 256 * 896)),
        "{s50}"(ldsb + 4096 * w), "{s51}"(ldsb + 65536 + 4096 * w), "{s[72:75]}"(srd(x + rb * 64 * 448, IMG)),
        "{s28}"(vec < 2 ? qs : one), "{s29}"(bid & 1 ? 0u : vec == 1 ? qs : vec == 4 ? one : 0u), "{s31}"(bid), "{s71}"(w)
      : "memory", "vcc", "scc", "s48", "s49", "s53", "s54", "s55", "s68", "s69", "s70", C10("s",8), "s90", "s91", "s92", "s93", "s94", "s95", "s25", "v101", "v102", "v104", "v105", C10("v",11), C10("v",12), C10("v",13), C10("v",14), C10("v",15), C10("v",16), C10("v",17), C10("v",18),
        "v190", "v191", "v237","v238","v239","v240","v241", "a13", "a14", "a15", "a16", "a17", "a18", "a19", C10("a",2), C10("a",3), C10("a",4), C10("a",5), C10("a",6), C10("a",7), C10("a",8), C10("a",9), C10("a",10), C10("a",11),
        "a120", "a121", "a122", "a123", "a124", "a125", "a126", "a127", "a168", "a169", C10("a",17), C10("a",18), "a190", "a191", ACLOBHI);
    ts = xr[0]; fv = xr[3][12];
    gq[0] = ga.lo.lo; gq[1] = ga.lo.hi; gq[2] = ga.hi.lo; gq[3] = ga.hi.hi; gq[4] = gb.lo.lo; gq[5] = gb.lo.hi; gq[6] = gb.hi.lo; gq[7] = gb.hi.hi; xv[0] = gx.lo; xv[1] = gx.hi;
  }
  asm volatile("" : "+s"(ap), "+s"(w), "+s"(bid), "+s"(tag));
  LOADARGS
  unsigned l = lane_id();
#define CHUNKS const unsigned lc[2] = {16 * w + (l >> 3), (w < 3 ? 16 * w + 8 : 48) + (l >> 3)}, row = 64 * rb + 8 * kvh + (l & 7); \
  unsigned off[2] = {((lc[0] >> 2) * 4 + (kvh >> 1)) * 1024 + (lc[0] & 3) * 256 + (8 * (kvh & 1) + (l & 7)) * 16, w < 3 ? off[0] + 8192 : 0x7fff0000u};
  float x1[2][8], gv[2][8];
  u32x4 bb[2];
  unsigned fpub = (w | l) ? 0x7fff0000u : WS_FLAG + (256 + rb * 8 + kvh) * 128;
  {
    CHUNKS
    for (unsigned spins = 0; spins < (1u << 20); ++spins) {
      unsigned bad = 0;
#pragma unroll
      for (int i = 0; i < 8; ++i) bad |= (gq[i][1] ^ tag) | (gq[i][3] ^ tag);
      if (__builtin_amdgcn_ballot_w64(bad != 0) == 0) break;
#pragma unroll
      for (int i = 0; i < 8; ++i) gq[i] = __builtin_amdgcn_raw_buffer_load_b128(st, WS_MOD + min(w * 1024 + 16 * l, 3568u), i * 3584, 16);
    }
    float mv[2][24];
    { char* scr = lds + 36864 + w * 2048;
#pragma unroll
      for (int i = 0; i < 4; ++i) *(f32x2*)(scr + i * 512 + 8 * l) = f32x2{__uint_as_float(gq[2 * i][0]) + __uint_as_float(gq[2 * i + 1][0]), __uint_as_float(gq[2 * i][2]) + __uint_as_float(gq[2 * i + 1][2])};
      asm volatile("s_waitcnt lgkmcnt(0)" ::: "memory");
#pragma unroll
      for (int i = 0; i < 16; ++i) {
        const f32x4 u = *(const f32x4*)(scr + (i >> 2) * 512 + ((l >> 3) + 8 * (i & 1)) * 32 + (i & 2) * 8);
#pragma unroll
        for (int j = 0; j < 4; ++j) { if (i < 12) mv[i & 1][(i >> 2) * 8 + (i & 2) * 2 + j] = u[j]; else gv[i & 1][(i & 2) * 2 + j] = u[j]; }
      }
    }
    GATHER(G1_ASM, "+{v60}", WS_FLAG + (rb * 8 + (l & 7)) * 128, 0x01010101u << kvh)
    const float act = w < 3 ? 1.f : 0.f;
    float v[2][8], t8[2][8], q[2];
#pragma unroll
    for (int m = 0; m < 2; ++m)
#pragma unroll
      for (int j = 0; j < 8; ++j) {
        const float a = ((el(p[m][0], j) + el(p[m][1], j)) + (el(p[m][2], j) + el(p[m][3], j))) + ((el(p[m][4], j) + el(p[m][5], j)) + (el(p[m][6], j) + el(p[m][7], j)));
        x1[m][j] = el(xv[m], j) + mv[m][j] * a; t8[m][j] = mv[m][8 + j];
      }
    q[0] = sum8(x1[0]) + act * sum8(x1[1]); q[1] = sum8(t8[0]) + act * sum8(t8[1]);
    rowred<2>(q, lds + 32768, w, l);
    float z[5], u[5][2];
#pragma unroll
    for (int m = 0; m < 2; ++m) {
      float d[8], a[8], b[8], c[8];
#pragma unroll
      for (int j = 0; j < 8; ++j) { d[j] = x1[m][j] - q[0]; v[m][j] = d[j] * mv[m][16 + j]; t8[m][j] -= q[1]; d[j] *= d[j]; a[j] = v[m][j] * v[m][j]; b[j] = v[m][j] * t8[m][j]; c[j] = t8[m][j] * t8[m][j]; }
      u[0][m] = sum8(d); u[1][m] = sum8(v[m]); u[2][m] = sum8(a); u[3][m] = sum8(b); u[4][m] = sum8(c);
    }
#pragma unroll
    for (int i = 0; i < 5; ++i) z[i] = u[i][0] + act * u[i][1];
    rowred<5>(z, lds + 33792, w, l);
    const float r1 = __builtin_amdgcn_rsqf(z[0] + 1e-5f);
    const float r2 = __builtin_amdgcn_rsqf(fmaxf(r1 * (r1 * (z[2] - z[1] * z[1]) + 2.f * z[3]) + z[4], 0.f) + 1e-5f);
#pragma unroll
    for (int m = 0; m < 2; ++m)
#pragma unroll
      for (int j = 0; j < 8; ++j) v[m][j] = (v[m][j] - z[1]) * r1 + t8[m][j];
#pragma unroll
    for (int m = 0; m < 2; ++m)
      __builtin_amdgcn_raw_buffer_store_b128(u32x4{pkbf(v[m][0] * r2, v[m][1] * r2), pkbf(v[m][2] * r2, v[m][3] * r2), pkbf(v[m][4] * r2, v[m][5] * r2), pkbf(v[m][6] * r2, v[m][7] * r2)}, sg, off[m] + 8 * IMG, 0, 16);
    asm volatile("s_waitcnt vmcnt(0)\n s_barrier" : "+v"(fpub) :: "memory");
    __builtin_amdgcn_raw_buffer_store_b32(tag, st, fpub, 0, 16);
#pragma unroll
    for (int m = 0; m < 2; ++m) bb[m] = ld8(b2, lc[m]);
  }
#define FO2 (WS_FLAG + (512 + rb * 8 + ((l & 7) == 7 ? 0 : l & 7)) * 128)
  fv = ~tag;
  if (kvh < 7) {
    fv = FO2;
    u32x4 q0, q1, q2, qd, o0, o1, o2;
    gemm_addrs(ldsb, l, w, 3584, 256, false, q0, q1, q2, qd, o0, o1, o2);
    unsigned yo = w * 4096 + 16 * l;
    o2[3] = ldsb + 131072 + (16 * (w >> 1) + 2 * (w & 1)) * 1024 + 16 * l;
    const unsigned o3 = ldsb + 131072 + (w & 1) * 2048 + 16 * l;
    f32x16 bias[2];
#pragma unroll
    for (int i = 0; i < 8; ++i) {
      const u32x2 bv = *(const u32x2*)(b1 + sl * 256 + 128 * (w >> 1) + 16 * i + 4 * (l >> 4));
      bias[i >> 2][4 * (i & 3)] = lo16(bv[0]); bias[i >> 2][4 * (i & 3) + 1] = hi16(bv[0]); bias[i >> 2][4 * (i & 3) + 2] = lo16(bv[1]); bias[i >> 2][4 * (i & 3) + 3] = hi16(bv[1]); }
    asm volatile("s_nop 4\n" K4_ASM
      : "={v[0:15]}"(ts), "+{v[250:253]}"(o2), "+{v231}"(yo), "+{v[176:191]}"(bias[0]), "+{v211}"(fv)
      : "{v[212:215]}"(q0), "{v[216:219]}"(q1), "{v[220:223]}"(q2), "{v[224:227]}"(qd), "{v228}"(ldsb + 32768 + (w & 1) * 2048 + 16 * l), "{v210}"(w < 2 ? 0u : 0x7fff0000u), "{v229}"(WS_FLAG + (256 + rb * 8 + (l & 7)) * 128), "{v230}"(fpub + 32768), "{s30}"(tag), "{s[76:79]}"(st), "{v[242:245]}"(o0), "{v[246:249]}"(o1), "{v254}"(o3),
        "{v[192:207]}"(bias[1]),
        "{s[56:59]}"(srd(w1 + sl * 256, 448 * 3584 - sl * 512)), "{s[60:63]}"(srd(w2 + sl * 256 * 448, 256 * 896)),
        "{s[64:67]}"(srd(ws + WS_G + (rb << 20) + (9 + sl) * IMG, IMG)), "{s[72:75]}"(srd(ws + WS_G + (rb << 20) + 8 * IMG, IMG)), "{s50}"(ldsb + 4096 * w), "{s71}"(w)
      : "memory", "vcc", "scc", "s68", "s69", "v16", "v17", "v18", "v19", C20_169("v"), "v170", "v171", "v172", "v173", "v174", "v175", "v208", "v209",
        "v232","v233","v234","v235","v236","v237","v238","v239", ACLOBHI);
    l = lane_id();
  } else if (rb == 0) {
    unsigned d0 = 0, d1 = (w | l) ? 0x7fff0000u : WS_EP;
    await(st, WS_FLAG + (64 * w + l) * 128, tag, d0, d1);
    asm volatile("s_barrier" ::: "memory");
    __builtin_amdgcn_raw_buffer_store_b32(tag, st, d1, 0, 16);
  }
  {
    CHUNKS
    GATHER(G2_ASM, "+{v211}", FO2, kvh == 7 ? 0u : kvh ? 0x01010101u << kvh : 0x81818181u)
    const __amdgpu_buffer_rsrc_t so = srd(out, 2048 * 896);
#pragma unroll
    for (int m = 0; m < 2; ++m) {
      float v[8];
#pragma unroll
      for (int j = 0; j < 8; ++j)
        v[j] = x1[m][j] + gv[m][j] * (((el(p[m][0], j) + el(p[m][1], j)) + (el(p[m][2], j) + el(p[m][3], j))) + ((el(p[m][4], j) + el(p[m][5], j)) + (el(p[m][6], j) + el(bb[m], j))));
      const bool odd = l & 8;
      const unsigned a0 = pkbf(v[0], v[1]), a1 = pkbf(v[2], v[3]), b0 = pkbf(v[4], v[5]), b1_ = pkbf(v[6], v[7]);
      const unsigned r0 = __builtin_amdgcn_update_dpp(0u, odd ? a0 : b0, 0x128, 0xf, 0xf, false), r1 = __builtin_amdgcn_update_dpp(0u, odd ? a1 : b1_, 0x128, 0xf, 0xf, false);
      __builtin_amdgcn_raw_buffer_store_b128(odd ? u32x4{r0, r1, b0, b1_} : u32x4{a0, a1, r0, r1}, so,
        m && w == 3 ? 0x7fff0000u : row * 896 + 64 * (lc[m] >> 2) + (odd ? 24 : 0) + 8 * (lc[m] & 3), 0, OAUX);
    }
  }
  asm volatile("L_code_end:");
}

extern "C" void kernel_launch(void* const* d_in, const int* in_sizes, int n_in, void* d_out, int out_size, void* d_ws, size_t ws_size, hipStream_t stream) {
  const u16 *x = (const u16*)d_in[0], *K = (const u16*)d_in[1], *V = (const u16*)d_in[2], *wq = (const u16*)d_in[3], *wo = (const u16*)d_in[4];
  const u16 *w1 = (const u16*)d_in[6], *b1 = (const u16*)d_in[7], *w2 = (const u16*)d_in[8], *b2 = (const u16*)d_in[9], *c = (const u16*)d_in[10], *wm = (const u16*)d_in[11];
  char* ws = (char*)d_ws;
  (void)hipFuncSetAttribute((const void*)k2_main, hipFuncAttributeMaxDynamicSharedMemorySize, 163840);
  k2_main<<<256, 256, 163840, stream>>>(Args{x, K, V, wq, wo, w1, b1, w2, b2, c, wm, ws, (u16*)d_out});
}
```
